```python
import torch

def _softmax(x, axis=-1):
    return torch.softmax(x, dim=axis)
def _gelu(x):
    return torch.nn.functional.gelu(x, approximate="tanh")
def _silu(x):
    return torch.nn.functional.silu(x)
def _sigmoid(x):
    return torch.sigmoid(x)
def _layernorm(x, axis=-1, eps=1e-5):
    m = x.mean(dim=axis, keepdim=True)
    v = x.var(dim=axis, keepdim=True, unbiased=False)
    return (x - m) / torch.sqrt(v + eps)
def _scatter_add(tgt, idx, src, axis=0):
    return tgt.clone().index_add_(axis, idx, src)
def _segment_sum(src, seg, n):
    out = torch.zeros((n,) + src.shape[1:], dtype=src.dtype)
    return out.index_add_(0, seg, src)
def _pad(x, pw):
    flat = tuple(v for pair in reversed(pw) for v in pair)
    return torch.nn.functional.pad(x, flat)

kernel_name = 'sequential_preLN__spectral_fnet__glu_gated_glu__prompt'

def setup_inputs():

    _w = (lambda a: (lambda t: t.to(torch.bfloat16) if t.is_floating_point() else t)(torch.as_tensor(a)))
    def _A_setup():
        B, S, D = 1, 2048, 512
        ns = torch.arange(S)[:, None] * torch.arange(S)[None, :]
        nd = torch.arange(D)[:, None] * torch.arange(D)[None, :]
        Ws_re = torch.cos(-2.0 * torch.pi * ns / S).to(float)
        Ws_im = torch.sin(-2.0 * torch.pi * ns / S).to(float)
        Wd_re = torch.cos(-2.0 * torch.pi * nd / D).to(float)
        Wd_im = torch.sin(-2.0 * torch.pi * nd / D).to(float)
        return {
            "x": _w((lambda *s: torch.randn(*s, dtype=torch.bfloat16) * 0.05)(B, S, D)),
            "dft_seq_re": _w(Ws_re),
            "dft_seq_im": _w(Ws_im),
            "dft_hid_re": _w(Wd_re),
            "dft_hid_im": _w(Wd_im),
        }
    _A_in = _A_setup()

    def _B_setup():
        B, S, D, F = 1, 2048, 512, 1792
        return {
            "x": _w((lambda *s: torch.randn(*s, dtype=torch.bfloat16) * 0.05)(B, S, D)),
            "w_gate": _w((lambda *s: torch.randn(*s, dtype=torch.bfloat16) * 1.6)(D, F)),
            "w_up": _w((lambda *s: torch.randn(*s, dtype=torch.bfloat16) * 0.8)(D, F)),
            "w_down": _w((lambda *s: torch.randn(*s, dtype=torch.bfloat16) * 0.8)(F, D)),
        }
    _B_in = _B_setup()

    return {
        "A_x": _A_in["x"],
        "A_dft_seq_re": _A_in["dft_seq_re"],
        "A_dft_seq_im": _A_in["dft_seq_im"],
        "A_dft_hid_re": _A_in["dft_hid_re"],
        "A_dft_hid_im": _A_in["dft_hid_im"],
        "B_x": _B_in["x"],
        "B_w_gate": _B_in["w_gate"],
        "B_w_up": _B_in["w_up"],
        "B_w_down": _B_in["w_down"],
    }

def reference(A_x, A_dft_seq_re, A_dft_seq_im, A_dft_hid_re, A_dft_hid_im, B_x, B_w_gate, B_w_up, B_w_down):
    def _A_ref(x, dft_seq_re, dft_seq_im, dft_hid_re, dft_hid_im):
        a = torch.einsum("bsd,ed->bse", x, dft_hid_re)
        b = torch.einsum("bsd,ed->bse", x, dft_hid_im)
        y = torch.einsum("ts,bse->bte", dft_seq_re, a) - torch.einsum("ts,bse->bte", dft_seq_im, b)
        return y

    def _B_ref(x, w_gate, w_up, w_down):
        g = torch.einsum("bsd,df->bsf", x, w_gate)
        u = torch.einsum("bsd,df->bsf", x, w_up)
        h = _sigmoid(g) * u
        return torch.einsum("bsf,fd->bsd", h, w_down)

    x = A_x
    h = x + _A_ref(_layernorm(x, axis=-1), A_dft_seq_re, A_dft_seq_im, A_dft_hid_re, A_dft_hid_im)
    h2 = h + _B_ref(_layernorm(h, axis=-1), B_w_gate, B_w_up, B_w_down)
    return h2
```

```cpp
#include <hip/hip_runtime.h>
#include <hip/hip_bf16.h>
#include <stdint.h>

typedef __attribute__((ext_vector_type(8))) short bf16x8;
typedef __attribute__((ext_vector_type(4))) float f32x4;
typedef unsigned short u16;

constexpr int S_ = 2048, D_ = 512, F_ = 1792;

__device__ __forceinline__ float bf2f(u16 v) { return __uint_as_float(((unsigned)v) << 16); }
__device__ __forceinline__ u16 f2bf(float f) {
  __hip_bfloat16 b = __float2bfloat16(f);
  return *reinterpret_cast<u16*>(&b);
}

__global__ __launch_bounds__(256) void ln_kernel(const u16* __restrict__ in, u16* __restrict__ out) {
  int row = blockIdx.x * 4 + (threadIdx.x >> 6);
  int lane = threadIdx.x & 63;
  bf16x8 v = *reinterpret_cast<const bf16x8*>(in + (size_t)row * D_ + lane * 8);
  float f[8]; float s = 0.f;
  for (int i = 0; i < 8; ++i) { f[i] = bf2f((u16)v[i]); s += f[i]; }
  for (int o = 32; o > 0; o >>= 1) s += __shfl_xor(s, o);
  float m = s * (1.f / D_);
  float q = 0.f;
  for (int i = 0; i < 8; ++i) { float d = f[i] - m; q += d * d; }
  for (int o = 32; o > 0; o >>= 1) q += __shfl_xor(q, o);
  float r = 1.0f / sqrtf(q * (1.f / D_) + 1e-5f);
  bf16x8 o8;
  for (int i = 0; i < 8; ++i) o8[i] = (short)f2bf((f[i] - m) * r);
  *reinterpret_cast<bf16x8*>(out + (size_t)row * D_ + lane * 8) = o8;
}

__global__ void transpose_kernel(const u16* __restrict__ in, u16* __restrict__ out, int R, int C) {
  __shared__ u16 t[32][33];
  int c0 = blockIdx.x * 32, r0 = blockIdx.y * 32;
  int tx = threadIdx.x & 31, ty = threadIdx.x >> 5;
  for (int i = ty; i < 32; i += 8) t[i][tx] = in[(size_t)(r0 + i) * C + c0 + tx];
  __syncthreads();
  for (int i = ty; i < 32; i += 8) out[(size_t)(c0 + i) * R + r0 + tx] = t[tx][i];
}

template <int MODE>
__global__ __launch_bounds__(256) void gemm_nt(const u16* __restrict__ A, const u16* __restrict__ A2, int lda,
                                               const u16* __restrict__ B, int ldb, int K, int Ksplit,
                                               const u16* __restrict__ res, void* __restrict__ out, int ldc) {
  int bm = blockIdx.y * 64, bn = blockIdx.x * 64;
  int wid = threadIdx.x >> 6, lane = threadIdx.x & 63;
  int wr = wid >> 1, wc = wid & 1, fr = lane & 15, fq = lane >> 4;
  f32x4 acc[2][2] = {};
  for (int k0 = 0; k0 < K; k0 += 32) {
    bf16x8 a[2], b[2];
    for (int i = 0; i < 2; ++i) {
      int r = bm + wr * 32 + i * 16 + fr;
      const u16* ap = (k0 < Ksplit) ? (A + (size_t)r * lda + k0) : (A2 + (size_t)r * lda + (k0 - Ksplit));
      a[i] = *reinterpret_cast<const bf16x8*>(ap + fq * 8);
      int c = bn + wc * 32 + i * 16 + fr;
      b[i] = *reinterpret_cast<const bf16x8*>(B + (size_t)c * ldb + k0 + fq * 8);
    }
    for (int i = 0; i < 2; ++i)
      for (int j = 0; j < 2; ++j)
        acc[i][j] = __builtin_amdgcn_mfma_f32_16x16x32_bf16(a[i], b[j], acc[i][j], 0, 0, 0);
  }
  for (int i = 0; i < 2; ++i)
    for (int j = 0; j < 2; ++j)
      for (int e = 0; e < 4; ++e) {
        int r = bm + wr * 32 + i * 16 + fq * 4 + e;
        int c = bn + wc * 32 + j * 16 + fr;
        float v = acc[i][j][e];
        if (MODE == 0) {
          ((u16*)out)[(size_t)r * ldc + c] = f2bf(v);
        } else if (MODE == 4) {
          ((u16*)out)[(size_t)r * ldc + c] = f2bf(-v);
        } else if (MODE == 1 || MODE == 3) {
          ((u16*)out)[(size_t)r * ldc + c] = f2bf(v + bf2f(res[(size_t)r * ldc + c]));
        } else {
          ((float*)out)[(size_t)r * ldc + c] = v;
        }
      }
}

__global__ void glu_kernel(const float* __restrict__ g, const float* __restrict__ u, u16* __restrict__ hh, int n) {
  int i = blockIdx.x * 256 + threadIdx.x;
  if (i < n) { float s = 1.f / (1.f + __expf(-g[i])); hh[i] = f2bf(s * u[i]); }
}

extern "C" void kernel_launch(void* const* d_in, const int* in_sizes, int n_in, void* d_out, int out_size,
                              void* d_ws, size_t ws_size, hipStream_t stream) {
  const u16* x = (const u16*)d_in[0];
  const u16* wsre = (const u16*)d_in[1];
  const u16* wsim = (const u16*)d_in[2];
  const u16* wdre = (const u16*)d_in[3];
  const u16* wdim = (const u16*)d_in[4];
  const u16* wg = (const u16*)d_in[6];
  const u16* wu = (const u16*)d_in[7];
  const u16* wdn = (const u16*)d_in[8];
  char* ws = (char*)d_ws;
  size_t off = 0;
  auto alloc = [&](size_t b) { void* p = ws + off; off += (b + 255) & ~(size_t)255; return p; };
  u16* xn = (u16*)alloc(S_ * D_ * 2);
  u16* abT = (u16*)alloc(D_ * 4096 * 2);
  u16* h = (u16*)alloc(S_ * D_ * 2);
  u16* hn = (u16*)alloc(S_ * D_ * 2);
  u16* wgT = (u16*)alloc(F_ * D_ * 2);
  u16* wuT = (u16*)alloc(F_ * D_ * 2);
  u16* wdT = (u16*)alloc(F_ * D_ * 2);
  float* g = (float*)alloc((size_t)S_ * F_ * 4);
  float* u = (float*)alloc((size_t)S_ * F_ * 4);
  u16* hh = (u16*)alloc((size_t)S_ * F_ * 2);

  ln_kernel<<<S_ / 4, 256, 0, stream>>>(x, xn);
  transpose_kernel<<<dim3(F_ / 32, D_ / 32), 256, 0, stream>>>(wg, wgT, D_, F_);
  transpose_kernel<<<dim3(F_ / 32, D_ / 32), 256, 0, stream>>>(wu, wuT, D_, F_);
  transpose_kernel<<<dim3(D_ / 32, F_ / 32), 256, 0, stream>>>(wdn, wdT, F_, D_);
  gemm_nt<0><<<dim3(S_ / 64, D_ / 64), 256, 0, stream>>>(wdre, wdre, D_, xn, D_, D_, D_, nullptr, abT, 4096);
  gemm_nt<4><<<dim3(S_ / 64, D_ / 64), 256, 0, stream>>>(wdim, wdim, D_, xn, D_, D_, D_, nullptr, abT + 2048, 4096);
  gemm_nt<1><<<dim3(D_ / 64, S_ / 64), 256, 0, stream>>>(wsre, wsim, S_, abT, 4096, 4096, 2048, x, h, D_);
  ln_kernel<<<S_ / 4, 256, 0, stream>>>(h, hn);
  gemm_nt<2><<<dim3(F_ / 64, S_ / 64), 256, 0, stream>>>(hn, hn, D_, wgT, D_, D_, D_, nullptr, g, F_);
  gemm_nt<2><<<dim3(F_ / 64, S_ / 64), 256, 0, stream>>>(hn, hn, D_, wuT, D_, D_, D_, nullptr, u, F_);
  glu_kernel<<<(S_ * F_ + 255) / 256, 256, 0, stream>>>(g, u, hh, S_ * F_);
  gemm_nt<3><<<dim3(D_ / 64, S_ / 64), 256, 0, stream>>>(hh, hh, F_, wdT, F_, F_, F_, h, d_out, D_);
}
```

```cpp
#include <hip/hip_runtime.h>
#include <hip/hip_bf16.h>
#include <stdint.h>

typedef __attribute__((ext_vector_type(8))) short bf16x8;
typedef __attribute__((ext_vector_type(4))) float f32x4;
typedef unsigned short u16;

constexpr int S_ = 2048, D_ = 512, F_ = 1792;

__device__ __forceinline__ float bf2f(u16 v) { return __uint_as_float(((unsigned)v) << 16); }
__device__ __forceinline__ u16 f2bf(float f) {
  __hip_bfloat16 b = __float2bfloat16(f);
  return *reinterpret_cast<u16*>(&b);
}

__global__ __launch_bounds__(256) void ln_kernel(const u16* __restrict__ in, u16* __restrict__ out) {
  int row = blockIdx.x * 4 + (threadIdx.x >> 6);
  int lane = threadIdx.x & 63;
  bf16x8 v = *reinterpret_cast<const bf16x8*>(in + (size_t)row * D_ + lane * 8);
  float f[8]; float s = 0.f;
  for (int i = 0; i < 8; ++i) { f[i] = bf2f((u16)v[i]); s += f[i]; }
  for (int o = 32; o > 0; o >>= 1) s += __shfl_xor(s, o);
  float m = s * (1.f / D_);
  float q = 0.f;
  for (int i = 0; i < 8; ++i) { float d = f[i] - m; q += d * d; }
  for (int o = 32; o > 0; o >>= 1) q += __shfl_xor(q, o);
  float r = 1.0f / sqrtf(q * (1.f / D_) + 1e-5f);
  bf16x8 o8;
  for (int i = 0; i < 8; ++i) o8[i] = (short)f2bf((f[i] - m) * r);
  *reinterpret_cast<bf16x8*>(out + (size_t)row * D_ + lane * 8) = o8;
}

__global__ void transpose_kernel(const u16* __restrict__ in, u16* __restrict__ out, int R, int C) {
  __shared__ u16 t[32][33];
  int c0 = blockIdx.x * 32, r0 = blockIdx.y * 32;
  int tx = threadIdx.x & 31, ty = threadIdx.x >> 5;
  for (int i = ty; i < 32; i += 8) t[i][tx] = in[(size_t)(r0 + i) * C + c0 + tx];
  __syncthreads();
  for (int i = ty; i < 32; i += 8) out[(size_t)(c0 + i) * R + r0 + tx] = t[tx][i];
}

template <int MODE>
__global__ __launch_bounds__(256) void gemm_nt(const u16* __restrict__ A, const u16* __restrict__ A2, int lda,
                                               const u16* __restrict__ B, int ldb, int K, int Ksplit,
                                               const u16* __restrict__ res, void* __restrict__ out, int ldc) {
  int bm = blockIdx.y * 64, bn = blockIdx.x * 64;
  int wid = threadIdx.x >> 6, lane = threadIdx.x & 63;
  int wr = wid >> 1, wc = wid & 1, fr = lane & 15, fq = lane >> 4;
  f32x4 acc[2][2] = {};
  for (int k0 = 0; k0 < K; k0 += 32) {
    bf16x8 a[2], b[2];
    for (int i = 0; i < 2; ++i) {
      int r = bm + wr * 32 + i * 16 + fr;
      const u16* ap = (k0 < Ksplit) ? (A + (size_t)r * lda + k0) : (A2 + (size_t)r * lda + (k0 - Ksplit));
      a[i] = *reinterpret_cast<const bf16x8*>(ap + fq * 8);
      int c = bn + wc * 32 + i * 16 + fr;
      b[i] = *reinterpret_cast<const bf16x8*>(B + (size_t)c * ldb + k0 + fq * 8);
    }
    for (int i = 0; i < 2; ++i)
      for (int j = 0; j < 2; ++j)
        acc[i][j] = __builtin_amdgcn_mfma_f32_16x16x32_bf16(a[i], b[j], acc[i][j], 0, 0, 0);
  }
  for (int i = 0; i < 2; ++i)
    for (int j = 0; j < 2; ++j)
      for (int e = 0; e < 4; ++e) {
        int r = bm + wr * 32 + i * 16 + fq * 4 + e;
        int c = bn + wc * 32 + j * 16 + fr;
        float v = acc[i][j][e];
        if (MODE == 0) {
          ((u16*)out)[(size_t)r * ldc + c] = f2bf(v);
        } else if (MODE == 4) {
          ((u16*)out)[(size_t)r * ldc + c] = f2bf(-v);
        } else if (MODE == 1 || MODE == 3) {
          ((u16*)out)[(size_t)r * ldc + c] = f2bf(v + bf2f(res[(size_t)r * ldc + c]));
        } else {
          ((float*)out)[(size_t)r * ldc + c] = v;
        }
      }
}

__global__ void glu_kernel(const float* __restrict__ g, const float* __restrict__ u, u16* __restrict__ hh, int n) {
  int i = blockIdx.x * 256 + threadIdx.x;
  if (i < n) { float s = 1.f / (1.f + __expf(-g[i])); hh[i] = f2bf(s * u[i]); }
}


template <int AUX>
__global__ __launch_bounds__(512) void probe_kernel(const char* __restrict__ a, const char* __restrict__ bq, int* sink, int mult) {
  extern __shared__ __attribute__((aligned(16))) char smem[];
  int tid = threadIdx.x, wid = tid >> 6, lane = tid & 63, b = blockIdx.x;
  const char* p = (b < 128 ? a + (size_t)b * 65536 : bq + (size_t)(b - 128) * 65536) + wid * 1024 + lane * 16;
  unsigned long long t0 = __builtin_amdgcn_s_memrealtime();
  for (int i = 0; i < 8; ++i)
    __builtin_amdgcn_global_load_lds((const unsigned*)(p + i * 8192), (unsigned*)(smem + (i * 8 + wid) * 1024), 16, 0, AUX);
  asm volatile("s_waitcnt vmcnt(0)" ::: "memory");
  __syncthreads();
  unsigned long long t1 = __builtin_amdgcn_s_memrealtime();
  unsigned long long d = t1 - t0;
  if (d > 3000) d = 3000;
  while (__builtin_amdgcn_s_memrealtime() - t1 < d * mult) __builtin_amdgcn_s_sleep(4);
  if (((int*)smem)[tid] == 0x12345679 && d == 77777) sink[tid] = 2;
}

extern "C" void kernel_launch(void* const* d_in, const int* in_sizes, int n_in, void* d_out, int out_size,
                              void* d_ws, size_t ws_size, hipStream_t stream) {
  const u16* x = (const u16*)d_in[0];
  const u16* wsre = (const u16*)d_in[1];
  const u16* wsim = (const u16*)d_in[2];
  const u16* wdre = (const u16*)d_in[3];
  const u16* wdim = (const u16*)d_in[4];
  const u16* wg = (const u16*)d_in[6];
  const u16* wu = (const u16*)d_in[7];
  const u16* wdn = (const u16*)d_in[8];
  char* ws = (char*)d_ws;
  size_t off = 0;
  auto alloc = [&](size_t b) { void* p = ws + off; off += (b + 255) & ~(size_t)255; return p; };
  u16* xn = (u16*)alloc(S_ * D_ * 2);
  u16* abT = (u16*)alloc(D_ * 4096 * 2);
  u16* h = (u16*)alloc(S_ * D_ * 2);
  u16* hn = (u16*)alloc(S_ * D_ * 2);
  u16* wgT = (u16*)alloc(F_ * D_ * 2);
  u16* wuT = (u16*)alloc(F_ * D_ * 2);
  u16* wdT = (u16*)alloc(F_ * D_ * 2);
  float* g = (float*)alloc((size_t)S_ * F_ * 4);
  float* u = (float*)alloc((size_t)S_ * F_ * 4);
  u16* hh = (u16*)alloc((size_t)S_ * F_ * 2);

  probe_kernel<0><<<256, 512, 65536, stream>>>((const char*)wsre, (const char*)wsim, (int*)(ws + (200u<<20)), 0);
  ln_kernel<<<S_ / 4, 256, 0, stream>>>(x, xn);
  transpose_kernel<<<dim3(F_ / 32, D_ / 32), 256, 0, stream>>>(wg, wgT, D_, F_);
  transpose_kernel<<<dim3(F_ / 32, D_ / 32), 256, 0, stream>>>(wu, wuT, D_, F_);
  transpose_kernel<<<dim3(D_ / 32, F_ / 32), 256, 0, stream>>>(wdn, wdT, F_, D_);
  gemm_nt<0><<<dim3(S_ / 64, D_ / 64), 256, 0, stream>>>(wdre, wdre, D_, xn, D_, D_, D_, nullptr, abT, 4096);
  gemm_nt<4><<<dim3(S_ / 64, D_ / 64), 256, 0, stream>>>(wdim, wdim, D_, xn, D_, D_, D_, nullptr, abT + 2048, 4096);
  gemm_nt<1><<<dim3(D_ / 64, S_ / 64), 256, 0, stream>>>(wsre, wsim, S_, abT, 4096, 4096, 2048, x, h, D_);
  ln_kernel<<<S_ / 4, 256, 0, stream>>>(h, hn);
  gemm_nt<2><<<dim3(F_ / 64, S_ / 64), 256, 0, stream>>>(hn, hn, D_, wgT, D_, D_, D_, nullptr, g, F_);
  gemm_nt<2><<<dim3(F_ / 64, S_ / 64), 256, 0, stream>>>(hn, hn, D_, wuT, D_, D_, D_, nullptr, u, F_);
  glu_kernel<<<(S_ * F_ + 255) / 256, 256, 0, stream>>>(g, u, hh, S_ * F_);
  gemm_nt<3><<<dim3(D_ / 64, S_ / 64), 256, 0, stream>>>(hh, hh, F_, wdT, F_, F_, F_, h, d_out, D_);
}
```

```cpp
#include <hip/hip_runtime.h>
#include <stdint.h>
#include <type_traits>

typedef unsigned int u32;
typedef unsigned long long u64;
typedef __attribute__((ext_vector_type(4))) u32 u32x4;
typedef __attribute__((ext_vector_type(2))) u32 u32x2;
typedef __attribute__((ext_vector_type(8))) __bf16 bf16x8;
typedef __attribute__((ext_vector_type(4))) __bf16 bf16x4;
typedef __attribute__((ext_vector_type(2))) __bf16 bf16x2;
typedef __attribute__((ext_vector_type(2))) float f32x2;
typedef __attribute__((ext_vector_type(4))) float f32x4;
typedef __attribute__((ext_vector_type(16))) float f32x16;
typedef __amdgpu_buffer_rsrc_t rsrc_t;
#define DI __device__ __forceinline__
#define LAS __attribute__((address_space(3)))
#define GAS __attribute__((address_space(1)))
#define RLX_AGENT __ATOMIC_RELAXED, __HIP_MEMORY_SCOPE_AGENT

constexpr u32 WS_EPOCH = 0;
constexpr u32 WS_F1 = 4096;
constexpr u32 WS_F2 = WS_F1 + 256 * 256;
constexpr u32 WS_F3 = WS_F2 + 256 * 256;
constexpr u32 WS_AB = 256 * 1024;
constexpr u32 WS_Y = WS_AB + 4 * 1024 * 1024;
constexpr u32 WS_HH = WS_Y + 4 * 1024 * 1024;
constexpr u32 WS_END = WS_HH + 64 * 112 * 1024;
constexpr u32 WS_STAMP = WS_END;
constexpr u32 RG = 32768;
constexpr u32 LDS_BYTES = 5 * RG;

template <int I, int N, class F> DI void sfor(F&& f) {
  if constexpr (I < N) { f(std::integral_constant<int, I>{}); sfor<I + 1, N>(f); }
}
DI u32 rfl(u32 x) { return __builtin_amdgcn_readfirstlane(x); }

template <int IMM> DI void bload(u32x4& d, u32 voff, rsrc_t r, u32 soff) {
  asm volatile("buffer_load_dwordx4 %0, %1, %2, %3 offen offset:%4" : "+v"(d) : "v"(voff), "s"(r), "s"(soff), "i"(IMM));
}
template <int IMM> DI void bload_sc1(u32x4& d, u32 voff, rsrc_t r, u32 soff) {
  asm volatile("buffer_load_dwordx4 %0, %1, %2, %3 offen offset:%4 sc1" : "+v"(d) : "v"(voff), "s"(r), "s"(soff), "i"(IMM));
}
DI void dma4(u32 m0, u32 voff, rsrc_t r, u32 s0, u32 s1, u32 s2, u32 s3) {
  asm volatile("s_mov_b32 m0, %0\n\ts_nop 0\n\t"
               "buffer_load_dwordx4 %1, %2, %3 offen lds\n\t"
               "buffer_load_dwordx4 %1, %2, %4 offen offset:1024 lds\n\t"
               "buffer_load_dwordx4 %1, %2, %5 offen offset:2048 lds\n\t"
               "buffer_load_dwordx4 %1, %2, %6 offen offset:3072 lds"
               :: "s"(m0), "v"(voff), "s"(r), "s"(s0), "s"(s1), "s"(s2), "s"(s3) : "memory");
}
DI void dma4_sc1(u32 m0, u32 voff, rsrc_t r, u32 s0) {
  asm volatile("s_mov_b32 m0, %0\n\ts_nop 0\n\t"
               "buffer_load_dwordx4 %1, %2, %3 offen sc1 lds\n\t"
               "buffer_load_dwordx4 %1, %2, %3 offen offset:1024 sc1 lds\n\t"
               "buffer_load_dwordx4 %1, %2, %3 offen offset:2048 sc1 lds\n\t"
               "buffer_load_dwordx4 %1, %2, %3 offen offset:3072 sc1 lds"
               :: "s"(m0), "v"(voff), "s"(r), "s"(s0) : "memory");
}
template <int N> DI void wait4(u32x4& a, u32x4& b, u32x4& c, u32x4& d) {
  asm volatile("s_waitcnt vmcnt(%4)" : "+v"(a), "+v"(b), "+v"(c), "+v"(d) : "i"(N));
}
DI void pin4(u32x4& a, u32x4& b, u32x4& c, u32x4& d) { asm volatile("" : "+v"(a), "+v"(b), "+v"(c), "+v"(d)); }
template <int N> DI void waitv() { asm volatile("s_waitcnt vmcnt(%0)" :: "i"(N) : "memory"); }
DI void bar() { asm volatile("s_waitcnt lgkmcnt(0)\n\ts_barrier" ::: "memory"); }
template <int N> DI void waitbar() { asm volatile("s_waitcnt vmcnt(%0) lgkmcnt(0)\n\ts_barrier" :: "i"(N) : "memory"); }

DI f32x16 mfma(u32x4 a, u32x4 b, f32x16 c) {
  return __builtin_amdgcn_mfma_f32_32x32x16_bf16(__builtin_bit_cast(bf16x8, a), __builtin_bit_cast(bf16x8, b), c, 0, 0, 0);
}
DI u32 cvtpk(float lo, float hi) { f32x2 v = {lo, hi}; return __builtin_bit_cast(u32, __builtin_convertvector(v, bf16x2)); }
DI float blo(u32 x) { return __uint_as_float(x << 16); }
DI float bhi(u32 x) { return __uint_as_float(x & 0xffff0000u); }
DI float dot2(u32 a, u32 b, float c) {
  return __builtin_amdgcn_fdot2_f32_bf16(__builtin_bit_cast(bf16x2, a), __builtin_bit_cast(bf16x2, b), c, false);
}
DI u32x4 pack8(const f32x16& a, int o) {
  u32x4 r;
  r[0] = cvtpk(a[o + 0], a[o + 1]); r[1] = cvtpk(a[o + 2], a[o + 3]);
  r[2] = cvtpk(a[o + 4], a[o + 5]); r[3] = cvtpk(a[o + 6], a[o + 7]);
  return r;
}
DI u32x4 lds128(u32 addr) { return *(LAS u32x4*)(uintptr_t)addr; }
DI void lds128w(u32 addr, u32x4 v) { *(LAS u32x4*)(uintptr_t)addr = v; }
DI u32x2 ldstr(u32 addr) { return __builtin_bit_cast(u32x2, __builtin_amdgcn_ds_read_tr16_b64_v4bf16((LAS bf16x4*)(uintptr_t)addr)); }
DI u32x4 ldstr2(u32 a0, u32 a1) { u32x2 x = ldstr(a0), y = ldstr(a1); u32x4 r = {x[0], x[1], y[0], y[1]}; return r; }

#define QSTAGE(QP)                                                                                          \
  asm volatile("s_mov_b64 vcc, %16\n\t"                                                                     \
               "v_cndmask_b32_dpp %0, %10, %8, vcc " QP " row_mask:0xf bank_mask:0xf\n\t"                   \
               "v_cndmask_b32_dpp %1, %11, %9, vcc " QP " row_mask:0xf bank_mask:0xf\n\t"                   \
               "v_cndmask_b32_dpp %4, %14, %12, vcc " QP " row_mask:0xf bank_mask:0xf\n\t"                  \
               "v_cndmask_b32_dpp %5, %15, %13, vcc " QP " row_mask:0xf bank_mask:0xf\n\t"                  \
               "s_not_b64 vcc, vcc\n\t"                                                                     \
               "v_cndmask_b32_dpp %2, %8, %10, vcc " QP " row_mask:0xf bank_mask:0xf\n\t"                   \
               "v_cndmask_b32_dpp %3, %9, %11, vcc " QP " row_mask:0xf bank_mask:0xf\n\t"                   \
               "v_cndmask_b32_dpp %6, %12, %14, vcc " QP " row_mask:0xf bank_mask:0xf\n\t"                  \
               "v_cndmask_b32_dpp %7, %13, %15, vcc " QP " row_mask:0xf bank_mask:0xf\n\t"                  \
               "s_nop 1"                                                                                    \
               : "=&v"(oa0), "=&v"(oa1), "=&v"(ob0), "=&v"(ob1), "=&v"(oc0), "=&v"(oc1), "=&v"(od0), "=&v"(od1) \
               : "v"(a0), "v"(a1), "v"(b0), "v"(b1), "v"(c0), "v"(c1), "v"(d0), "v"(d1), "s"(mask)          \
               : "vcc", "scc")
DI void qstage1(u32& oa0, u32& oa1, u32& ob0, u32& ob1, u32& oc0, u32& oc1, u32& od0, u32& od1,
                u32 a0, u32 a1, u32 b0, u32 b1, u32 c0, u32 c1, u32 d0, u32 d1) {
  const u64 mask = 0x5555555555555555ull;
  QSTAGE("quad_perm:[1,0,3,2]");
}
DI void qstage2(u32& oa0, u32& oa1, u32& ob0, u32& ob1, u32& oc0, u32& oc1, u32& od0, u32& od1,
                u32 a0, u32 a1, u32 b0, u32 b1, u32 c0, u32 c1, u32 d0, u32 d1) {
  const u64 mask = 0x3333333333333333ull;
  QSTAGE("quad_perm:[2,3,0,1]");
}
DI void qtrans(const u32x4 (&D)[4], u32x4 (&T)[4]) {
  u32 E[4][4], R[4][4];
#pragma unroll
  for (int c = 0; c < 4; c += 2)
    qstage1(E[0][c], E[0][c + 1], E[1][c], E[1][c + 1], E[2][c], E[2][c + 1], E[3][c], E[3][c + 1],
            D[0][c], D[0][c + 1], D[1][c], D[1][c + 1], D[2][c], D[2][c + 1], D[3][c], D[3][c + 1]);
#pragma unroll
  for (int c = 0; c < 4; c += 2)
    qstage2(R[0][c], R[0][c + 1], R[2][c], R[2][c + 1], R[1][c], R[1][c + 1], R[3][c], R[3][c + 1],
            E[0][c], E[0][c + 1], E[2][c], E[2][c + 1], E[1][c], E[1][c + 1], E[3][c], E[3][c + 1]);
#pragma unroll
  for (int j = 0; j < 4; ++j) { T[j][0] = R[j][0]; T[j][1] = R[j][1]; T[j][2] = R[j][2]; T[j][3] = R[j][3]; }
}

DI void poll_flags(char* ws, u32 off, u32 n, u64 tag, u32 lane) {
  GAS u64* p = (GAS u64*)(ws + off + (size_t)(lane < n ? lane : 0) * 256);
  for (u32 spins = 0; spins < (1u << 16); ++spins) {
    u64 v = __hip_atomic_load(p, RLX_AGENT);
    if (__all(v == tag)) break;
    __builtin_amdgcn_s_sleep(1);
  }
  asm volatile("" ::: "memory");
}

#define STAMP(k) do { } while (0)

__global__ __launch_bounds__(512, 2) void fused_kernel(const void* __restrict__ x_, const void* __restrict__ wsre_, const void* __restrict__ wsim_,
                                                      const void* __restrict__ wdre_, const void* __restrict__ wdim_,
                                                      const void* __restrict__ wg_, const void* __restrict__ wu_, const void* __restrict__ wdn_,
                                                      void* __restrict__ out_, char* __restrict__ ws) {
  extern __shared__ __attribute__((aligned(16))) char smem[];
  const u32 L0 = (u32)(uintptr_t)(LAS char*)smem;
  const u32 tid = threadIdx.x, lane = tid & 63, w = rfl(tid >> 6);
  const u32 b = blockIdx.x, xcd = b & 7, i = b >> 3, ri = xcd & 1;
  const u32 l31 = lane & 31, hh = lane >> 5;
  STAMP(0);

  const rsrc_t rX = __builtin_amdgcn_make_buffer_rsrc((void*)x_, 0, 2048 * 512 * 2, 0x00020000);
  const rsrc_t rWs = __builtin_amdgcn_make_buffer_rsrc((void*)(ri ? wsim_ : wsre_), 0, 2048 * 2048 * 2, 0x00020000);
  const rsrc_t rWd = __builtin_amdgcn_make_buffer_rsrc((void*)(ri ? wdim_ : wdre_), 0, 512 * 512 * 2, 0x00020000);
  const rsrc_t rW = __builtin_amdgcn_make_buffer_rsrc((void*)ws, 0, WS_END, 0x00020000);

  u64 tag;
  asm volatile("buffer_load_dwordx2 %0, off, %1, 0 sc1" : "=v"(tag) : "s"(rW));

  const u32 wq = (lane >> 2) & 7;
  const u32 rmcol = hh * 64 + (lane & 3) * 16;

  u32x4 W[8][4];

  const u32 st = (xcd >> 1) * 8 + (i & 7), et128 = i >> 3;
  const u32 tt = (xcd >> 1) * 4 + (i & 3), et64 = i >> 2;
  const u32 kh = w >> 2, t32 = tt * 4 + (w & 3);
  const u32 s0 = (et64 + tt) & 3, s1 = et64 >> 2;
  const u32 vWs = (t32 * 32 + wq * 4) * 4096 + rmcol;
  auto ws_blk = [&](int n) -> u32 {
    return kh * 16 + (n & 8) + 2 * (((n >> 1) + s0) & 3) + ((n & 1) ^ s1);
  };
  auto issue_ws = [&](auto nc, auto slotc) {
    constexpr int n = nc, slot = slotc;
    const u32 so = ws_blk(n) * 128;
    bload<0>(W[slot][0], vWs, rWs, so);
    bload<0>(W[slot][1], vWs, rWs, so + 4096);
    bload<0>(W[slot][2], vWs, rWs, so + 8192);
    bload<0>(W[slot][3], vWs, rWs, so + 12288);
  };
  f32x16 acc[2];
  {
    u32x4 xr[8];
    const u32 vx = (st * 64 + w * 8 + (lane >> 3)) * 1024 + (lane & 7) * 16;
    sfor<0, 8>([&](auto kc) { constexpr int k = kc; bload<0>(xr[k], vx, rX, ((k + 2 * et128) & 7) * 128); });
    const u32 M1 = w & 1, e64 = et128 * 2 + ((w >> 1) & 1);
    if (w < 4) {
      const u32 vd = (e64 * 64 + 32 * (wq & 1) + 16 * M1 + 8 * (wq >> 2) + 4 * ((wq >> 1) & 1)) * 1024 + rmcol;
      sfor<0, 8>([&](auto nc) {
        constexpr int n = nc;
        const u32 so = ((n + st) & 7) * 128;
        bload<0>(W[n][0], vd, rWd, so); bload<1024>(W[n][1], vd, rWd, so);
        bload<2048>(W[n][2], vd, rWd, so); bload<3072>(W[n][3], vd, rWd, so);
      });
      waitv<32>();
    } else {
      waitv<0>();
    }
    pin4(xr[0], xr[1], xr[2], xr[3]);
    pin4(xr[4], xr[5], xr[6], xr[7]);
    asm volatile("" : "+v"(tag));
    tag += 1;
    float sm = 0.f, sq = 0.f;
#pragma unroll
    for (int k = 0; k < 8; ++k)
#pragma unroll
      for (int c = 0; c < 4; ++c) { sm = dot2(xr[k][c], 0x3f803f80u, sm); sq = dot2(xr[k][c], xr[k][c], sq); }
    sm += __builtin_bit_cast(float, __builtin_amdgcn_mov_dpp(__builtin_bit_cast(int, sm), 0xB1, 0xf, 0xf, true));
    sq += __builtin_bit_cast(float, __builtin_amdgcn_mov_dpp(__builtin_bit_cast(int, sq), 0xB1, 0xf, 0xf, true));
    sm += __builtin_bit_cast(float, __builtin_amdgcn_mov_dpp(__builtin_bit_cast(int, sm), 0x4E, 0xf, 0xf, true));
    sq += __builtin_bit_cast(float, __builtin_amdgcn_mov_dpp(__builtin_bit_cast(int, sq), 0x4E, 0xf, 0xf, true));
    sm += __builtin_bit_cast(float, __builtin_amdgcn_mov_dpp(__builtin_bit_cast(int, sm), 0x141, 0xf, 0xf, true));
    sq += __builtin_bit_cast(float, __builtin_amdgcn_mov_dpp(__builtin_bit_cast(int, sq), 0x141, 0xf, 0xf, true));
    const float mean = sm * (1.f / 512.f);
    const float var = fmaxf(sq * (1.f / 512.f) - mean * mean, 0.f);
    const float rstd = __builtin_amdgcn_rsqf(var + 1e-5f);
    const float nb = -mean * rstd;
    const u32 sl = w * 8 + (lane >> 3);
    const u32 pp = 32 * ((sl >> 4) & 1) + 16 * ((sl >> 3) & 1) + 8 * ((sl >> 2) & 1) + 4 * (sl >> 5) + (sl & 3);
    const u32 lw = L0 + pp * 1040 + (lane & 7) * 16;
#pragma unroll
    for (int k = 0; k < 8; ++k) {
      u32x4 o;
#pragma unroll
      for (int c = 0; c < 4; ++c) o[c] = cvtpk(fmaf(blo(xr[k][c]), rstd, nb), fmaf(bhi(xr[k][c]), rstd, nb));
      lds128w(lw + ((k + 2 * et128) & 7) * 128, o);
    }
    u32x4 X[4];
    if (w >= 4) {
      if (ri == 0) {
        const u32 vr = (t32 * 32 + wq * 4) * 1024 + et64 * 128 + rmcol;
        bload<0>(X[0], vr, rX, 0); bload<1024>(X[1], vr, rX, 0); bload<2048>(X[2], vr, rX, 0); bload<3072>(X[3], vr, rX, 0);
      }
      sfor<0, 8>([&](auto nc) { issue_ws(nc, nc); });
    }
    bar();
    STAMP(1);
#pragma unroll
    for (int r = 0; r < 16; ++r) { acc[0][r] = 0.f; acc[1][r] = 0.f; }
    if (w < 4) {
      const u32 la = L0 + l31 * 1040 + hh * 64;
      sfor<0, 8>([&](auto nc) {
        constexpr int n = nc;
        wait4<4 * (7 - n)>(W[n][0], W[n][1], W[n][2], W[n][3]);
        u32x4 T[4];
        qtrans(W[n], T);
        const u32 lb = la + ((n + st) & 7) * 128;
#pragma unroll
        for (int j = 0; j < 4; ++j) {
          acc[0] = mfma(lds128(lb + j * 16), T[j], acc[0]);
          acc[1] = mfma(lds128(lb + 32 * 1040 + j * 16), T[j], acc[1]);
        }
      });
      if (ri) {
#pragma unroll
        for (int r = 0; r < 16; ++r) { acc[0][r] = -acc[0][r]; acc[1][r] = -acc[1][r]; }
      }
      const u32 ob = WS_AB + (((ri * 8 + e64) * 32 + st) * 2 + M1) * 4096;
#pragma unroll
      for (int j = 0; j < 4; ++j)
        __builtin_amdgcn_raw_buffer_store_b128(pack8(acc[j >> 1], 8 * (j & 1)), rW, lane * 16 + j * 1024, ob, 16);
      waitv<0>();
#pragma unroll
      for (int r = 0; r < 16; ++r) { acc[0][r] = 0.f; acc[1][r] = 0.f; }
    } else if (ri == 0) {
      wait4<32>(X[0], X[1], X[2], X[3]);
      u32x4 T[4];
      qtrans(X, T);
      const bool on = hh == ((l31 >> 2) & 1);
      const u32 ii = 4 * ((l31 >> 3) & 1) + (l31 & 3);
      const u32 val = on ? ((ii & 1) ? 0x3f800000u : 0x00003f80u) : 0u;
      u32x4 I0, I1;
#pragma unroll
      for (int c = 0; c < 4; ++c) {
        const u32 z = ((ii >> 1) == (u32)c) ? val : 0u;
        I0[c] = (l31 < 16) ? z : 0u; I1[c] = (l31 < 16) ? 0u : z;
      }
      acc[0] = mfma(I0, T[0], acc[0]); acc[0] = mfma(I1, T[1], acc[0]);
      acc[1] = mfma(I0, T[2], acc[1]); acc[1] = mfma(I1, T[3], acc[1]);
    }
    bar();
    STAMP(2);
    if (tid == 0) __hip_atomic_store((GAS u64*)(ws + WS_F1 + ((ri * 4 + et128) * 32 + st) * 256), tag, RLX_AGENT);
    if (w < 4) sfor<0, 8>([&](auto nc) { issue_ws(nc, nc); });
    if (w == 0) poll_flags(ws, WS_F1 + (ri * 4 + (et64 >> 1)) * 32 * 256, 32, tag, lane);
    bar();
    STAMP(3);
  }

  const bool p3 = i < 28;
  const u32 J = p3 ? (xcd & 1) * 7 + (i >> 2) : 0;
  const rsrc_t rW3 = __builtin_amdgcn_make_buffer_rsrc((void*)((w & 1) ? wu_ : wg_), 0, 512 * 1792 * 2, 0x00020000);
  const u32 vW3 = (lane >> 4) * 3584 + J * 256 + ((((lane & 15) >> 1) ^ (2 * ((lane >> 4) & 3) + (w & 1))) * 32) + (lane & 1) * 16;
  auto w3_unit = [&](int n, u32 region, u32 kq) {
    const u32 c = (n + 2 * (tt & 3)) & 7;
    const u32 so = (c * 64 + kq * 16) * 3584;
    dma4(L0 + region * RG + (w & 1) * 16384 + kq * 4096, vW3, rW3, so, so + 4 * 3584 - 1024, so + 8 * 3584 - 2048, so + 12 * 3584 - 3072);
  };
  {
    const u32 abase = WS_AB + (ri * 8 + et64) * 32 * 8192 + (kh * 16 + ((w >> 1) & 1)) * 8192 + (w & 1) * 4096;
    auto issue_ab = [&](int sg) {
      const u32 rs = (sg & 4) + ((sg + s0) & 3);
      dma4_sc1(L0 + (sg & 3) * RG + w * 4096, lane * 16, rW, abase + rs * 16384);
    };
    issue_ab(0); issue_ab(1); issue_ab(2);
    sfor<0, 8>([&](auto sgc) {
      constexpr int sg = sgc;
      constexpr int NW[8] = {8, 16, 24, 32, 32, 28, 20, 12};
      waitbar<NW[sg]>();
      if constexpr (sg + 3 < 8) issue_ab(sg + 3);
      if constexpr (sg == 4) w3_unit(0, 4, w >> 1);
      if constexpr (sg == 5) w3_unit(1, 0, w >> 1);
      if constexpr (sg == 6) w3_unit(2, 1, w >> 1);
      sfor<0, 2>([&](auto bc) {
        constexpr int bb = bc, n = 2 * sg + bb, slot = n & 7;
        pin4(W[slot][0], W[slot][1], W[slot][2], W[slot][3]);
        u32x4 T[4];
        qtrans(W[slot], T);
        if constexpr (n + 8 < 16) issue_ws(std::integral_constant<int, n + 8>{}, std::integral_constant<int, slot>{});
        const u32 lb = L0 + (sg & 3) * RG + kh * 16384 + ((u32)bb ^ s1) * 8192 + lane * 16;
#pragma unroll
        for (int j = 0; j < 4; ++j) {
          acc[0] = mfma(lds128(lb + j * 1024), T[j], acc[0]);
          acc[1] = mfma(lds128(lb + 4096 + j * 1024), T[j], acc[1]);
        }
      });
    });
    STAMP(4);
    const u32 lr = L0 + 2 * RG + (w & 3) * 8192 + lane * 16;
    if (w >= 4) {
#pragma unroll
      for (int g = 0; g < 8; ++g) {
        f32x4 v = {acc[g >> 2][4 * (g & 3)], acc[g >> 2][4 * (g & 3) + 1], acc[g >> 2][4 * (g & 3) + 2], acc[g >> 2][4 * (g & 3) + 3]};
        lds128w(lr + g * 1024, __builtin_bit_cast(u32x4, v));
      }
    }
    waitbar<0>();
    if (w >= 4) {
      w3_unit(3, 3, (w >> 1) & 1); w3_unit(3, 3, 2 + ((w >> 1) & 1));
    } else {
#pragma unroll
      for (int g = 0; g < 8; ++g) {
        f32x4 v = __builtin_bit_cast(f32x4, lds128(lr + g * 1024));
#pragma unroll
        for (int c = 0; c < 4; ++c) acc[g >> 2][4 * (g & 3) + c] += v[c];
      }
      const u32 ob = WS_Y + ((ri * 64 + t32) * 8 + et64) * 4096;
#pragma unroll
      for (int q = 0; q < 4; ++q)
        __builtin_amdgcn_raw_buffer_store_b128(pack8(acc[q >> 1], 8 * (q & 1)), rW, lane * 16 + q * 1024, ob, 16);
    }
  }

  const u32 g16 = lane >> 4, tq = (lane >> 2) & 3, tp = lane & 3;
  const u32 t64 = xcd * 4 + (i & 3);
  const rsrc_t rW4 = __builtin_amdgcn_make_buffer_rsrc((void*)wdn_, 0, 1792 * 512 * 2, 0x00020000);
  const u32 vW4 = (lane >> 3) * 1024 + et64 * 128 + ((((lane & 7) >> 1) ^ ((lane >> 4) & 1)) * 32) + (lane & 1) * 16;
  const u32 rot4 = et64 + 2 * (i & 3);
  auto c4 = [&](int n) -> u32 { u32 c = n + rot4; return c % 7; };
  auto w4_unit = [&](int n, u32 region, u32 u) {
    const u32 so = (c4(n) * 256 + u * 32) * 1024;
    dma4(L0 + region * RG + u * 4096, vW4, rW4, so, so + 8192 - 1024, so + 16384 - 2048, so + 24576 - 3072);
  };
  {
    const u32 ltr = L0 + (g16 & 1) * 16384 + ((g16 >> 1) * 32 + tq) * 256 + ((w ^ (2 * tq + (g16 & 1))) * 32) + tp * 8;
    auto w3_read = [&](auto nc, u32 region) {
      constexpr int n = nc;
#pragma unroll
      for (int j = 0; j < 4; ++j) W[n][j] = ldstr2(ltr + region * RG + j * 2048, ltr + region * RG + j * 2048 + 1024);
    };
    typedef std::integral_constant<int, 0> C0; typedef std::integral_constant<int, 1> C1; typedef std::integral_constant<int, 2> C2;
    typedef std::integral_constant<int, 3> C3; typedef std::integral_constant<int, 4> C4; typedef std::integral_constant<int, 5> C5;
    typedef std::integral_constant<int, 6> C6; typedef std::integral_constant<int, 7> C7;
    w3_read(C0{}, 4); w3_read(C1{}, 0); w3_read(C2{}, 1);
    if (w < 4) waitv<0>();
    bar();
    STAMP(5);
    if (tid == 0) __hip_atomic_store((GAS u64*)(ws + WS_F2 + ((tt * 2 + ri) * 8 + et64) * 256), tag, RLX_AGENT);
    if (p3) {
      if (w >= 4) {
        const u32 kq = (w >> 1) & 1;
        w3_unit(4, 2, kq); w3_unit(4, 2, 2 + kq); w3_unit(5, 4, kq); w3_unit(5, 4, 2 + kq);
        w3_unit(6, 0, kq); w3_unit(6, 0, 2 + kq); w3_unit(7, 1, kq); w3_unit(7, 1, 2 + kq);
      }
      if (w == 0) poll_flags(ws, WS_F2 + tt * 16 * 256, 16, tag, lane);
      waitbar<32>();
      STAMP(6);
      if (b == 0 && tid == 0) __hip_atomic_store((GAS u64*)(ws + WS_EPOCH), tag, RLX_AGENT);
      u32x4 Yb[1][8];
      auto rtile = [&](int r) -> u32 { return (r + J) & 3; };
      auto issue_y = [&](auto rc) {
        constexpr int r = rc, s = 0;
        const u32 so = WS_Y + ((tt * 4 + rtile(r)) * 8 + w) * 4096;
        bload_sc1<0>(Yb[s][0], lane * 16, rW, so); bload_sc1<1024>(Yb[s][1], lane * 16, rW, so);
        bload_sc1<2048>(Yb[s][2], lane * 16, rW, so); bload_sc1<3072>(Yb[s][3], lane * 16, rW, so);
        bload_sc1<0>(Yb[s][4], lane * 16, rW, so + 2097152); bload_sc1<1024>(Yb[s][5], lane * 16, rW, so + 2097152);
        bload_sc1<2048>(Yb[s][6], lane * 16, rW, so + 2097152); bload_sc1<3072>(Yb[s][7], lane * 16, rW, so + 2097152);
      };
      constexpr u32 HREG[4] = {3, 2, 4, 0};
      const u32 lst = L0 + RG + 24576;
      auto stage_h = [&](auto rc, auto nwc) {
        constexpr int r = rc, s = 0, nw = nwc;
        wait4<nw>(Yb[s][0], Yb[s][1], Yb[s][2], Yb[s][3]);
        pin4(Yb[s][4], Yb[s][5], Yb[s][6], Yb[s][7]);
        float sm = 0.f, sq = 0.f;
#pragma unroll
        for (int q = 0; q < 4; ++q) {
          u32x4 o;
#pragma unroll
          for (int c = 0; c < 4; ++c) {
            const float lo = blo(Yb[s][q][c]) + blo(Yb[s][4 + q][c]);
            const float hi = bhi(Yb[s][q][c]) + bhi(Yb[s][4 + q][c]);
            o[c] = cvtpk(lo, hi);
            sm = dot2(o[c], 0x3f803f80u, sm); sq = dot2(o[c], o[c], sq);
          }
          lds128w(L0 + HREG[r] * RG + w * 4096 + q * 1024 + lane * 16, o);
        }
        auto ps = __builtin_amdgcn_permlane32_swap(__float_as_uint(sm), __float_as_uint(sm), false, false);
        auto pq = __builtin_amdgcn_permlane32_swap(__float_as_uint(sq), __float_as_uint(sq), false, false);
        f32x2 v = {__uint_as_float(ps[0]) + __uint_as_float(ps[1]), __uint_as_float(pq[0]) + __uint_as_float(pq[1])};
        *(LAS f32x2*)(uintptr_t)(lst + r * 2048 + l31 * 64 + w * 8) = v;
      };
      issue_y(C0{});
      w3_read(C3{}, 3);
      waitbar<8>();
      w3_read(C4{}, 2); w3_read(C5{}, 4); w3_read(C6{}, 0); w3_read(C7{}, 1);
      f32x16 cs;
#pragma unroll
      for (int r = 0; r < 16; ++r) cs[r] = 0.f;
      {
        const u32x4 ones = {0x3f803f80u, 0x3f803f80u, 0x3f803f80u, 0x3f803f80u};
#pragma unroll
        for (int n = 0; n < 8; ++n)
#pragma unroll
          for (int j = 0; j < 4; ++j) cs = mfma(W[n][j], ones, cs);
      }
      bar();
      const u32 lcs = L0 + RG + 23552 + w * 128 + hh * 64;
      if (l31 == 0) {
#pragma unroll
        for (int g = 0; g < 4; ++g) {
          f32x4 v = {cs[4 * g], cs[4 * g + 1], cs[4 * g + 2], cs[4 * g + 3]};
          lds128w(lcs + g * 16, __builtin_bit_cast(u32x4, v));
        }
      }
      stage_h(C0{}, std::integral_constant<int, 0>{});
      issue_y(C1{});
      auto epilogue = [&](auto rc) {
        constexpr int r = rc;
        float sm = 0.f, sq = 0.f;
#pragma unroll
        for (int k = 0; k < 4; ++k) {
          f32x4 v = __builtin_bit_cast(f32x4, lds128(lst + r * 2048 + l31 * 64 + k * 16));
          sm += v[0] + v[2]; sq += v[1] + v[3];
        }
        const float mean = sm * (1.f / 512.f);
        const float var = fmaxf(sq * (1.f / 512.f) - mean * mean, 0.f);
        const float rstd = __builtin_amdgcn_rsqf(var + 1e-5f);
        const float ge = -rstd * 1.4426950408889634f;
        float hv[8];
        f32x4 cv[4];
#pragma unroll
        for (int k = 0; k < 4; ++k) cv[k] = __builtin_bit_cast(f32x4, lds128(lcs + k * 16));
#pragma unroll
        for (int k = 0; k < 8; ++k) {
          const float g = fmaf(-mean, cv[k >> 2][k & 3], acc[r & 1][k]);
          const float u = fmaf(-mean, cv[2 + (k >> 2)][k & 3], acc[r & 1][8 + k]);
          const float e = __builtin_amdgcn_exp2f(g * ge);
          hv[k] = (u * rstd) * __builtin_amdgcn_rcpf(1.f + e);
        }
        u32x4 o = {cvtpk(hv[0], hv[1]), cvtpk(hv[2], hv[3]), cvtpk(hv[4], hv[5]), cvtpk(hv[6], hv[7])};
        __builtin_amdgcn_raw_buffer_store_b128(o, rW, lane * 16, WS_HH + ((tt * 4 + rtile(r)) * 112 + J * 8 + w) * 1024, 16);
      };
      sfor<0, 4>([&](auto rc) {
        constexpr int r = rc;
        bar();
        if constexpr (r == 0) STAMP(7);
        if constexpr (r >= 1) w4_unit(r - 1, HREG[r - 1], w);
#pragma unroll
        for (int k = 0; k < 16; ++k) acc[r & 1][k] = 0.f;
        sfor<0, 8>([&](auto nc) {
          constexpr int n = nc;
          const u32 lb = L0 + HREG[r] * RG + ((n + 2 * (tt & 3)) & 7) * 4096 + lane * 16;
#pragma unroll
          for (int j = 0; j < 4; ++j) acc[r & 1] = mfma(W[n][j], lds128(lb + j * 1024), acc[r & 1]);
          if constexpr (n == 1 && r >= 1) epilogue(std::integral_constant<int, (r >= 1 ? r - 1 : 0)>{});
          if constexpr (n == 4 && r < 3) {
            constexpr int NWY[3] = {0, 4, 4};
            stage_h(std::integral_constant<int, (r < 3 ? r + 1 : 0)>{}, std::integral_constant<int, NWY[r < 3 ? r : 0]>{});
            if constexpr (r < 2) issue_y(std::integral_constant<int, r + 2>{});
          }
        });
      });
      STAMP(8);
      epilogue(C3{});
      waitbar<0>();
      STAMP(9);
      if (tid == 0) __hip_atomic_store((GAS u64*)(ws + WS_F3 + (tt * 16 + J) * 256), tag, RLX_AGENT);
    } else {
      waitbar<0>();
      w4_unit(0, 3, w); w4_unit(1, 2, w); w4_unit(2, 4, w);
      waitbar<0>();
    }
  }

  {
    const u32 kg = w >> 1, eh = w & 1;
    u32x4 V[7][4];
    const u32 ltr = L0 + (kg * 16 + (g16 >> 1) * 4 + tq) * 128 + ((2 * (g16 & 1) + (eh ^ (tq >> 1))) * 32) + tp * 8;
    auto w4_read = [&](auto nc, u32 region) {
      constexpr int n = nc;
#pragma unroll
      for (int j = 0; j < 4; ++j) V[n][j] = ldstr2(ltr + region * RG + j * 8192, ltr + region * RG + j * 8192 + 1024);
    };
    w4_unit(3, 0, w); w4_unit(4, 1, w);
    w4_read(std::integral_constant<int, 0>{}, 3); w4_read(std::integral_constant<int, 1>{}, 2); w4_read(std::integral_constant<int, 2>{}, 4);
    bar();
    w4_unit(5, 3, w); w4_unit(6, 2, w);
    if (w == 0) poll_flags(ws, WS_F3 + (t64 >> 1) * 16 * 256, 14, tag, lane);
    waitbar<0>();
    STAMP(10);
    u32x4 Yr[2];
    {
      const u32 so = WS_Y + (((kg & 1) * 64 + t64 * 2 + (kg >> 1)) * 8 + et64) * 4096 + eh * 2048;
      bload_sc1<0>(Yr[0], lane * 16, rW, so); bload_sc1<1024>(Yr[1], lane * 16, rW, so);
    }
    w4_read(std::integral_constant<int, 3>{}, 0); w4_read(std::integral_constant<int, 4>{}, 1);
    w4_read(std::integral_constant<int, 5>{}, 3); w4_read(std::integral_constant<int, 6>{}, 2);
    bar();
    auto issue_hh = [&](int n) {
      dma4_sc1(L0 + (n % 5) * RG + w * 4096, lane * 16, rW, WS_HH + ((t64 * 2 + (w >> 2)) * 112 + c4(n) * 16 + (w & 3) * 4) * 1024);
    };
    issue_hh(0); issue_hh(1); issue_hh(2); issue_hh(3); issue_hh(4);
#pragma unroll
    for (int r = 0; r < 16; ++r) { acc[0][r] = 0.f; acc[1][r] = 0.f; }
    {
      asm volatile("s_waitcnt vmcnt(20)" : "+v"(Yr[0]), "+v"(Yr[1]));
      const bool on = (l31 >> 4) == hh;
      const u32 ii = l31 & 7;
      const u32 val = on ? ((ii & 1) ? 0x3f800000u : 0x00003f80u) : 0u;
      u32x4 I0, I1;
#pragma unroll
      for (int c = 0; c < 4; ++c) {
        const u32 z = ((ii >> 1) == (u32)c) ? val : 0u;
        I0[c] = (l31 & 8) ? 0u : z; I1[c] = (l31 & 8) ? z : 0u;
      }
      if (kg >> 1) { acc[1] = mfma(I0, Yr[0], acc[1]); acc[1] = mfma(I1, Yr[1], acc[1]); }
      else         { acc[0] = mfma(I0, Yr[0], acc[0]); acc[0] = mfma(I1, Yr[1], acc[0]); }
    }
    sfor<0, 7>([&](auto nc) {
      constexpr int n = nc;
      constexpr int NW[7] = {16, 12, 12, 12, 8, 4, 0};
      waitbar<NW[n]>();
      if constexpr (n == 1) issue_hh(5);
      if constexpr (n == 2) issue_hh(6);
      const u32 lb = L0 + (n % 5) * RG + kg * 1024 + lane * 16;
#pragma unroll
      for (int j = 0; j < 4; ++j) {
        acc[0] = mfma(V[n][j], lds128(lb + j * 4096), acc[0]);
        acc[1] = mfma(V[n][j], lds128(lb + 16384 + j * 4096), acc[1]);
      }
    });
    STAMP(11);
    const u32 lp = L0 + 2 * RG + kg * (64 * 272);
#pragma unroll
    for (int T = 0; T < 2; ++T)
#pragma unroll
      for (int g = 0; g < 4; ++g) {
        f32x4 v = {acc[T][4 * g], acc[T][4 * g + 1], acc[T][4 * g + 2], acc[T][4 * g + 3]};
        const u32 e = 32 * (g >> 1) + 16 * eh + 8 * (g & 1) + 4 * hh;
        lds128w(lp + (T * 32 + l31) * 272 + e * 4, __builtin_bit_cast(u32x4, v));
      }
    bar();
    const u32 tr = tid >> 3, c8 = tid & 7;
    f32x4 s0v = {0.f, 0.f, 0.f, 0.f}, s1v = {0.f, 0.f, 0.f, 0.f};
#pragma unroll
    for (int k = 0; k < 4; ++k) {
      const u32 a = L0 + 2 * RG + k * (64 * 272) + tr * 272 + c8 * 32;
      s0v += __builtin_bit_cast(f32x4, lds128(a)); s1v += __builtin_bit_cast(f32x4, lds128(a + 16));
    }
    u32x4 o = {cvtpk(s0v[0], s0v[1]), cvtpk(s0v[2], s0v[3]), cvtpk(s1v[0], s1v[1]), cvtpk(s1v[2], s1v[3])};
    const rsrc_t rO = __builtin_amdgcn_make_buffer_rsrc(out_, 0, 2048 * 512 * 2, 0x00020000);
    __builtin_amdgcn_raw_buffer_store_b128(o, rO, (t64 * 64 + tr) * 1024 + et64 * 128 + c8 * 16, 0, 0);
    STAMP(12);
  }
}

extern "C" void kernel_launch(void* const* d_in, const int* in_sizes, int n_in, void* d_out, int out_size,
                              void* d_ws, size_t ws_size, hipStream_t stream) {
  (void)in_sizes; (void)n_in; (void)out_size; (void)ws_size;
  (void)hipFuncSetAttribute((const void*)fused_kernel, hipFuncAttributeMaxDynamicSharedMemorySize, LDS_BYTES);
  fused_kernel<<<256, 512, LDS_BYTES, stream>>>(d_in[0], d_in[1], d_in[2], d_in[3], d_in[4], d_in[6], d_in[7], d_in[8], d_out, (char*)d_ws);
}
```

```cpp
#include <hip/hip_runtime.h>
#include <stdint.h>

constexpr unsigned LDS_BYTES = 163840;

__global__ __launch_bounds__(512, 2) void fused_kernel(const void* x, const void* wsre, const void* wsim, const void* wdre, const void* wdim,
                                                      const void* wg, const void* wu, const void* wdn, void* out, void* ws) {
  unsigned tid = threadIdx.x;
  asm volatile(R"ASM(
.Lfk_start:
s_load_dwordx16 s[4:19], s[0:1], 0x0
s_load_dwordx4 s[20:23], s[0:1], 0x40
v_readfirstlane_b32 s62, v0
v_and_b32 v0, 63, v0
s_and_b32 s63, s2, 7
s_lshr_b32 s64, s2, 3
s_and_b32 s65, s2, 1
s_lshr_b32 s62, s62, 6
s_lshr_b32 s96, s63, 1
s_lshl_b32 s96, s96, 3
s_and_b32 s97, s64, 7
s_add_u32 s66, s96, s97
s_lshr_b32 s67, s64, 3
v_lshlrev_b32 v1, 4, v0
s_add_u32 s96, s66, s67
s_lshl_b32 s78, s96, 8
s_and_b32 s70, s78, 0x300
s_add_u32 s96, s78, 0x100
s_and_b32 s71, s96, 0x300
s_add_u32 s96, s78, 0x200
s_and_b32 s72, s96, 0x300
s_add_u32 s96, s78, 0x300
s_and_b32 s73, s96, 0x300
s_cmp_eq_u32 s62, 7
s_cbranch_scc0 .Lfk_else1
s_getpc_b64 s[96:97]
.Lfk_pc:
s_and_b32 s97, s97, 0xffff
s_mov_b32 s98, .Lfk_end-.Lfk_pc
s_mov_b32 s99, 0x20000
s_and_b32 s100, s64, 7
s_lshl_b32 s100, s100, 13
v_lshlrev_b32 v12, 7, v0
buffer_load_dword v111, v12, s[96:99], s100 offen
.Lfk_else1:
s_cmp_lt_u32 s62, 4
s_cbranch_scc0 .Lfk_else2
s_setprio 3
s_lshl_b32 s96, s66, 6
s_lshl_b32 s97, s62, 3
s_add_u32 s96, s96, s97
v_lshrrev_b32 v12, 3, v0
v_add_u32 v12, s96, v12
v_lshlrev_b32 v12, 10, v12
v_and_b32 v13, 0x70, v1
v_add_u32 v4, v12, v13
v_add_u32 v5, 0x8000, v4
s_waitcnt lgkmcnt(0)
s_mov_b32 s24, s4
s_and_b32 s25, s5, 0xffff
s_mov_b32 s26, 0x200000
s_mov_b32 s27, 0x20000
buffer_load_dwordx4 v[32:35], v4, s[24:27], s70 offen
buffer_load_dwordx4 v[36:39], v4, s[24:27], s70 offen offset:128
buffer_load_dwordx4 v[40:43], v4, s[24:27], s71 offen
buffer_load_dwordx4 v[44:47], v4, s[24:27], s71 offen offset:128
buffer_load_dwordx4 v[48:51], v4, s[24:27], s72 offen
buffer_load_dwordx4 v[52:55], v4, s[24:27], s72 offen offset:128
buffer_load_dwordx4 v[56:59], v4, s[24:27], s73 offen
buffer_load_dwordx4 v[60:63], v4, s[24:27], s73 offen offset:128
buffer_load_dwordx4 v[64:67], v5, s[24:27], s70 offen
buffer_load_dwordx4 v[68:71], v5, s[24:27], s70 offen offset:128
buffer_load_dwordx4 v[72:75], v5, s[24:27], s71 offen
buffer_load_dwordx4 v[76:79], v5, s[24:27], s71 offen offset:128
buffer_load_dwordx4 v[80:83], v5, s[24:27], s72 offen
buffer_load_dwordx4 v[84:87], v5, s[24:27], s72 offen offset:128
buffer_load_dwordx4 v[88:91], v5, s[24:27], s73 offen
buffer_load_dwordx4 v[92:95], v5, s[24:27], s73 offen offset:128
s_mov_b32 s36, s22
s_and_b32 s37, s23, 0xffff
s_mov_b32 s38, 0x1e00000
s_mov_b32 s39, 0x20000
buffer_load_dwordx2 v[10:11], off, s[36:39], 0 sc1
v_lshrrev_b32 v12, 4, v0
v_lshlrev_b32 v12, 6, v12
v_and_b32 v13, 48, v1
v_add_u32 v5, v12, v13
s_and_b32 s68, s2, 3
s_lshr_b32 s96, s63, 2
s_lshl_b32 s96, s96, 3
s_and_b32 s97, s64, 7
s_add_u32 s69, s96, s97
s_cmp_eq_u32 s65, 1
s_cselect_b32 s28, s8, s6
s_cselect_b32 s29, s9, s7
s_and_b32 s29, s29, 0xffff
s_mov_b32 s30, 0x800000
s_mov_b32 s31, 0x20000
s_lshl_b32 s96, s69, 7
s_and_b32 s97, s62, 3
s_lshl_b32 s97, s97, 5
s_add_u32 s96, s96, s97
v_and_b32 v12, 12, v0
v_add_u32 v12, s96, v12
v_lshlrev_b32 v12, 12, v12
v_add_u32 v6, v12, v5
s_and_b32 s80, s67, 2
s_mov_b32 s56, 0x55555555
s_mov_b32 s57, 0x55555555
s_mov_b32 s58, 0x33333333
s_mov_b32 s59, 0x33333333
s_mov_b32 s40, s14
s_and_b32 s41, s15, 0xffff
s_mov_b32 s42, 0x1c0000
s_mov_b32 s43, 0x20000
s_mov_b32 s52, s16
s_and_b32 s53, s17, 0xffff
s_mov_b32 s54, 0x1c0000
s_mov_b32 s55, 0x20000
s_mov_b32 s44, s18
s_and_b32 s45, s19, 0xffff
s_mov_b32 s46, 0x1c0000
s_mov_b32 s47, 0x20000
s_mov_b32 s48, s20
s_and_b32 s49, s21, 0xffff
s_mov_b32 s50, 0x200000
s_mov_b32 s51, 0x20000
v_lshlrev_b32 v2, 3, v0
v_and_b32 v3, 0x78, v2
s_add_u32 s83, s69, 0
s_lshl_b32 s96, s68, 2
s_add_u32 s82, s96, s67
s_cmp_eq_u32 s62, 7
s_cselect_b32 s78, 0x10000000, 0
v_add_u32 v20, s78, v1
s_lshl_b32 s89, s62, 10
s_and_b32 s96, s62, 3
s_lshl_b32 s96, s96, 1
s_lshr_b32 s97, s62, 2
s_add_u32 s88, s96, s97
s_and_b32 s96, s68, 1
s_lshl_b32 s96, s96, 1
s_lshr_b32 s97, s68, 1
s_add_u32 s96, s96, s97
s_lshl_b32 s96, s96, 2
s_add_u32 s96, s96, s67
s_lshl_b32 s96, s96, 18
s_add_u32 s96, 0x100000, s96
s_xor_b32 s97, s62, s80
s_lshl_b32 s97, s97, 12
s_add_u32 s74, s96, s97
s_lshl_b32 s75, s62, 12
s_lshl_b32 s96, s69, 2
s_add_u32 s96, s96, s67
s_lshl_b32 s96, s96, 5
s_add_u32 s96, s96, s88
s_lshl_b32 s96, s96, 12
s_add_u32 s76, 0x1000000, s96
s_lshl_b32 s96, s69, 5
s_add_u32 s96, s96, s88
s_lshl_b32 s96, s96, 12
s_add_u32 s96, 0x500000, s96
s_lshl_b32 s97, s67, 10
s_add_u32 s96, s96, s97
s_lshl_b32 s97, s68, 8
s_add_u32 s77, s96, s97
s_lshl_b32 s78, s68, 4
s_lshl_b64 s[86:87], 0xffff, s78
s_not_b64 s[86:87], s[86:87]
s_lshr_b32 s96, s62, 2
s_lshl_b32 s96, s96, 14
v_add_u32 v21, s96, v1
v_add_u32 v22, 0x10000, v21
s_lshl_b32 s96, s62, 10
v_add_u32 v12, s96, v1
v_add_u32 v23, 0x20000, v12
v_xor_b32 v24, 0x1000, v23
v_lshrrev_b32 v12, 4, v0
v_lshlrev_b32 v12, 10, v12
v_and_b32 v13, 15, v0
v_lshlrev_b32 v13, 4, v13
v_add_u32 v12, v12, v13
s_lshl_b32 s96, s68, 8
v_add_u32 v25, s96, v12
s_lshl_b32 s96, s68, 10
v_add_u32 v27, s96, v1
v_and_b32 v12, 15, v0
v_lshlrev_b32 v101, 4, v12
v_min_u32 v101, 0xd0, v101
v_lshrrev_b32 v12, 4, v0
v_mul_u32_u24 v12, 0xe00, v12
v_add_u32 v100, v12, v101
s_lshl_b32 s96, s83, 5
s_lshl_b32 s97, s62, 3
s_add_u32 s96, s96, s97
s_mul_i32 s96, s96, 0xe00
s_mul_i32 s97, s82, 0xe0
s_add_u32 s78, s96, s97
s_add_u32 s79, s78, 14336
buffer_load_dwordx4 v[240:243], v100, s[40:43], s78 offen
buffer_load_dwordx4 v[244:247], v100, s[40:43], s79 offen
buffer_load_dwordx4 v[248:251], v100, s[52:55], s78 offen
buffer_load_dwordx4 v[252:255], v100, s[52:55], s79 offen
v_lshrrev_b32 v12, 5, v0
v_lshlrev_b32 v12, 12, v12
s_lshl_b32 s96, s62, 2
v_lshrrev_b32 v13, 3, v0
v_and_b32 v13, 3, v13
v_add_u32 v13, s96, v13
v_lshlrev_b32 v13, 8, v13
v_add_u32 v12, v12, v13
v_and_b32 v13, 7, v0
v_lshrrev_b32 v14, 3, v0
v_and_b32 v14, 3, v14
v_lshlrev_b32 v14, 2, v14
v_add_u32 v14, s62, v14
v_xor_b32 v13, v13, v14
v_lshlrev_b32 v13, 4, v13
v_add_u32 v7, v12, v13
v_xor_b32 v8, 0x80, v7
s_lshl_b32 s79, s65, 31
v_mov_b32 v96, 0
v_mov_b32 v97, 0
v_mov_b32 v98, 0
v_mov_b32 v99, 0
s_mov_b32 s78, 0x3f803f80
s_waitcnt vmcnt(20)
v_dot2c_f32_bf16 v96, s78, v32
v_dot2c_f32_bf16 v98, v32, v32
v_dot2c_f32_bf16 v97, s78, v33
v_dot2c_f32_bf16 v99, v33, v33
v_dot2c_f32_bf16 v96, s78, v34
v_dot2c_f32_bf16 v98, v34, v34
v_dot2c_f32_bf16 v97, s78, v35
v_dot2c_f32_bf16 v99, v35, v35
s_waitcnt vmcnt(19)
v_dot2c_f32_bf16 v96, s78, v36
v_dot2c_f32_bf16 v98, v36, v36
v_dot2c_f32_bf16 v97, s78, v37
v_dot2c_f32_bf16 v99, v37, v37
v_dot2c_f32_bf16 v96, s78, v38
v_dot2c_f32_bf16 v98, v38, v38
v_dot2c_f32_bf16 v97, s78, v39
v_dot2c_f32_bf16 v99, v39, v39
s_waitcnt vmcnt(18)
v_dot2c_f32_bf16 v96, s78, v40
v_dot2c_f32_bf16 v98, v40, v40
v_dot2c_f32_bf16 v97, s78, v41
v_dot2c_f32_bf16 v99, v41, v41
v_dot2c_f32_bf16 v96, s78, v42
v_dot2c_f32_bf16 v98, v42, v42
v_dot2c_f32_bf16 v97, s78, v43
v_dot2c_f32_bf16 v99, v43, v43
s_waitcnt vmcnt(17)
v_dot2c_f32_bf16 v96, s78, v44
v_dot2c_f32_bf16 v98, v44, v44
v_dot2c_f32_bf16 v97, s78, v45
v_dot2c_f32_bf16 v99, v45, v45
v_dot2c_f32_bf16 v96, s78, v46
v_dot2c_f32_bf16 v98, v46, v46
v_dot2c_f32_bf16 v97, s78, v47
v_dot2c_f32_bf16 v99, v47, v47
s_waitcnt vmcnt(16)
v_dot2c_f32_bf16 v96, s78, v48
v_dot2c_f32_bf16 v98, v48, v48
v_dot2c_f32_bf16 v97, s78, v49
v_dot2c_f32_bf16 v99, v49, v49
v_dot2c_f32_bf16 v96, s78, v50
v_dot2c_f32_bf16 v98, v50, v50
v_dot2c_f32_bf16 v97, s78, v51
v_dot2c_f32_bf16 v99, v51, v51
s_waitcnt vmcnt(15)
v_dot2c_f32_bf16 v96, s78, v52
v_dot2c_f32_bf16 v98, v52, v52
v_dot2c_f32_bf16 v97, s78, v53
v_dot2c_f32_bf16 v99, v53, v53
v_dot2c_f32_bf16 v96, s78, v54
v_dot2c_f32_bf16 v98, v54, v54
v_dot2c_f32_bf16 v97, s78, v55
v_dot2c_f32_bf16 v99, v55, v55
s_waitcnt vmcnt(14)
v_dot2c_f32_bf16 v96, s78, v56
v_dot2c_f32_bf16 v98, v56, v56
v_dot2c_f32_bf16 v97, s78, v57
v_dot2c_f32_bf16 v99, v57, v57
v_dot2c_f32_bf16 v96, s78, v58
v_dot2c_f32_bf16 v98, v58, v58
v_dot2c_f32_bf16 v97, s78, v59
v_dot2c_f32_bf16 v99, v59, v59
s_waitcnt vmcnt(13)
v_dot2c_f32_bf16 v96, s78, v60
v_dot2c_f32_bf16 v98, v60, v60
v_dot2c_f32_bf16 v97, s78, v61
v_dot2c_f32_bf16 v99, v61, v61
v_dot2c_f32_bf16 v96, s78, v62
v_dot2c_f32_bf16 v98, v62, v62
v_dot2c_f32_bf16 v97, s78, v63
v_dot2c_f32_bf16 v99, v63, v63
s_nop 1
v_add_f32 v96, v96, v97
v_add_f32 v98, v98, v99
s_nop 0
v_add_f32_dpp v96, v96, v96 quad_perm:[1,0,3,2] row_mask:0xf bank_mask:0xf
v_add_f32_dpp v98, v98, v98 quad_perm:[1,0,3,2] row_mask:0xf bank_mask:0xf
s_nop 0
v_add_f32_dpp v96, v96, v96 quad_perm:[2,3,0,1] row_mask:0xf bank_mask:0xf
v_add_f32_dpp v98, v98, v98 quad_perm:[2,3,0,1] row_mask:0xf bank_mask:0xf
s_nop 0
v_add_f32_dpp v96, v96, v96 row_half_mirror row_mask:0xf bank_mask:0xf
v_add_f32_dpp v98, v98, v98 row_half_mirror row_mask:0xf bank_mask:0xf
v_mul_f32 v96, 0x3b000000, v96
v_mul_f32 v98, 0x3b000000, v98
v_fma_f32 v98, -v96, v96, v98
v_max_f32 v98, 0, v98
v_add_f32 v98, 0x3727c5ac, v98
v_rsq_f32 v98, v98
s_mov_b32 s78, 0xffff0000
v_xor_b32 v98, s79, v98
v_mul_f32 v96, v98, -v96
v_lshlrev_b32 v100, 16, v32
v_and_b32 v101, s78, v32
v_lshlrev_b32 v102, 16, v33
v_and_b32 v103, s78, v33
v_lshlrev_b32 v104, 16, v34
v_and_b32 v105, s78, v34
v_lshlrev_b32 v106, 16, v35
v_and_b32 v107, s78, v35
v_pk_fma_f32 v[100:101], v[100:101], v[98:99], v[96:97] op_sel_hi:[1,0,0]
v_pk_fma_f32 v[102:103], v[102:103], v[98:99], v[96:97] op_sel_hi:[1,0,0]
v_pk_fma_f32 v[104:105], v[104:105], v[98:99], v[96:97] op_sel_hi:[1,0,0]
v_pk_fma_f32 v[106:107], v[106:107], v[98:99], v[96:97] op_sel_hi:[1,0,0]
v_cvt_pk_bf16_f32 v32, v100, v101
v_cvt_pk_bf16_f32 v33, v102, v103
v_cvt_pk_bf16_f32 v34, v104, v105
v_cvt_pk_bf16_f32 v35, v106, v107
ds_write_b128 v7, v[32:35]
v_lshlrev_b32 v100, 16, v36
v_and_b32 v101, s78, v36
v_lshlrev_b32 v102, 16, v37
v_and_b32 v103, s78, v37
v_lshlrev_b32 v104, 16, v38
v_and_b32 v105, s78, v38
v_lshlrev_b32 v106, 16, v39
v_and_b32 v107, s78, v39
v_pk_fma_f32 v[100:101], v[100:101], v[98:99], v[96:97] op_sel_hi:[1,0,0]
v_pk_fma_f32 v[102:103], v[102:103], v[98:99], v[96:97] op_sel_hi:[1,0,0]
v_pk_fma_f32 v[104:105], v[104:105], v[98:99], v[96:97] op_sel_hi:[1,0,0]
v_pk_fma_f32 v[106:107], v[106:107], v[98:99], v[96:97] op_sel_hi:[1,0,0]
v_cvt_pk_bf16_f32 v36, v100, v101
v_cvt_pk_bf16_f32 v37, v102, v103
v_cvt_pk_bf16_f32 v38, v104, v105
v_cvt_pk_bf16_f32 v39, v106, v107
ds_write_b128 v8, v[36:39]
v_lshlrev_b32 v100, 16, v40
v_and_b32 v101, s78, v40
v_lshlrev_b32 v102, 16, v41
v_and_b32 v103, s78, v41
v_lshlrev_b32 v104, 16, v42
v_and_b32 v105, s78, v42
v_lshlrev_b32 v106, 16, v43
v_and_b32 v107, s78, v43
v_pk_fma_f32 v[100:101], v[100:101], v[98:99], v[96:97] op_sel_hi:[1,0,0]
v_pk_fma_f32 v[102:103], v[102:103], v[98:99], v[96:97] op_sel_hi:[1,0,0]
v_pk_fma_f32 v[104:105], v[104:105], v[98:99], v[96:97] op_sel_hi:[1,0,0]
v_pk_fma_f32 v[106:107], v[106:107], v[98:99], v[96:97] op_sel_hi:[1,0,0]
v_cvt_pk_bf16_f32 v40, v100, v101
v_cvt_pk_bf16_f32 v41, v102, v103
v_cvt_pk_bf16_f32 v42, v104, v105
v_cvt_pk_bf16_f32 v43, v106, v107
ds_write_b128 v7, v[40:43] offset:16384
v_lshlrev_b32 v100, 16, v44
v_and_b32 v101, s78, v44
v_lshlrev_b32 v102, 16, v45
v_and_b32 v103, s78, v45
v_lshlrev_b32 v104, 16, v46
v_and_b32 v105, s78, v46
v_lshlrev_b32 v106, 16, v47
v_and_b32 v107, s78, v47
v_pk_fma_f32 v[100:101], v[100:101], v[98:99], v[96:97] op_sel_hi:[1,0,0]
v_pk_fma_f32 v[102:103], v[102:103], v[98:99], v[96:97] op_sel_hi:[1,0,0]
v_pk_fma_f32 v[104:105], v[104:105], v[98:99], v[96:97] op_sel_hi:[1,0,0]
v_pk_fma_f32 v[106:107], v[106:107], v[98:99], v[96:97] op_sel_hi:[1,0,0]
v_cvt_pk_bf16_f32 v44, v100, v101
v_cvt_pk_bf16_f32 v45, v102, v103
v_cvt_pk_bf16_f32 v46, v104, v105
v_cvt_pk_bf16_f32 v47, v106, v107
ds_write_b128 v8, v[44:47] offset:16384
v_lshlrev_b32 v100, 16, v48
v_and_b32 v101, s78, v48
v_lshlrev_b32 v102, 16, v49
v_and_b32 v103, s78, v49
v_lshlrev_b32 v104, 16, v50
v_and_b32 v105, s78, v50
v_lshlrev_b32 v106, 16, v51
v_and_b32 v107, s78, v51
v_pk_fma_f32 v[100:101], v[100:101], v[98:99], v[96:97] op_sel_hi:[1,0,0]
v_pk_fma_f32 v[102:103], v[102:103], v[98:99], v[96:97] op_sel_hi:[1,0,0]
v_pk_fma_f32 v[104:105], v[104:105], v[98:99], v[96:97] op_sel_hi:[1,0,0]
v_pk_fma_f32 v[106:107], v[106:107], v[98:99], v[96:97] op_sel_hi:[1,0,0]
v_cvt_pk_bf16_f32 v48, v100, v101
v_cvt_pk_bf16_f32 v49, v102, v103
v_cvt_pk_bf16_f32 v50, v104, v105
v_cvt_pk_bf16_f32 v51, v106, v107
ds_write_b128 v7, v[48:51] offset:32768
v_lshlrev_b32 v100, 16, v52
v_and_b32 v101, s78, v52
v_lshlrev_b32 v102, 16, v53
v_and_b32 v103, s78, v53
v_lshlrev_b32 v104, 16, v54
v_and_b32 v105, s78, v54
v_lshlrev_b32 v106, 16, v55
v_and_b32 v107, s78, v55
v_pk_fma_f32 v[100:101], v[100:101], v[98:99], v[96:97] op_sel_hi:[1,0,0]
v_pk_fma_f32 v[102:103], v[102:103], v[98:99], v[96:97] op_sel_hi:[1,0,0]
v_pk_fma_f32 v[104:105], v[104:105], v[98:99], v[96:97] op_sel_hi:[1,0,0]
v_pk_fma_f32 v[106:107], v[106:107], v[98:99], v[96:97] op_sel_hi:[1,0,0]
v_cvt_pk_bf16_f32 v52, v100, v101
v_cvt_pk_bf16_f32 v53, v102, v103
v_cvt_pk_bf16_f32 v54, v104, v105
v_cvt_pk_bf16_f32 v55, v106, v107
ds_write_b128 v8, v[52:55] offset:32768
v_lshlrev_b32 v100, 16, v56
v_and_b32 v101, s78, v56
v_lshlrev_b32 v102, 16, v57
v_and_b32 v103, s78, v57
v_lshlrev_b32 v104, 16, v58
v_and_b32 v105, s78, v58
v_lshlrev_b32 v106, 16, v59
v_and_b32 v107, s78, v59
v_pk_fma_f32 v[100:101], v[100:101], v[98:99], v[96:97] op_sel_hi:[1,0,0]
v_pk_fma_f32 v[102:103], v[102:103], v[98:99], v[96:97] op_sel_hi:[1,0,0]
v_pk_fma_f32 v[104:105], v[104:105], v[98:99], v[96:97] op_sel_hi:[1,0,0]
v_pk_fma_f32 v[106:107], v[106:107], v[98:99], v[96:97] op_sel_hi:[1,0,0]
v_cvt_pk_bf16_f32 v56, v100, v101
v_cvt_pk_bf16_f32 v57, v102, v103
v_cvt_pk_bf16_f32 v58, v104, v105
v_cvt_pk_bf16_f32 v59, v106, v107
ds_write_b128 v7, v[56:59] offset:49152
v_lshlrev_b32 v100, 16, v60
v_and_b32 v101, s78, v60
v_lshlrev_b32 v102, 16, v61
v_and_b32 v103, s78, v61
v_lshlrev_b32 v104, 16, v62
v_and_b32 v105, s78, v62
v_lshlrev_b32 v106, 16, v63
v_and_b32 v107, s78, v63
v_pk_fma_f32 v[100:101], v[100:101], v[98:99], v[96:97] op_sel_hi:[1,0,0]
v_pk_fma_f32 v[102:103], v[102:103], v[98:99], v[96:97] op_sel_hi:[1,0,0]
v_pk_fma_f32 v[104:105], v[104:105], v[98:99], v[96:97] op_sel_hi:[1,0,0]
v_pk_fma_f32 v[106:107], v[106:107], v[98:99], v[96:97] op_sel_hi:[1,0,0]
v_cvt_pk_bf16_f32 v60, v100, v101
v_cvt_pk_bf16_f32 v61, v102, v103
v_cvt_pk_bf16_f32 v62, v104, v105
v_cvt_pk_bf16_f32 v63, v106, v107
ds_write_b128 v8, v[60:63] offset:49152
v_mov_b32 v96, 0
v_mov_b32 v97, 0
v_mov_b32 v98, 0
v_mov_b32 v99, 0
s_mov_b32 s78, 0x3f803f80
s_waitcnt vmcnt(12)
v_dot2c_f32_bf16 v96, s78, v64
v_dot2c_f32_bf16 v98, v64, v64
v_dot2c_f32_bf16 v97, s78, v65
v_dot2c_f32_bf16 v99, v65, v65
v_dot2c_f32_bf16 v96, s78, v66
v_dot2c_f32_bf16 v98, v66, v66
v_dot2c_f32_bf16 v97, s78, v67
v_dot2c_f32_bf16 v99, v67, v67
s_waitcnt vmcnt(11)
v_dot2c_f32_bf16 v96, s78, v68
v_dot2c_f32_bf16 v98, v68, v68
v_dot2c_f32_bf16 v97, s78, v69
v_dot2c_f32_bf16 v99, v69, v69
v_dot2c_f32_bf16 v96, s78, v70
v_dot2c_f32_bf16 v98, v70, v70
v_dot2c_f32_bf16 v97, s78, v71
v_dot2c_f32_bf16 v99, v71, v71
s_waitcnt vmcnt(10)
v_dot2c_f32_bf16 v96, s78, v72
v_dot2c_f32_bf16 v98, v72, v72
v_dot2c_f32_bf16 v97, s78, v73
v_dot2c_f32_bf16 v99, v73, v73
v_dot2c_f32_bf16 v96, s78, v74
v_dot2c_f32_bf16 v98, v74, v74
v_dot2c_f32_bf16 v97, s78, v75
v_dot2c_f32_bf16 v99, v75, v75
s_waitcnt vmcnt(9)
v_dot2c_f32_bf16 v96, s78, v76
v_dot2c_f32_bf16 v98, v76, v76
v_dot2c_f32_bf16 v97, s78, v77
v_dot2c_f32_bf16 v99, v77, v77
v_dot2c_f32_bf16 v96, s78, v78
v_dot2c_f32_bf16 v98, v78, v78
v_dot2c_f32_bf16 v97, s78, v79
v_dot2c_f32_bf16 v99, v79, v79
s_waitcnt vmcnt(8)
v_dot2c_f32_bf16 v96, s78, v80
v_dot2c_f32_bf16 v98, v80, v80
v_dot2c_f32_bf16 v97, s78, v81
v_dot2c_f32_bf16 v99, v81, v81
v_dot2c_f32_bf16 v96, s78, v82
v_dot2c_f32_bf16 v98, v82, v82
v_dot2c_f32_bf16 v97, s78, v83
v_dot2c_f32_bf16 v99, v83, v83
s_waitcnt vmcnt(7)
v_dot2c_f32_bf16 v96, s78, v84
v_dot2c_f32_bf16 v98, v84, v84
v_dot2c_f32_bf16 v97, s78, v85
v_dot2c_f32_bf16 v99, v85, v85
v_dot2c_f32_bf16 v96, s78, v86
v_dot2c_f32_bf16 v98, v86, v86
v_dot2c_f32_bf16 v97, s78, v87
v_dot2c_f32_bf16 v99, v87, v87
s_waitcnt vmcnt(6)
v_dot2c_f32_bf16 v96, s78, v88
v_dot2c_f32_bf16 v98, v88, v88
v_dot2c_f32_bf16 v97, s78, v89
v_dot2c_f32_bf16 v99, v89, v89
v_dot2c_f32_bf16 v96, s78, v90
v_dot2c_f32_bf16 v98, v90, v90
v_dot2c_f32_bf16 v97, s78, v91
v_dot2c_f32_bf16 v99, v91, v91
s_waitcnt vmcnt(5)
v_dot2c_f32_bf16 v96, s78, v92
v_dot2c_f32_bf16 v98, v92, v92
v_dot2c_f32_bf16 v97, s78, v93
v_dot2c_f32_bf16 v99, v93, v93
v_dot2c_f32_bf16 v96, s78, v94
v_dot2c_f32_bf16 v98, v94, v94
v_dot2c_f32_bf16 v97, s78, v95
v_dot2c_f32_bf16 v99, v95, v95
s_nop 1
v_add_f32 v96, v96, v97
v_add_f32 v98, v98, v99
s_nop 0
v_add_f32_dpp v96, v96, v96 quad_perm:[1,0,3,2] row_mask:0xf bank_mask:0xf
v_add_f32_dpp v98, v98, v98 quad_perm:[1,0,3,2] row_mask:0xf bank_mask:0xf
s_nop 0
v_add_f32_dpp v96, v96, v96 quad_perm:[2,3,0,1] row_mask:0xf bank_mask:0xf
v_add_f32_dpp v98, v98, v98 quad_perm:[2,3,0,1] row_mask:0xf bank_mask:0xf
s_nop 0
v_add_f32_dpp v96, v96, v96 row_half_mirror row_mask:0xf bank_mask:0xf
v_add_f32_dpp v98, v98, v98 row_half_mirror row_mask:0xf bank_mask:0xf
v_mul_f32 v96, 0x3b000000, v96
v_mul_f32 v98, 0x3b000000, v98
v_fma_f32 v98, -v96, v96, v98
v_max_f32 v98, 0, v98
v_add_f32 v98, 0x3727c5ac, v98
v_rsq_f32 v98, v98
s_mov_b32 s78, 0xffff0000
v_xor_b32 v98, s79, v98
v_mul_f32 v96, v98, -v96
v_lshlrev_b32 v100, 16, v64
v_and_b32 v101, s78, v64
v_lshlrev_b32 v102, 16, v65
v_and_b32 v103, s78, v65
v_lshlrev_b32 v104, 16, v66
v_and_b32 v105, s78, v66
v_lshlrev_b32 v106, 16, v67
v_and_b32 v107, s78, v67
v_pk_fma_f32 v[100:101], v[100:101], v[98:99], v[96:97] op_sel_hi:[1,0,0]
v_pk_fma_f32 v[102:103], v[102:103], v[98:99], v[96:97] op_sel_hi:[1,0,0]
v_pk_fma_f32 v[104:105], v[104:105], v[98:99], v[96:97] op_sel_hi:[1,0,0]
v_pk_fma_f32 v[106:107], v[106:107], v[98:99], v[96:97] op_sel_hi:[1,0,0]
v_cvt_pk_bf16_f32 v64, v100, v101
v_cvt_pk_bf16_f32 v65, v102, v103
v_cvt_pk_bf16_f32 v66, v104, v105
v_cvt_pk_bf16_f32 v67, v106, v107
ds_write_b128 v7, v[64:67] offset:8192
v_lshlrev_b32 v100, 16, v68
v_and_b32 v101, s78, v68
v_lshlrev_b32 v102, 16, v69
v_and_b32 v103, s78, v69
v_lshlrev_b32 v104, 16, v70
v_and_b32 v105, s78, v70
v_lshlrev_b32 v106, 16, v71
v_and_b32 v107, s78, v71
v_pk_fma_f32 v[100:101], v[100:101], v[98:99], v[96:97] op_sel_hi:[1,0,0]
v_pk_fma_f32 v[102:103], v[102:103], v[98:99], v[96:97] op_sel_hi:[1,0,0]
v_pk_fma_f32 v[104:105], v[104:105], v[98:99], v[96:97] op_sel_hi:[1,0,0]
v_pk_fma_f32 v[106:107], v[106:107], v[98:99], v[96:97] op_sel_hi:[1,0,0]
v_cvt_pk_bf16_f32 v68, v100, v101
v_cvt_pk_bf16_f32 v69, v102, v103
v_cvt_pk_bf16_f32 v70, v104, v105
v_cvt_pk_bf16_f32 v71, v106, v107
ds_write_b128 v8, v[68:71] offset:8192
v_lshlrev_b32 v100, 16, v72
v_and_b32 v101, s78, v72
v_lshlrev_b32 v102, 16, v73
v_and_b32 v103, s78, v73
v_lshlrev_b32 v104, 16, v74
v_and_b32 v105, s78, v74
v_lshlrev_b32 v106, 16, v75
v_and_b32 v107, s78, v75
v_pk_fma_f32 v[100:101], v[100:101], v[98:99], v[96:97] op_sel_hi:[1,0,0]
v_pk_fma_f32 v[102:103], v[102:103], v[98:99], v[96:97] op_sel_hi:[1,0,0]
v_pk_fma_f32 v[104:105], v[104:105], v[98:99], v[96:97] op_sel_hi:[1,0,0]
v_pk_fma_f32 v[106:107], v[106:107], v[98:99], v[96:97] op_sel_hi:[1,0,0]
v_cvt_pk_bf16_f32 v72, v100, v101
v_cvt_pk_bf16_f32 v73, v102, v103
v_cvt_pk_bf16_f32 v74, v104, v105
v_cvt_pk_bf16_f32 v75, v106, v107
ds_write_b128 v7, v[72:75] offset:24576
v_lshlrev_b32 v100, 16, v76
v_and_b32 v101, s78, v76
v_lshlrev_b32 v102, 16, v77
v_and_b32 v103, s78, v77
v_lshlrev_b32 v104, 16, v78
v_and_b32 v105, s78, v78
v_lshlrev_b32 v106, 16, v79
v_and_b32 v107, s78, v79
v_pk_fma_f32 v[100:101], v[100:101], v[98:99], v[96:97] op_sel_hi:[1,0,0]
v_pk_fma_f32 v[102:103], v[102:103], v[98:99], v[96:97] op_sel_hi:[1,0,0]
v_pk_fma_f32 v[104:105], v[104:105], v[98:99], v[96:97] op_sel_hi:[1,0,0]
v_pk_fma_f32 v[106:107], v[106:107], v[98:99], v[96:97] op_sel_hi:[1,0,0]
v_cvt_pk_bf16_f32 v76, v100, v101
v_cvt_pk_bf16_f32 v77, v102, v103
v_cvt_pk_bf16_f32 v78, v104, v105
v_cvt_pk_bf16_f32 v79, v106, v107
ds_write_b128 v8, v[76:79] offset:24576
v_lshlrev_b32 v100, 16, v80
v_and_b32 v101, s78, v80
v_lshlrev_b32 v102, 16, v81
v_and_b32 v103, s78, v81
v_lshlrev_b32 v104, 16, v82
v_and_b32 v105, s78, v82
v_lshlrev_b32 v106, 16, v83
v_and_b32 v107, s78, v83
v_pk_fma_f32 v[100:101], v[100:101], v[98:99], v[96:97] op_sel_hi:[1,0,0]
v_pk_fma_f32 v[102:103], v[102:103], v[98:99], v[96:97] op_sel_hi:[1,0,0]
v_pk_fma_f32 v[104:105], v[104:105], v[98:99], v[96:97] op_sel_hi:[1,0,0]
v_pk_fma_f32 v[106:107], v[106:107], v[98:99], v[96:97] op_sel_hi:[1,0,0]
v_cvt_pk_bf16_f32 v80, v100, v101
v_cvt_pk_bf16_f32 v81, v102, v103
v_cvt_pk_bf16_f32 v82, v104, v105
v_cvt_pk_bf16_f32 v83, v106, v107
ds_write_b128 v7, v[80:83] offset:40960
v_lshlrev_b32 v100, 16, v84
v_and_b32 v101, s78, v84
v_lshlrev_b32 v102, 16, v85
v_and_b32 v103, s78, v85
v_lshlrev_b32 v104, 16, v86
v_and_b32 v105, s78, v86
v_lshlrev_b32 v106, 16, v87
v_and_b32 v107, s78, v87
v_pk_fma_f32 v[100:101], v[100:101], v[98:99], v[96:97] op_sel_hi:[1,0,0]
v_pk_fma_f32 v[102:103], v[102:103], v[98:99], v[96:97] op_sel_hi:[1,0,0]
v_pk_fma_f32 v[104:105], v[104:105], v[98:99], v[96:97] op_sel_hi:[1,0,0]
v_pk_fma_f32 v[106:107], v[106:107], v[98:99], v[96:97] op_sel_hi:[1,0,0]
v_cvt_pk_bf16_f32 v84, v100, v101
v_cvt_pk_bf16_f32 v85, v102, v103
v_cvt_pk_bf16_f32 v86, v104, v105
v_cvt_pk_bf16_f32 v87, v106, v107
ds_write_b128 v8, v[84:87] offset:40960
v_lshlrev_b32 v100, 16, v88
v_and_b32 v101, s78, v88
v_lshlrev_b32 v102, 16, v89
v_and_b32 v103, s78, v89
v_lshlrev_b32 v104, 16, v90
v_and_b32 v105, s78, v90
v_lshlrev_b32 v106, 16, v91
v_and_b32 v107, s78, v91
v_pk_fma_f32 v[100:101], v[100:101], v[98:99], v[96:97] op_sel_hi:[1,0,0]
v_pk_fma_f32 v[102:103], v[102:103], v[98:99], v[96:97] op_sel_hi:[1,0,0]
v_pk_fma_f32 v[104:105], v[104:105], v[98:99], v[96:97] op_sel_hi:[1,0,0]
v_pk_fma_f32 v[106:107], v[106:107], v[98:99], v[96:97] op_sel_hi:[1,0,0]
v_cvt_pk_bf16_f32 v88, v100, v101
v_cvt_pk_bf16_f32 v89, v102, v103
v_cvt_pk_bf16_f32 v90, v104, v105
v_cvt_pk_bf16_f32 v91, v106, v107
ds_write_b128 v7, v[88:91] offset:57344
v_lshlrev_b32 v100, 16, v92
v_and_b32 v101, s78, v92
v_lshlrev_b32 v102, 16, v93
v_and_b32 v103, s78, v93
v_lshlrev_b32 v104, 16, v94
v_and_b32 v105, s78, v94
v_lshlrev_b32 v106, 16, v95
v_and_b32 v107, s78, v95
v_pk_fma_f32 v[100:101], v[100:101], v[98:99], v[96:97] op_sel_hi:[1,0,0]
v_pk_fma_f32 v[102:103], v[102:103], v[98:99], v[96:97] op_sel_hi:[1,0,0]
v_pk_fma_f32 v[104:105], v[104:105], v[98:99], v[96:97] op_sel_hi:[1,0,0]
v_pk_fma_f32 v[106:107], v[106:107], v[98:99], v[96:97] op_sel_hi:[1,0,0]
v_cvt_pk_bf16_f32 v92, v100, v101
v_cvt_pk_bf16_f32 v93, v102, v103
v_cvt_pk_bf16_f32 v94, v104, v105
v_cvt_pk_bf16_f32 v95, v106, v107
ds_write_b128 v8, v[92:95] offset:57344
s_waitcnt lgkmcnt(0)
s_barrier
s_setprio 0
s_lshr_b32 s96, s2, 5
s_lshl_b32 s78, s96, 5
s_cmp_ge_u32 s78, 96
s_cselect_b32 s79, 16, 0
s_add_u32 s78, s78, s79
s_cmp_ge_u32 s2, 192
s_cselect_b32 s78, 96, s78
s_cselect_b32 s79, 7, 1
s_lshl_b32 s79, s79, 14
v_lshrrev_b32 v12, 4, v0
v_lshlrev_b32 v12, 10, v12
v_lshrrev_b32 v13, 1, v0
v_lshrrev_b32 v14, 4, v0
s_lshl_b32 s96, s62, 2
v_add_u32 v14, s96, v14
v_xor_b32 v13, v13, v14
v_and_b32 v13, 7, v13
v_lshlrev_b32 v13, 5, v13
v_add_u32 v12, v12, v13
v_and_b32 v13, 1, v0
v_lshlrev_b32 v13, 4, v13
v_add_u32 v110, v12, v13
s_lshr_b32 s96, s2, 2
s_and_b32 s96, s96, 7
s_mul_i32 s96, s96, 0xe0
s_add_u32 s96, s96, s78
s_and_b32 s97, s62, 3
s_lshl_b32 s97, s97, 2
s_add_u32 s96, s96, s97
s_lshl_b32 s96, s96, 10
s_and_b32 s97, s2, 3
s_lshl_b32 s97, s97, 8
s_add_u32 s78, s96, s97
buffer_load_dwordx4 v[232:235], v110, s[44:47], s78 offen
s_add_u32 s78, s78, s79
buffer_load_dwordx4 v[236:239], v110, s[44:47], s78 offen
s_lshl_b32 s96, s69, 7
s_lshl_b32 s97, s88, 4
s_add_u32 s96, s96, s97
v_and_b32 v12, 15, v0
v_add_u32 v12, s96, v12
v_lshlrev_b32 v12, 10, v12
v_lshrrev_b32 v13, 4, v0
v_lshlrev_b32 v13, 4, v13
v_add_u32 v12, v12, v13
s_lshl_b32 s96, s67, 8
s_lshl_b32 s97, s68, 6
s_add_u32 s96, s96, s97
v_add_u32 v12, s96, v12
buffer_load_dwordx4 v[16:19], v12, s[24:27], 0 offen
s_lshr_b32 s96, s68, 1
s_lshl_b32 s96, s96, 11
s_and_b32 s97, s62, 4
s_xor_b32 s97, s97, s80
s_lshl_b32 s97, s97, 8
s_add_u32 s96, s96, s97
s_lshr_b32 s97, s62, 2
s_lshl_b32 s97, s97, 16
s_add_u32 s81, s96, s97
buffer_load_dwordx4 v[128:131], v6, s[28:31], s81 offen
s_add_u32 s81, s81, 0x1000
buffer_load_dwordx4 v[136:139], v6, s[28:31], s81 offen
s_add_u32 s81, s81, 0x1000
buffer_load_dwordx4 v[144:147], v6, s[28:31], s81 offen
s_add_u32 s81, s81, 0x1000
buffer_load_dwordx4 v[152:155], v6, s[28:31], s81 offen
s_lshr_b32 s96, s68, 1
s_lshl_b32 s96, s96, 11
s_and_b32 s97, s62, 4
s_xor_b32 s97, s97, s80
s_lshl_b32 s97, s97, 8
s_add_u32 s96, s96, s97
s_lshr_b32 s97, s62, 2
s_xor_b32 s97, s97, 1
s_lshl_b32 s97, s97, 16
s_add_u32 s81, s96, s97
buffer_load_dwordx4 v[132:135], v6, s[28:31], s81 offen
s_add_u32 s81, s81, 0x1000
buffer_load_dwordx4 v[140:143], v6, s[28:31], s81 offen
s_add_u32 s81, s81, 0x1000
buffer_load_dwordx4 v[148:151], v6, s[28:31], s81 offen
s_add_u32 s81, s81, 0x1000
buffer_load_dwordx4 v[156:159], v6, s[28:31], s81 offen
s_lshr_b32 s96, s68, 1
s_lshl_b32 s96, s96, 11
s_and_b32 s97, s62, 4
s_add_u32 s97, s97, 1
s_xor_b32 s97, s97, s80
s_lshl_b32 s97, s97, 8
s_add_u32 s96, s96, s97
s_lshr_b32 s97, s62, 2
s_lshl_b32 s97, s97, 16
s_add_u32 s81, s96, s97
buffer_load_dwordx4 v[160:163], v6, s[28:31], s81 offen
s_add_u32 s81, s81, 0x1000
buffer_load_dwordx4 v[168:171], v6, s[28:31], s81 offen
s_add_u32 s81, s81, 0x1000
buffer_load_dwordx4 v[176:179], v6, s[28:31], s81 offen
s_add_u32 s81, s81, 0x1000
buffer_load_dwordx4 v[184:187], v6, s[28:31], s81 offen
s_lshr_b32 s96, s68, 1
s_lshl_b32 s96, s96, 11
s_and_b32 s97, s62, 4
s_add_u32 s97, s97, 1
s_xor_b32 s97, s97, s80
s_lshl_b32 s97, s97, 8
s_add_u32 s96, s96, s97
s_lshr_b32 s97, s62, 2
s_xor_b32 s97, s97, 1
s_lshl_b32 s97, s97, 16
s_add_u32 s81, s96, s97
buffer_load_dwordx4 v[164:167], v6, s[28:31], s81 offen
s_add_u32 s81, s81, 0x1000
buffer_load_dwordx4 v[172:175], v6, s[28:31], s81 offen
s_add_u32 s81, s81, 0x1000
buffer_load_dwordx4 v[180:183], v6, s[28:31], s81 offen
s_add_u32 s81, s81, 0x1000
buffer_load_dwordx4 v[188:191], v6, s[28:31], s81 offen
s_waitcnt vmcnt(23)
v_readfirstlane_b32 s60, v10
v_readfirstlane_b32 s61, v11
s_add_u32 s60, s60, 1
s_addc_u32 s61, s61, 0
v_mov_b32 v10, s60
v_mov_b32 v11, s61
s_setprio 2
s_barrier
s_lshr_b32 s96, s68, 1
s_lshl_b32 s96, s96, 11
s_and_b32 s97, s62, 4
s_add_u32 s97, s97, 2
s_xor_b32 s97, s97, s80
s_lshl_b32 s97, s97, 8
s_add_u32 s96, s96, s97
s_lshr_b32 s97, s62, 2
s_lshl_b32 s97, s97, 16
s_add_u32 s81, s96, s97
buffer_load_dwordx4 v[192:195], v6, s[28:31], s81 offen
s_add_u32 s81, s81, 0x1000
buffer_load_dwordx4 v[200:203], v6, s[28:31], s81 offen
s_add_u32 s81, s81, 0x1000
buffer_load_dwordx4 v[208:211], v6, s[28:31], s81 offen
s_add_u32 s81, s81, 0x1000
buffer_load_dwordx4 v[216:219], v6, s[28:31], s81 offen
s_lshr_b32 s96, s68, 1
s_lshl_b32 s96, s96, 11
s_and_b32 s97, s62, 4
s_add_u32 s97, s97, 2
s_xor_b32 s97, s97, s80
s_lshl_b32 s97, s97, 8
s_add_u32 s96, s96, s97
s_lshr_b32 s97, s62, 2
s_xor_b32 s97, s97, 1
s_lshl_b32 s97, s97, 16
s_add_u32 s81, s96, s97
buffer_load_dwordx4 v[196:199], v6, s[28:31], s81 offen
s_add_u32 s81, s81, 0x1000
buffer_load_dwordx4 v[204:207], v6, s[28:31], s81 offen
s_add_u32 s81, s81, 0x1000
buffer_load_dwordx4 v[212:215], v6, s[28:31], s81 offen
s_add_u32 s81, s81, 0x1000
buffer_load_dwordx4 v[220:223], v6, s[28:31], s81 offen
s_setprio 0
s_lshl_b32 s96, s62, 10
s_add_u32 s96, s96, 0x26000
v_add_u32 v100, s96, v1
s_waitcnt vmcnt(26)
ds_write_b128 v100, v[232:235]
s_waitcnt vmcnt(25)
ds_write_b128 v100, v[236:239] offset:4096
v_lshrrev_b32 v12, 1, v0
v_and_b32 v12, 7, v12
v_lshlrev_b32 v12, 10, v12
v_lshrrev_b32 v13, 4, v0
v_lshlrev_b32 v13, 5, v13
v_add_u32 v12, v12, v13
v_and_b32 v13, 1, v0
v_lshlrev_b32 v13, 4, v13
v_add_u32 v12, v12, v13
s_lshl_b32 s96, s62, 7
s_add_u32 s96, s96, 0x22000
v_add_u32 v100, s96, v12
ds_write_b128 v100, v[240:243]
ds_write_b128 v100, v[244:247] offset:512
ds_write_b128 v100, v[248:251] offset:8192
ds_write_b128 v100, v[252:255] offset:8704
s_mov_b64 vcc, s[56:57]
s_waitcnt vmcnt(23)
v_mov_b32_dpp v12, v128 quad_perm:[1,0,3,2] row_mask:0xf bank_mask:0xf
s_waitcnt vmcnt(22)
v_cndmask_b32_dpp v128, v136, v128, vcc quad_perm:[1,0,3,2] row_mask:0xf bank_mask:0xf
v_cndmask_b32_e64 v136, v136, v12, s[56:57]
v_mov_b32_dpp v13, v129 quad_perm:[1,0,3,2] row_mask:0xf bank_mask:0xf
v_cndmask_b32_dpp v129, v137, v129, vcc quad_perm:[1,0,3,2] row_mask:0xf bank_mask:0xf
v_cndmask_b32_e64 v137, v137, v13, s[56:57]
v_mov_b32_dpp v12, v130 quad_perm:[1,0,3,2] row_mask:0xf bank_mask:0xf
v_cndmask_b32_dpp v130, v138, v130, vcc quad_perm:[1,0,3,2] row_mask:0xf bank_mask:0xf
v_cndmask_b32_e64 v138, v138, v12, s[56:57]
v_mov_b32_dpp v13, v131 quad_perm:[1,0,3,2] row_mask:0xf bank_mask:0xf
v_cndmask_b32_dpp v131, v139, v131, vcc quad_perm:[1,0,3,2] row_mask:0xf bank_mask:0xf
v_cndmask_b32_e64 v139, v139, v13, s[56:57]
s_waitcnt vmcnt(21)
v_mov_b32_dpp v12, v144 quad_perm:[1,0,3,2] row_mask:0xf bank_mask:0xf
s_waitcnt vmcnt(20)
v_cndmask_b32_dpp v144, v152, v144, vcc quad_perm:[1,0,3,2] row_mask:0xf bank_mask:0xf
v_cndmask_b32_e64 v152, v152, v12, s[56:57]
v_mov_b32_dpp v13, v145 quad_perm:[1,0,3,2] row_mask:0xf bank_mask:0xf
v_cndmask_b32_dpp v145, v153, v145, vcc quad_perm:[1,0,3,2] row_mask:0xf bank_mask:0xf
v_cndmask_b32_e64 v153, v153, v13, s[56:57]
v_mov_b32_dpp v12, v146 quad_perm:[1,0,3,2] row_mask:0xf bank_mask:0xf
v_cndmask_b32_dpp v146, v154, v146, vcc quad_perm:[1,0,3,2] row_mask:0xf bank_mask:0xf
v_cndmask_b32_e64 v154, v154, v12, s[56:57]
v_mov_b32_dpp v13, v147 quad_perm:[1,0,3,2] row_mask:0xf bank_mask:0xf
v_cndmask_b32_dpp v147, v155, v147, vcc quad_perm:[1,0,3,2] row_mask:0xf bank_mask:0xf
v_cndmask_b32_e64 v155, v155, v13, s[56:57]
s_mov_b64 vcc, s[58:59]
v_mov_b32_dpp v12, v128 quad_perm:[2,3,0,1] row_mask:0xf bank_mask:0xf
v_cndmask_b32_dpp v128, v144, v128, vcc quad_perm:[2,3,0,1] row_mask:0xf bank_mask:0xf
v_cndmask_b32_e64 v144, v144, v12, s[58:59]
v_mov_b32_dpp v13, v129 quad_perm:[2,3,0,1] row_mask:0xf bank_mask:0xf
v_cndmask_b32_dpp v129, v145, v129, vcc quad_perm:[2,3,0,1] row_mask:0xf bank_mask:0xf
v_cndmask_b32_e64 v145, v145, v13, s[58:59]
v_mov_b32_dpp v12, v130 quad_perm:[2,3,0,1] row_mask:0xf bank_mask:0xf
v_cndmask_b32_dpp v130, v146, v130, vcc quad_perm:[2,3,0,1] row_mask:0xf bank_mask:0xf
v_cndmask_b32_e64 v146, v146, v12, s[58:59]
v_mov_b32_dpp v13, v131 quad_perm:[2,3,0,1] row_mask:0xf bank_mask:0xf
v_cndmask_b32_dpp v131, v147, v131, vcc quad_perm:[2,3,0,1] row_mask:0xf bank_mask:0xf
v_cndmask_b32_e64 v147, v147, v13, s[58:59]
v_mov_b32_dpp v12, v136 quad_perm:[2,3,0,1] row_mask:0xf bank_mask:0xf
v_cndmask_b32_dpp v136, v152, v136, vcc quad_perm:[2,3,0,1] row_mask:0xf bank_mask:0xf
v_cndmask_b32_e64 v152, v152, v12, s[58:59]
v_mov_b32_dpp v13, v137 quad_perm:[2,3,0,1] row_mask:0xf bank_mask:0xf
v_cndmask_b32_dpp v137, v153, v137, vcc quad_perm:[2,3,0,1] row_mask:0xf bank_mask:0xf
v_cndmask_b32_e64 v153, v153, v13, s[58:59]
v_mov_b32_dpp v12, v138 quad_perm:[2,3,0,1] row_mask:0xf bank_mask:0xf
v_cndmask_b32_dpp v138, v154, v138, vcc quad_perm:[2,3,0,1] row_mask:0xf bank_mask:0xf
v_cndmask_b32_e64 v154, v154, v12, s[58:59]
v_mov_b32_dpp v13, v139 quad_perm:[2,3,0,1] row_mask:0xf bank_mask:0xf
v_cndmask_b32_dpp v139, v155, v139, vcc quad_perm:[2,3,0,1] row_mask:0xf bank_mask:0xf
v_cndmask_b32_e64 v155, v155, v13, s[58:59]
s_mov_b64 vcc, s[56:57]
s_waitcnt vmcnt(19)
v_mov_b32_dpp v12, v132 quad_perm:[1,0,3,2] row_mask:0xf bank_mask:0xf
s_waitcnt vmcnt(18)
v_cndmask_b32_dpp v132, v140, v132, vcc quad_perm:[1,0,3,2] row_mask:0xf bank_mask:0xf
v_cndmask_b32_e64 v140, v140, v12, s[56:57]
v_mov_b32_dpp v13, v133 quad_perm:[1,0,3,2] row_mask:0xf bank_mask:0xf
v_cndmask_b32_dpp v133, v141, v133, vcc quad_perm:[1,0,3,2] row_mask:0xf bank_mask:0xf
v_cndmask_b32_e64 v141, v141, v13, s[56:57]
v_mov_b32_dpp v12, v134 quad_perm:[1,0,3,2] row_mask:0xf bank_mask:0xf
v_cndmask_b32_dpp v134, v142, v134, vcc quad_perm:[1,0,3,2] row_mask:0xf bank_mask:0xf
v_cndmask_b32_e64 v142, v142, v12, s[56:57]
v_mov_b32_dpp v13, v135 quad_perm:[1,0,3,2] row_mask:0xf bank_mask:0xf
v_cndmask_b32_dpp v135, v143, v135, vcc quad_perm:[1,0,3,2] row_mask:0xf bank_mask:0xf
v_cndmask_b32_e64 v143, v143, v13, s[56:57]
s_waitcnt vmcnt(17)
v_mov_b32_dpp v12, v148 quad_perm:[1,0,3,2] row_mask:0xf bank_mask:0xf
s_waitcnt vmcnt(16)
v_cndmask_b32_dpp v148, v156, v148, vcc quad_perm:[1,0,3,2] row_mask:0xf bank_mask:0xf
v_cndmask_b32_e64 v156, v156, v12, s[56:57]
v_mov_b32_dpp v13, v149 quad_perm:[1,0,3,2] row_mask:0xf bank_mask:0xf
v_cndmask_b32_dpp v149, v157, v149, vcc quad_perm:[1,0,3,2] row_mask:0xf bank_mask:0xf
v_cndmask_b32_e64 v157, v157, v13, s[56:57]
v_mov_b32_dpp v12, v150 quad_perm:[1,0,3,2] row_mask:0xf bank_mask:0xf
v_cndmask_b32_dpp v150, v158, v150, vcc quad_perm:[1,0,3,2] row_mask:0xf bank_mask:0xf
v_cndmask_b32_e64 v158, v158, v12, s[56:57]
v_mov_b32_dpp v13, v151 quad_perm:[1,0,3,2] row_mask:0xf bank_mask:0xf
v_cndmask_b32_dpp v151, v159, v151, vcc quad_perm:[1,0,3,2] row_mask:0xf bank_mask:0xf
v_cndmask_b32_e64 v159, v159, v13, s[56:57]
s_mov_b64 vcc, s[58:59]
v_mov_b32_dpp v12, v132 quad_perm:[2,3,0,1] row_mask:0xf bank_mask:0xf
v_cndmask_b32_dpp v132, v148, v132, vcc quad_perm:[2,3,0,1] row_mask:0xf bank_mask:0xf
v_cndmask_b32_e64 v148, v148, v12, s[58:59]
v_mov_b32_dpp v13, v133 quad_perm:[2,3,0,1] row_mask:0xf bank_mask:0xf
v_cndmask_b32_dpp v133, v149, v133, vcc quad_perm:[2,3,0,1] row_mask:0xf bank_mask:0xf
v_cndmask_b32_e64 v149, v149, v13, s[58:59]
v_mov_b32_dpp v12, v134 quad_perm:[2,3,0,1] row_mask:0xf bank_mask:0xf
v_cndmask_b32_dpp v134, v150, v134, vcc quad_perm:[2,3,0,1] row_mask:0xf bank_mask:0xf
v_cndmask_b32_e64 v150, v150, v12, s[58:59]
v_mov_b32_dpp v13, v135 quad_perm:[2,3,0,1] row_mask:0xf bank_mask:0xf
v_cndmask_b32_dpp v135, v151, v135, vcc quad_perm:[2,3,0,1] row_mask:0xf bank_mask:0xf
v_cndmask_b32_e64 v151, v151, v13, s[58:59]
v_mov_b32_dpp v12, v140 quad_perm:[2,3,0,1] row_mask:0xf bank_mask:0xf
v_cndmask_b32_dpp v140, v156, v140, vcc quad_perm:[2,3,0,1] row_mask:0xf bank_mask:0xf
v_cndmask_b32_e64 v156, v156, v12, s[58:59]
v_mov_b32_dpp v13, v141 quad_perm:[2,3,0,1] row_mask:0xf bank_mask:0xf
v_cndmask_b32_dpp v141, v157, v141, vcc quad_perm:[2,3,0,1] row_mask:0xf bank_mask:0xf
v_cndmask_b32_e64 v157, v157, v13, s[58:59]
v_mov_b32_dpp v12, v142 quad_perm:[2,3,0,1] row_mask:0xf bank_mask:0xf
v_cndmask_b32_dpp v142, v158, v142, vcc quad_perm:[2,3,0,1] row_mask:0xf bank_mask:0xf
v_cndmask_b32_e64 v158, v158, v12, s[58:59]
v_mov_b32_dpp v13, v143 quad_perm:[2,3,0,1] row_mask:0xf bank_mask:0xf
v_cndmask_b32_dpp v143, v159, v143, vcc quad_perm:[2,3,0,1] row_mask:0xf bank_mask:0xf
v_cndmask_b32_e64 v159, v159, v13, s[58:59]
s_mov_b64 vcc, s[56:57]
s_waitcnt vmcnt(15)
v_mov_b32_dpp v12, v160 quad_perm:[1,0,3,2] row_mask:0xf bank_mask:0xf
s_waitcnt vmcnt(14)
v_cndmask_b32_dpp v160, v168, v160, vcc quad_perm:[1,0,3,2] row_mask:0xf bank_mask:0xf
v_cndmask_b32_e64 v168, v168, v12, s[56:57]
v_mov_b32_dpp v13, v161 quad_perm:[1,0,3,2] row_mask:0xf bank_mask:0xf
v_cndmask_b32_dpp v161, v169, v161, vcc quad_perm:[1,0,3,2] row_mask:0xf bank_mask:0xf
v_cndmask_b32_e64 v169, v169, v13, s[56:57]
v_mov_b32_dpp v12, v162 quad_perm:[1,0,3,2] row_mask:0xf bank_mask:0xf
v_cndmask_b32_dpp v162, v170, v162, vcc quad_perm:[1,0,3,2] row_mask:0xf bank_mask:0xf
v_cndmask_b32_e64 v170, v170, v12, s[56:57]
v_mov_b32_dpp v13, v163 quad_perm:[1,0,3,2] row_mask:0xf bank_mask:0xf
v_cndmask_b32_dpp v163, v171, v163, vcc quad_perm:[1,0,3,2] row_mask:0xf bank_mask:0xf
v_cndmask_b32_e64 v171, v171, v13, s[56:57]
s_waitcnt vmcnt(13)
v_mov_b32_dpp v12, v176 quad_perm:[1,0,3,2] row_mask:0xf bank_mask:0xf
s_waitcnt vmcnt(12)
v_cndmask_b32_dpp v176, v184, v176, vcc quad_perm:[1,0,3,2] row_mask:0xf bank_mask:0xf
v_cndmask_b32_e64 v184, v184, v12, s[56:57]
v_mov_b32_dpp v13, v177 quad_perm:[1,0,3,2] row_mask:0xf bank_mask:0xf
v_cndmask_b32_dpp v177, v185, v177, vcc quad_perm:[1,0,3,2] row_mask:0xf bank_mask:0xf
v_cndmask_b32_e64 v185, v185, v13, s[56:57]
v_mov_b32_dpp v12, v178 quad_perm:[1,0,3,2] row_mask:0xf bank_mask:0xf
v_cndmask_b32_dpp v178, v186, v178, vcc quad_perm:[1,0,3,2] row_mask:0xf bank_mask:0xf
v_cndmask_b32_e64 v186, v186, v12, s[56:57]
v_mov_b32_dpp v13, v179 quad_perm:[1,0,3,2] row_mask:0xf bank_mask:0xf
v_cndmask_b32_dpp v179, v187, v179, vcc quad_perm:[1,0,3,2] row_mask:0xf bank_mask:0xf
v_cndmask_b32_e64 v187, v187, v13, s[56:57]
s_mov_b64 vcc, s[58:59]
v_mov_b32_dpp v12, v160 quad_perm:[2,3,0,1] row_mask:0xf bank_mask:0xf
v_cndmask_b32_dpp v160, v176, v160, vcc quad_perm:[2,3,0,1] row_mask:0xf bank_mask:0xf
v_cndmask_b32_e64 v176, v176, v12, s[58:59]
v_mov_b32_dpp v13, v161 quad_perm:[2,3,0,1] row_mask:0xf bank_mask:0xf
v_cndmask_b32_dpp v161, v177, v161, vcc quad_perm:[2,3,0,1] row_mask:0xf bank_mask:0xf
v_cndmask_b32_e64 v177, v177, v13, s[58:59]
v_mov_b32_dpp v12, v162 quad_perm:[2,3,0,1] row_mask:0xf bank_mask:0xf
v_cndmask_b32_dpp v162, v178, v162, vcc quad_perm:[2,3,0,1] row_mask:0xf bank_mask:0xf
v_cndmask_b32_e64 v178, v178, v12, s[58:59]
v_mov_b32_dpp v13, v163 quad_perm:[2,3,0,1] row_mask:0xf bank_mask:0xf
v_cndmask_b32_dpp v163, v179, v163, vcc quad_perm:[2,3,0,1] row_mask:0xf bank_mask:0xf
v_cndmask_b32_e64 v179, v179, v13, s[58:59]
v_mov_b32_dpp v12, v168 quad_perm:[2,3,0,1] row_mask:0xf bank_mask:0xf
v_cndmask_b32_dpp v168, v184, v168, vcc quad_perm:[2,3,0,1] row_mask:0xf bank_mask:0xf
v_cndmask_b32_e64 v184, v184, v12, s[58:59]
v_mov_b32_dpp v13, v169 quad_perm:[2,3,0,1] row_mask:0xf bank_mask:0xf
v_cndmask_b32_dpp v169, v185, v169, vcc quad_perm:[2,3,0,1] row_mask:0xf bank_mask:0xf
v_cndmask_b32_e64 v185, v185, v13, s[58:59]
v_mov_b32_dpp v12, v170 quad_perm:[2,3,0,1] row_mask:0xf bank_mask:0xf
v_cndmask_b32_dpp v170, v186, v170, vcc quad_perm:[2,3,0,1] row_mask:0xf bank_mask:0xf
v_cndmask_b32_e64 v186, v186, v12, s[58:59]
v_mov_b32_dpp v13, v171 quad_perm:[2,3,0,1] row_mask:0xf bank_mask:0xf
v_cndmask_b32_dpp v171, v187, v171, vcc quad_perm:[2,3,0,1] row_mask:0xf bank_mask:0xf
v_cndmask_b32_e64 v187, v187, v13, s[58:59]
s_mov_b64 vcc, s[56:57]
s_waitcnt vmcnt(11)
v_mov_b32_dpp v12, v164 quad_perm:[1,0,3,2] row_mask:0xf bank_mask:0xf
s_waitcnt vmcnt(10)
v_cndmask_b32_dpp v164, v172, v164, vcc quad_perm:[1,0,3,2] row_mask:0xf bank_mask:0xf
v_cndmask_b32_e64 v172, v172, v12, s[56:57]
v_mov_b32_dpp v13, v165 quad_perm:[1,0,3,2] row_mask:0xf bank_mask:0xf
v_cndmask_b32_dpp v165, v173, v165, vcc quad_perm:[1,0,3,2] row_mask:0xf bank_mask:0xf
v_cndmask_b32_e64 v173, v173, v13, s[56:57]
v_mov_b32_dpp v12, v166 quad_perm:[1,0,3,2] row_mask:0xf bank_mask:0xf
v_cndmask_b32_dpp v166, v174, v166, vcc quad_perm:[1,0,3,2] row_mask:0xf bank_mask:0xf
v_cndmask_b32_e64 v174, v174, v12, s[56:57]
v_mov_b32_dpp v13, v167 quad_perm:[1,0,3,2] row_mask:0xf bank_mask:0xf
v_cndmask_b32_dpp v167, v175, v167, vcc quad_perm:[1,0,3,2] row_mask:0xf bank_mask:0xf
v_cndmask_b32_e64 v175, v175, v13, s[56:57]
s_waitcnt vmcnt(9)
v_mov_b32_dpp v12, v180 quad_perm:[1,0,3,2] row_mask:0xf bank_mask:0xf
s_waitcnt vmcnt(8)
v_cndmask_b32_dpp v180, v188, v180, vcc quad_perm:[1,0,3,2] row_mask:0xf bank_mask:0xf
v_cndmask_b32_e64 v188, v188, v12, s[56:57]
v_mov_b32_dpp v13, v181 quad_perm:[1,0,3,2] row_mask:0xf bank_mask:0xf
v_cndmask_b32_dpp v181, v189, v181, vcc quad_perm:[1,0,3,2] row_mask:0xf bank_mask:0xf
v_cndmask_b32_e64 v189, v189, v13, s[56:57]
v_mov_b32_dpp v12, v182 quad_perm:[1,0,3,2] row_mask:0xf bank_mask:0xf
v_cndmask_b32_dpp v182, v190, v182, vcc quad_perm:[1,0,3,2] row_mask:0xf bank_mask:0xf
v_cndmask_b32_e64 v190, v190, v12, s[56:57]
v_mov_b32_dpp v13, v183 quad_perm:[1,0,3,2] row_mask:0xf bank_mask:0xf
v_cndmask_b32_dpp v183, v191, v183, vcc quad_perm:[1,0,3,2] row_mask:0xf bank_mask:0xf
v_cndmask_b32_e64 v191, v191, v13, s[56:57]
s_mov_b64 vcc, s[58:59]
v_mov_b32_dpp v12, v164 quad_perm:[2,3,0,1] row_mask:0xf bank_mask:0xf
v_cndmask_b32_dpp v164, v180, v164, vcc quad_perm:[2,3,0,1] row_mask:0xf bank_mask:0xf
v_cndmask_b32_e64 v180, v180, v12, s[58:59]
v_mov_b32_dpp v13, v165 quad_perm:[2,3,0,1] row_mask:0xf bank_mask:0xf
v_cndmask_b32_dpp v165, v181, v165, vcc quad_perm:[2,3,0,1] row_mask:0xf bank_mask:0xf
v_cndmask_b32_e64 v181, v181, v13, s[58:59]
v_mov_b32_dpp v12, v166 quad_perm:[2,3,0,1] row_mask:0xf bank_mask:0xf
v_cndmask_b32_dpp v166, v182, v166, vcc quad_perm:[2,3,0,1] row_mask:0xf bank_mask:0xf
v_cndmask_b32_e64 v182, v182, v12, s[58:59]
v_mov_b32_dpp v13, v167 quad_perm:[2,3,0,1] row_mask:0xf bank_mask:0xf
v_cndmask_b32_dpp v167, v183, v167, vcc quad_perm:[2,3,0,1] row_mask:0xf bank_mask:0xf
v_cndmask_b32_e64 v183, v183, v13, s[58:59]
v_mov_b32_dpp v12, v172 quad_perm:[2,3,0,1] row_mask:0xf bank_mask:0xf
v_cndmask_b32_dpp v172, v188, v172, vcc quad_perm:[2,3,0,1] row_mask:0xf bank_mask:0xf
v_cndmask_b32_e64 v188, v188, v12, s[58:59]
v_mov_b32_dpp v13, v173 quad_perm:[2,3,0,1] row_mask:0xf bank_mask:0xf
v_cndmask_b32_dpp v173, v189, v173, vcc quad_perm:[2,3,0,1] row_mask:0xf bank_mask:0xf
v_cndmask_b32_e64 v189, v189, v13, s[58:59]
v_mov_b32_dpp v12, v174 quad_perm:[2,3,0,1] row_mask:0xf bank_mask:0xf
v_cndmask_b32_dpp v174, v190, v174, vcc quad_perm:[2,3,0,1] row_mask:0xf bank_mask:0xf
v_cndmask_b32_e64 v190, v190, v12, s[58:59]
v_mov_b32_dpp v13, v175 quad_perm:[2,3,0,1] row_mask:0xf bank_mask:0xf
v_cndmask_b32_dpp v175, v191, v175, vcc quad_perm:[2,3,0,1] row_mask:0xf bank_mask:0xf
v_cndmask_b32_e64 v191, v191, v13, s[58:59]
s_lshl_b32 s96, s65, 2
s_add_u32 s96, s96, s67
s_lshl_b32 s96, s96, 4
s_lshr_b32 s97, s68, 1
s_lshl_b32 s97, s97, 3
s_add_u32 s96, s96, s97
s_lshl_b32 s96, s96, 7
s_add_u32 s79, 0x20000, s96
v_lshrrev_b32 v12, 3, v0
v_lshlrev_b32 v12, 7, v12
v_and_b32 v13, 7, v0
v_lshlrev_b32 v13, 3, v13
v_add_u32 v4, v12, v13
s_mov_b32 s84, 0x10000
.Lfk_poll3:
buffer_load_dwordx2 v[12:13], v4, s[36:39], s79 offen sc1
s_waitcnt vmcnt(0)
v_cmp_ne_u64 vcc, s[60:61], v[12:13]
s_cmp_lg_u64 vcc, 0
s_cbranch_scc0 .Lfk_done4
s_sub_u32 s84, s84, 1
s_cbranch_scc1 .Lfk_done4
s_sleep 1
s_branch .Lfk_poll3
.Lfk_done4:
s_add_u32 m0, s75, 0x0
s_and_b32 s96, s68, 1
s_lshl_b32 s96, s96, 1
s_lshr_b32 s97, s68, 1
s_add_u32 s96, s96, s97
s_lshl_b32 s96, s96, 2
s_add_u32 s96, s96, s67
s_lshl_b32 s96, s96, 18
s_add_u32 s96, 0x100000, s96
s_and_b32 s97, s62, 1
s_lshl_b32 s97, s97, 15
s_add_u32 s96, s96, s97
s_lshr_b32 s97, s62, 1
s_xor_b32 s97, s97, s80
s_lshl_b32 s97, s97, 12
s_add_u32 s78, s96, s97
buffer_load_dwordx4 v1, s[36:39], s78 offen sc1 lds
buffer_load_dwordx4 v1, s[36:39], s78 offen offset:1024 sc1 lds
buffer_load_dwordx4 v1, s[36:39], s78 offen offset:2048 sc1 lds
buffer_load_dwordx4 v1, s[36:39], s78 offen offset:3072 sc1 lds
s_add_u32 m0, s75, 0x4000
s_and_b32 s96, s68, 1
s_lshl_b32 s96, s96, 1
s_lshr_b32 s97, s68, 1
s_add_u32 s96, s96, s97
s_lshl_b32 s96, s96, 2
s_add_u32 s96, s96, s67
s_lshl_b32 s96, s96, 18
s_add_u32 s96, 0x100000, s96
s_and_b32 s97, s62, 1
s_lshl_b32 s97, s97, 15
s_add_u32 s96, s96, s97
s_lshr_b32 s97, s62, 1
s_add_u32 s97, s97, 4
s_xor_b32 s97, s97, s80
s_lshl_b32 s97, s97, 12
s_add_u32 s78, s96, s97
buffer_load_dwordx4 v1, s[36:39], s78 offen sc1 lds
buffer_load_dwordx4 v1, s[36:39], s78 offen offset:1024 sc1 lds
buffer_load_dwordx4 v1, s[36:39], s78 offen offset:2048 sc1 lds
buffer_load_dwordx4 v1, s[36:39], s78 offen offset:3072 sc1 lds
s_lshr_b32 s96, s68, 1
s_lshl_b32 s96, s96, 11
s_and_b32 s97, s62, 4
s_add_u32 s97, s97, 3
s_xor_b32 s97, s97, s80
s_lshl_b32 s97, s97, 8
s_add_u32 s96, s96, s97
s_lshr_b32 s97, s62, 2
s_lshl_b32 s97, s97, 16
s_add_u32 s81, s96, s97
buffer_load_dwordx4 v[224:227], v6, s[28:31], s81 offen
s_add_u32 s81, s81, 0x1000
buffer_load_dwordx4 v[232:235], v6, s[28:31], s81 offen
s_add_u32 s81, s81, 0x1000
buffer_load_dwordx4 v[240:243], v6, s[28:31], s81 offen
s_add_u32 s81, s81, 0x1000
buffer_load_dwordx4 v[248:251], v6, s[28:31], s81 offen
s_lshr_b32 s96, s68, 1
s_lshl_b32 s96, s96, 11
s_and_b32 s97, s62, 4
s_add_u32 s97, s97, 3
s_xor_b32 s97, s97, s80
s_lshl_b32 s97, s97, 8
s_add_u32 s96, s96, s97
s_lshr_b32 s97, s62, 2
s_xor_b32 s97, s97, 1
s_lshl_b32 s97, s97, 16
s_add_u32 s81, s96, s97
buffer_load_dwordx4 v[228:231], v6, s[28:31], s81 offen
s_add_u32 s81, s81, 0x1000
buffer_load_dwordx4 v[236:239], v6, s[28:31], s81 offen
s_add_u32 s81, s81, 0x1000
buffer_load_dwordx4 v[244:247], v6, s[28:31], s81 offen
s_add_u32 s81, s81, 0x1000
buffer_load_dwordx4 v[252:255], v6, s[28:31], s81 offen
s_add_u32 m0, s75, 0x8000
s_and_b32 s96, s68, 1
s_lshl_b32 s96, s96, 1
s_lshr_b32 s97, s68, 1
s_add_u32 s96, s96, s97
s_lshl_b32 s96, s96, 2
s_add_u32 s96, s96, s67
s_lshl_b32 s96, s96, 18
s_add_u32 s96, 0x100000, s96
s_and_b32 s97, s62, 1
s_lshl_b32 s97, s97, 15
s_add_u32 s96, s96, s97
s_lshr_b32 s97, s62, 1
s_add_u32 s97, s97, 2
s_xor_b32 s97, s97, s80
s_lshl_b32 s97, s97, 12
s_add_u32 s78, s96, s97
buffer_load_dwordx4 v1, s[36:39], s78 offen sc1 lds
buffer_load_dwordx4 v1, s[36:39], s78 offen offset:1024 sc1 lds
buffer_load_dwordx4 v1, s[36:39], s78 offen offset:2048 sc1 lds
buffer_load_dwordx4 v1, s[36:39], s78 offen offset:3072 sc1 lds
s_add_u32 m0, s75, 0xc000
s_and_b32 s96, s68, 1
s_lshl_b32 s96, s96, 1
s_lshr_b32 s97, s68, 1
s_add_u32 s96, s96, s97
s_lshl_b32 s96, s96, 2
s_add_u32 s96, s96, s67
s_lshl_b32 s96, s96, 18
s_add_u32 s96, 0x100000, s96
s_and_b32 s97, s62, 1
s_lshl_b32 s97, s97, 15
s_add_u32 s96, s96, s97
s_lshr_b32 s97, s62, 1
s_add_u32 s97, s97, 6
s_xor_b32 s97, s97, s80
s_lshl_b32 s97, s97, 12
s_add_u32 s78, s96, s97
buffer_load_dwordx4 v1, s[36:39], s78 offen sc1 lds
buffer_load_dwordx4 v1, s[36:39], s78 offen offset:1024 sc1 lds
buffer_load_dwordx4 v1, s[36:39], s78 offen offset:2048 sc1 lds
buffer_load_dwordx4 v1, s[36:39], s78 offen offset:3072 sc1 lds
s_mov_b64 vcc, s[56:57]
v_mov_b32_dpp v12, v192 quad_perm:[1,0,3,2] row_mask:0xf bank_mask:0xf
v_cndmask_b32_dpp v192, v200, v192, vcc quad_perm:[1,0,3,2] row_mask:0xf bank_mask:0xf
v_cndmask_b32_e64 v200, v200, v12, s[56:57]
v_mov_b32_dpp v13, v193 quad_perm:[1,0,3,2] row_mask:0xf bank_mask:0xf
v_cndmask_b32_dpp v193, v201, v193, vcc quad_perm:[1,0,3,2] row_mask:0xf bank_mask:0xf
v_cndmask_b32_e64 v201, v201, v13, s[56:57]
v_mov_b32_dpp v12, v194 quad_perm:[1,0,3,2] row_mask:0xf bank_mask:0xf
v_cndmask_b32_dpp v194, v202, v194, vcc quad_perm:[1,0,3,2] row_mask:0xf bank_mask:0xf
v_cndmask_b32_e64 v202, v202, v12, s[56:57]
v_mov_b32_dpp v13, v195 quad_perm:[1,0,3,2] row_mask:0xf bank_mask:0xf
v_cndmask_b32_dpp v195, v203, v195, vcc quad_perm:[1,0,3,2] row_mask:0xf bank_mask:0xf
v_cndmask_b32_e64 v203, v203, v13, s[56:57]
v_mov_b32_dpp v12, v208 quad_perm:[1,0,3,2] row_mask:0xf bank_mask:0xf
v_cndmask_b32_dpp v208, v216, v208, vcc quad_perm:[1,0,3,2] row_mask:0xf bank_mask:0xf
v_cndmask_b32_e64 v216, v216, v12, s[56:57]
v_mov_b32_dpp v13, v209 quad_perm:[1,0,3,2] row_mask:0xf bank_mask:0xf
v_cndmask_b32_dpp v209, v217, v209, vcc quad_perm:[1,0,3,2] row_mask:0xf bank_mask:0xf
v_cndmask_b32_e64 v217, v217, v13, s[56:57]
v_mov_b32_dpp v12, v210 quad_perm:[1,0,3,2] row_mask:0xf bank_mask:0xf
v_cndmask_b32_dpp v210, v218, v210, vcc quad_perm:[1,0,3,2] row_mask:0xf bank_mask:0xf
v_cndmask_b32_e64 v218, v218, v12, s[56:57]
v_mov_b32_dpp v13, v211 quad_perm:[1,0,3,2] row_mask:0xf bank_mask:0xf
v_cndmask_b32_dpp v211, v219, v211, vcc quad_perm:[1,0,3,2] row_mask:0xf bank_mask:0xf
v_cndmask_b32_e64 v219, v219, v13, s[56:57]
s_mov_b64 vcc, s[58:59]
v_mov_b32_dpp v12, v192 quad_perm:[2,3,0,1] row_mask:0xf bank_mask:0xf
v_cndmask_b32_dpp v192, v208, v192, vcc quad_perm:[2,3,0,1] row_mask:0xf bank_mask:0xf
v_cndmask_b32_e64 v208, v208, v12, s[58:59]
v_mov_b32_dpp v13, v193 quad_perm:[2,3,0,1] row_mask:0xf bank_mask:0xf
v_cndmask_b32_dpp v193, v209, v193, vcc quad_perm:[2,3,0,1] row_mask:0xf bank_mask:0xf
v_cndmask_b32_e64 v209, v209, v13, s[58:59]
v_mov_b32_dpp v12, v194 quad_perm:[2,3,0,1] row_mask:0xf bank_mask:0xf
v_cndmask_b32_dpp v194, v210, v194, vcc quad_perm:[2,3,0,1] row_mask:0xf bank_mask:0xf
v_cndmask_b32_e64 v210, v210, v12, s[58:59]
v_mov_b32_dpp v13, v195 quad_perm:[2,3,0,1] row_mask:0xf bank_mask:0xf
v_cndmask_b32_dpp v195, v211, v195, vcc quad_perm:[2,3,0,1] row_mask:0xf bank_mask:0xf
v_cndmask_b32_e64 v211, v211, v13, s[58:59]
v_mov_b32_dpp v12, v200 quad_perm:[2,3,0,1] row_mask:0xf bank_mask:0xf
v_cndmask_b32_dpp v200, v216, v200, vcc quad_perm:[2,3,0,1] row_mask:0xf bank_mask:0xf
v_cndmask_b32_e64 v216, v216, v12, s[58:59]
v_mov_b32_dpp v13, v201 quad_perm:[2,3,0,1] row_mask:0xf bank_mask:0xf
v_cndmask_b32_dpp v201, v217, v201, vcc quad_perm:[2,3,0,1] row_mask:0xf bank_mask:0xf
v_cndmask_b32_e64 v217, v217, v13, s[58:59]
v_mov_b32_dpp v12, v202 quad_perm:[2,3,0,1] row_mask:0xf bank_mask:0xf
v_cndmask_b32_dpp v202, v218, v202, vcc quad_perm:[2,3,0,1] row_mask:0xf bank_mask:0xf
v_cndmask_b32_e64 v218, v218, v12, s[58:59]
v_mov_b32_dpp v13, v203 quad_perm:[2,3,0,1] row_mask:0xf bank_mask:0xf
v_cndmask_b32_dpp v203, v219, v203, vcc quad_perm:[2,3,0,1] row_mask:0xf bank_mask:0xf
v_cndmask_b32_e64 v219, v219, v13, s[58:59]
s_mov_b64 vcc, s[56:57]
v_mov_b32_dpp v12, v196 quad_perm:[1,0,3,2] row_mask:0xf bank_mask:0xf
v_cndmask_b32_dpp v196, v204, v196, vcc quad_perm:[1,0,3,2] row_mask:0xf bank_mask:0xf
v_cndmask_b32_e64 v204, v204, v12, s[56:57]
v_mov_b32_dpp v13, v197 quad_perm:[1,0,3,2] row_mask:0xf bank_mask:0xf
v_cndmask_b32_dpp v197, v205, v197, vcc quad_perm:[1,0,3,2] row_mask:0xf bank_mask:0xf
v_cndmask_b32_e64 v205, v205, v13, s[56:57]
v_mov_b32_dpp v12, v198 quad_perm:[1,0,3,2] row_mask:0xf bank_mask:0xf
v_cndmask_b32_dpp v198, v206, v198, vcc quad_perm:[1,0,3,2] row_mask:0xf bank_mask:0xf
v_cndmask_b32_e64 v206, v206, v12, s[56:57]
v_mov_b32_dpp v13, v199 quad_perm:[1,0,3,2] row_mask:0xf bank_mask:0xf
v_cndmask_b32_dpp v199, v207, v199, vcc quad_perm:[1,0,3,2] row_mask:0xf bank_mask:0xf
v_cndmask_b32_e64 v207, v207, v13, s[56:57]
v_mov_b32_dpp v12, v212 quad_perm:[1,0,3,2] row_mask:0xf bank_mask:0xf
v_cndmask_b32_dpp v212, v220, v212, vcc quad_perm:[1,0,3,2] row_mask:0xf bank_mask:0xf
v_cndmask_b32_e64 v220, v220, v12, s[56:57]
v_mov_b32_dpp v13, v213 quad_perm:[1,0,3,2] row_mask:0xf bank_mask:0xf
v_cndmask_b32_dpp v213, v221, v213, vcc quad_perm:[1,0,3,2] row_mask:0xf bank_mask:0xf
v_cndmask_b32_e64 v221, v221, v13, s[56:57]
v_mov_b32_dpp v12, v214 quad_perm:[1,0,3,2] row_mask:0xf bank_mask:0xf
v_cndmask_b32_dpp v214, v222, v214, vcc quad_perm:[1,0,3,2] row_mask:0xf bank_mask:0xf
v_cndmask_b32_e64 v222, v222, v12, s[56:57]
v_mov_b32_dpp v13, v215 quad_perm:[1,0,3,2] row_mask:0xf bank_mask:0xf
v_cndmask_b32_dpp v215, v223, v215, vcc quad_perm:[1,0,3,2] row_mask:0xf bank_mask:0xf
v_cndmask_b32_e64 v223, v223, v13, s[56:57]
s_mov_b64 vcc, s[58:59]
v_mov_b32_dpp v12, v196 quad_perm:[2,3,0,1] row_mask:0xf bank_mask:0xf
v_cndmask_b32_dpp v196, v212, v196, vcc quad_perm:[2,3,0,1] row_mask:0xf bank_mask:0xf
v_cndmask_b32_e64 v212, v212, v12, s[58:59]
v_mov_b32_dpp v13, v197 quad_perm:[2,3,0,1] row_mask:0xf bank_mask:0xf
v_cndmask_b32_dpp v197, v213, v197, vcc quad_perm:[2,3,0,1] row_mask:0xf bank_mask:0xf
v_cndmask_b32_e64 v213, v213, v13, s[58:59]
v_mov_b32_dpp v12, v198 quad_perm:[2,3,0,1] row_mask:0xf bank_mask:0xf
v_cndmask_b32_dpp v198, v214, v198, vcc quad_perm:[2,3,0,1] row_mask:0xf bank_mask:0xf
v_cndmask_b32_e64 v214, v214, v12, s[58:59]
v_mov_b32_dpp v13, v199 quad_perm:[2,3,0,1] row_mask:0xf bank_mask:0xf
v_cndmask_b32_dpp v199, v215, v199, vcc quad_perm:[2,3,0,1] row_mask:0xf bank_mask:0xf
v_cndmask_b32_e64 v215, v215, v13, s[58:59]
v_mov_b32_dpp v12, v204 quad_perm:[2,3,0,1] row_mask:0xf bank_mask:0xf
v_cndmask_b32_dpp v204, v220, v204, vcc quad_perm:[2,3,0,1] row_mask:0xf bank_mask:0xf
v_cndmask_b32_e64 v220, v220, v12, s[58:59]
v_mov_b32_dpp v13, v205 quad_perm:[2,3,0,1] row_mask:0xf bank_mask:0xf
v_cndmask_b32_dpp v205, v221, v205, vcc quad_perm:[2,3,0,1] row_mask:0xf bank_mask:0xf
v_cndmask_b32_e64 v221, v221, v13, s[58:59]
v_mov_b32_dpp v12, v206 quad_perm:[2,3,0,1] row_mask:0xf bank_mask:0xf
v_cndmask_b32_dpp v206, v222, v206, vcc quad_perm:[2,3,0,1] row_mask:0xf bank_mask:0xf
v_cndmask_b32_e64 v222, v222, v12, s[58:59]
v_mov_b32_dpp v13, v207 quad_perm:[2,3,0,1] row_mask:0xf bank_mask:0xf
v_cndmask_b32_dpp v207, v223, v207, vcc quad_perm:[2,3,0,1] row_mask:0xf bank_mask:0xf
v_cndmask_b32_e64 v223, v223, v13, s[58:59]
v_mov_b32 v124, -1
v_mov_b32 v125, -1
v_mov_b32 v126, -1
v_mov_b32 v127, -1
s_add_u32 s78, s76, 0x0
s_mov_b64 exec, s[86:87]
buffer_store_dwordx4 v[124:127], v25, s[36:39], s78 offen sc1
s_mov_b64 exec, -1
s_add_u32 s78, s76, 0x1000
s_mov_b64 exec, s[86:87]
buffer_store_dwordx4 v[124:127], v25, s[36:39], s78 offen sc1
s_mov_b64 exec, -1
s_add_u32 s78, s76, 0x8000
s_mov_b64 exec, s[86:87]
buffer_store_dwordx4 v[124:127], v25, s[36:39], s78 offen sc1
s_mov_b64 exec, -1
s_add_u32 s78, s76, 0x9000
s_mov_b64 exec, s[86:87]
buffer_store_dwordx4 v[124:127], v25, s[36:39], s78 offen sc1
s_mov_b64 exec, -1
s_mov_b32 s70, s80
s_mov_b32 s80, 0xffff0000
s_add_u32 s74, s74, 0x8000
s_waitcnt vmcnt(20) lgkmcnt(0)
s_barrier
ds_read_b128 v[72:75], v21
ds_read_b128 v[76:79], v21 offset:1024
ds_read_b128 v[80:83], v21 offset:2048
ds_read_b128 v[84:87], v21 offset:3072
ds_read_b128 v[88:91], v21 offset:4096
s_waitcnt lgkmcnt(4)
v_mfma_f32_16x16x32_bf16 v[56:59], v[72:75], v[128:131], 0
v_mfma_f32_16x16x32_bf16 v[60:63], v[72:75], v[132:135], 0
s_lshl_b32 s96, s83, 5
s_lshl_b32 s97, s62, 1
s_add_u32 s96, s96, s97
s_lshl_b32 s96, s96, 12
s_add_u32 s29, 0x500000, s96
s_add_u32 m0, s75, 0x10000
s_add_u32 s74, s74, 0x8000
buffer_load_dwordx4 v1, s[36:39], s74 offen sc1 lds
ds_read_b128 v[92:95], v21 offset:5120
s_waitcnt lgkmcnt(4)
v_mfma_f32_16x16x32_bf16 v[56:59], v[76:79], v[136:139], v[56:59]
s_lshl_b32 s96, s82, 18
s_add_u32 s96, 0x1a00000, s96
s_lshl_b32 s97, s62, 10
s_add_u32 s30, s96, s97
s_lshl_b32 s96, s62, 12
v_add_u32 v5, s96, v1
v_mfma_f32_16x16x32_bf16 v[60:63], v[76:79], v[140:143], v[60:63]
v_add_u32 v6, 0x20000, v5
s_add_u32 m0, s75, 0x10000
s_nop 0
buffer_load_dwordx4 v1, s[36:39], s74 offen offset:1024 sc1 lds
v_and_b32 v12, 1, v0
v_lshlrev_b32 v52, 4, v12
s_movk_i32 s78, 0x3f80
v_lshlrev_b32_e64 v52, v52, s78
ds_read_b128 v[72:75], v21 offset:6144
s_waitcnt lgkmcnt(4)
v_mfma_f32_16x16x32_bf16 v[56:59], v[80:83], v[144:147], v[56:59]
s_mov_b32 vcc_lo, 0x30003
s_mov_b32 vcc_hi, 0x30003
v_cndmask_b32 v48, 0, v52, vcc
v_mfma_f32_16x16x32_bf16 v[60:63], v[80:83], v[148:151], v[60:63]
s_mov_b32 vcc_lo, 0x300030
s_mov_b32 vcc_hi, 0x300030
v_cndmask_b32 v49, 0, v52, vcc
s_add_u32 m0, s75, 0x10000
s_nop 0
buffer_load_dwordx4 v1, s[36:39], s74 offen offset:2048 sc1 lds
s_mov_b32 vcc_lo, 0x3000300
s_mov_b32 vcc_hi, 0x3000300
v_cndmask_b32 v50, 0, v52, vcc
ds_read_b128 v[76:79], v21 offset:7168
s_waitcnt lgkmcnt(4)
v_mfma_f32_16x16x32_bf16 v[56:59], v[84:87], v[152:155], v[56:59]
s_mov_b32 vcc_lo, 0x30003000
s_mov_b32 vcc_hi, 0x30003000
v_cndmask_b32 v51, 0, v52, vcc
v_mfma_f32_16x16x32_bf16 v[60:63], v[84:87], v[156:159], v[60:63]
s_lshl_b32 s96, s62, 10
v_add_u32 v12, s96, v2
v_add_u32 v100, 0x22000, v12
s_add_u32 m0, s75, 0x10000
s_nop 0
buffer_load_dwordx4 v1, s[36:39], s74 offen offset:3072 sc1 lds
ds_read_b64_tr_b16 v[112:113], v100
ds_read_b128 v[80:83], v21 offset:8192
s_waitcnt lgkmcnt(5)
v_mfma_f32_16x16x32_bf16 v[64:67], v[88:91], v[128:131], 0
ds_read_b64_tr_b16 v[114:115], v100 offset:512
ds_read_b64_tr_b16 v[116:117], v100 offset:8192
v_mfma_f32_16x16x32_bf16 v[68:71], v[88:91], v[132:135], 0
ds_read_b64_tr_b16 v[118:119], v100 offset:8704
v_mov_b32 v124, 0x3f803f80
ds_read_b128 v[84:87], v21 offset:9216
s_waitcnt lgkmcnt(8)
v_mfma_f32_16x16x32_bf16 v[64:67], v[92:95], v[136:139], v[64:67]
v_mov_b32 v125, 0x3f803f80
v_mov_b32 v126, 0x3f803f80
v_mfma_f32_16x16x32_bf16 v[68:71], v[92:95], v[140:143], v[68:71]
v_mov_b32 v127, 0x3f803f80
ds_read_b128 v[88:91], v21 offset:10240
s_waitcnt lgkmcnt(8)
v_mfma_f32_16x16x32_bf16 v[64:67], v[72:75], v[144:147], v[64:67]
v_mfma_f32_16x16x32_bf16 v[68:71], v[72:75], v[148:151], v[68:71]
s_waitcnt lgkmcnt(4)
v_mfma_f32_16x16x32_bf16 v[100:103], v[112:115], v[124:127], 0
ds_read_b128 v[92:95], v21 offset:11264
v_mfma_f32_16x16x32_bf16 v[64:67], v[76:79], v[152:155], v[64:67]
s_waitcnt lgkmcnt(3)
v_mfma_f32_16x16x32_bf16 v[104:107], v[116:119], v[124:127], 0
v_lshrrev_b32 v12, 4, v0
v_lshlrev_b32 v12, 5, v12
v_and_b32 v13, 1, v0
v_lshlrev_b32 v13, 4, v13
v_add_u32 v53, v12, v13
v_mfma_f32_16x16x32_bf16 v[68:71], v[76:79], v[156:159], v[68:71]
ds_read_b128 v[72:75], v21 offset:12288
v_mfma_f32_16x16x32_bf16 v[56:59], v[80:83], v[160:163], v[56:59]
v_cndmask_b32_e64 v100, v104, v100, s[56:57]
v_mfma_f32_16x16x32_bf16 v[60:63], v[80:83], v[164:167], v[60:63]
v_cndmask_b32_e64 v101, v105, v101, s[56:57]
v_cndmask_b32_e64 v102, v106, v102, s[56:57]
ds_read_b128 v[76:79], v21 offset:13312
s_waitcnt lgkmcnt(4)
v_mfma_f32_16x16x32_bf16 v[56:59], v[84:87], v[168:171], v[56:59]
v_cndmask_b32_e64 v103, v107, v103, s[56:57]
s_lshl_b32 s96, s82, 3
s_add_u32 s96, s96, s62
s_lshl_b32 s96, s96, 4
s_add_u32 s96, s96, s83
s_lshl_b32 s96, s96, 7
s_add_u32 s78, 0x80000, s96
s_mov_b32 exec_lo, 0x30003
s_mov_b32 exec_hi, 0x30003
buffer_store_dwordx4 v[100:103], v53, s[36:39], s78 offen sc1
s_mov_b64 exec, -1
v_mfma_f32_16x16x32_bf16 v[60:63], v[84:87], v[172:175], v[60:63]
ds_read_b128 v[80:83], v21 offset:14336
s_waitcnt lgkmcnt(4)
v_mfma_f32_16x16x32_bf16 v[56:59], v[88:91], v[176:179], v[56:59]
v_mfma_f32_16x16x32_bf16 v[60:63], v[88:91], v[180:183], v[60:63]
ds_read_b128 v[84:87], v21 offset:15360
s_waitcnt lgkmcnt(4)
v_mfma_f32_16x16x32_bf16 v[56:59], v[92:95], v[184:187], v[56:59]
s_add_u32 m0, s75, 0x18000
s_add_u32 s74, s74, 0x8000
buffer_load_dwordx4 v1, s[36:39], s74 offen sc1 lds
s_lshl_b32 s96, s83, 14
s_add_u32 s96, s30, s96
s_add_u32 s78, s96, 0
buffer_store_dwordx4 v[112:115], v20, s[36:39], s78 offen sc1
s_waitcnt vmcnt(11)
s_barrier
v_mfma_f32_16x16x32_bf16 v[60:63], v[92:95], v[188:191], v[60:63]
ds_read_b128 v[88:91], v21 offset:32768
s_waitcnt lgkmcnt(4)
v_mfma_f32_16x16x32_bf16 v[64:67], v[72:75], v[160:163], v[64:67]
v_mfma_f32_16x16x32_bf16 v[68:71], v[72:75], v[164:167], v[68:71]
s_add_u32 m0, s75, 0x18000
s_nop 0
buffer_load_dwordx4 v1, s[36:39], s74 offen offset:1024 sc1 lds
ds_read_b128 v[92:95], v21 offset:33792
s_waitcnt lgkmcnt(4)
v_mfma_f32_16x16x32_bf16 v[64:67], v[76:79], v[168:171], v[64:67]
v_mfma_f32_16x16x32_bf16 v[68:71], v[76:79], v[172:175], v[68:71]
ds_read_b128 v[72:75], v21 offset:34816
s_waitcnt lgkmcnt(4)
v_mfma_f32_16x16x32_bf16 v[64:67], v[80:83], v[176:179], v[64:67]
v_mfma_f32_16x16x32_bf16 v[68:71], v[80:83], v[180:183], v[68:71]
s_add_u32 m0, s75, 0x18000
s_nop 0
buffer_load_dwordx4 v1, s[36:39], s74 offen offset:2048 sc1 lds
v_mov_b32 v108, -1
ds_read_b128 v[76:79], v21 offset:35840
s_waitcnt lgkmcnt(4)
v_mfma_f32_16x16x32_bf16 v[64:67], v[84:87], v[184:187], v[64:67]
v_mov_b32 v109, -1
v_mov_b32 v110, -1
v_mfma_f32_16x16x32_bf16 v[68:71], v[84:87], v[188:191], v[68:71]
v_mov_b32 v111, -1
ds_read_b128 v[80:83], v21 offset:36864
s_waitcnt lgkmcnt(4)
v_mfma_f32_16x16x32_bf16 v[56:59], v[88:91], v[192:195], v[56:59]
s_add_u32 m0, s75, 0x18000
s_nop 0
buffer_load_dwordx4 v1, s[36:39], s74 offen offset:3072 sc1 lds
s_lshl_b32 s96, s83, 14
s_add_u32 s96, s30, s96
s_add_u32 s78, s96, 0x2000
buffer_store_dwordx4 v[116:119], v20, s[36:39], s78 offen sc1
v_mfma_f32_16x16x32_bf16 v[60:63], v[88:91], v[196:199], v[60:63]
ds_read_b128 v[84:87], v21 offset:37888
s_waitcnt lgkmcnt(4)
v_mfma_f32_16x16x32_bf16 v[56:59], v[92:95], v[200:203], v[56:59]
v_mfma_f32_16x16x32_bf16 v[60:63], v[92:95], v[204:207], v[60:63]
ds_read_b128 v[88:91], v21 offset:38912
s_waitcnt lgkmcnt(4)
v_mfma_f32_16x16x32_bf16 v[56:59], v[72:75], v[208:211], v[56:59]
s_mov_b64 vcc, s[56:57]
v_mov_b32_dpp v32, v224 quad_perm:[1,0,3,2] row_mask:0xf bank_mask:0xf
v_cndmask_b32_dpp v224, v232, v224, vcc quad_perm:[1,0,3,2] row_mask:0xf bank_mask:0xf
v_cndmask_b32_e64 v232, v232, v32, s[56:57]
s_mov_b64 vcc, s[56:57]
v_mov_b32_dpp v33, v225 quad_perm:[1,0,3,2] row_mask:0xf bank_mask:0xf
v_cndmask_b32_dpp v225, v233, v225, vcc quad_perm:[1,0,3,2] row_mask:0xf bank_mask:0xf
v_cndmask_b32_e64 v233, v233, v33, s[56:57]
v_mfma_f32_16x16x32_bf16 v[60:63], v[72:75], v[212:215], v[60:63]
s_mov_b64 vcc, s[56:57]
v_mov_b32_dpp v34, v226 quad_perm:[1,0,3,2] row_mask:0xf bank_mask:0xf
v_cndmask_b32_dpp v226, v234, v226, vcc quad_perm:[1,0,3,2] row_mask:0xf bank_mask:0xf
v_cndmask_b32_e64 v234, v234, v34, s[56:57]
s_mov_b64 vcc, s[56:57]
v_mov_b32_dpp v35, v227 quad_perm:[1,0,3,2] row_mask:0xf bank_mask:0xf
v_cndmask_b32_dpp v227, v235, v227, vcc quad_perm:[1,0,3,2] row_mask:0xf bank_mask:0xf
v_cndmask_b32_e64 v235, v235, v35, s[56:57]
s_mov_b64 vcc, s[56:57]
v_mov_b32_dpp v32, v240 quad_perm:[1,0,3,2] row_mask:0xf bank_mask:0xf
v_cndmask_b32_dpp v240, v248, v240, vcc quad_perm:[1,0,3,2] row_mask:0xf bank_mask:0xf
v_cndmask_b32_e64 v248, v248, v32, s[56:57]
ds_read_b128 v[92:95], v21 offset:39936
s_waitcnt lgkmcnt(4)
v_mfma_f32_16x16x32_bf16 v[56:59], v[76:79], v[216:219], v[56:59]
s_mov_b64 vcc, s[56:57]
v_mov_b32_dpp v33, v241 quad_perm:[1,0,3,2] row_mask:0xf bank_mask:0xf
v_cndmask_b32_dpp v241, v249, v241, vcc quad_perm:[1,0,3,2] row_mask:0xf bank_mask:0xf
v_cndmask_b32_e64 v249, v249, v33, s[56:57]
s_mov_b64 vcc, s[56:57]
v_mov_b32_dpp v34, v242 quad_perm:[1,0,3,2] row_mask:0xf bank_mask:0xf
v_cndmask_b32_dpp v242, v250, v242, vcc quad_perm:[1,0,3,2] row_mask:0xf bank_mask:0xf
v_cndmask_b32_e64 v250, v250, v34, s[56:57]
s_waitcnt vmcnt(7)
s_barrier
s_mov_b64 vcc, s[56:57]
v_mov_b32_dpp v35, v243 quad_perm:[1,0,3,2] row_mask:0xf bank_mask:0xf
v_cndmask_b32_dpp v243, v251, v243, vcc quad_perm:[1,0,3,2] row_mask:0xf bank_mask:0xf
v_cndmask_b32_e64 v251, v251, v35, s[56:57]
v_mfma_f32_16x16x32_bf16 v[60:63], v[76:79], v[220:223], v[60:63]
s_cmp_eq_u32 s62, 0
s_cbranch_scc1 .Lfk_rare6
.Lfk_join5:
s_mov_b64 vcc, s[58:59]
v_mov_b32_dpp v32, v224 quad_perm:[2,3,0,1] row_mask:0xf bank_mask:0xf
v_cndmask_b32_dpp v224, v240, v224, vcc quad_perm:[2,3,0,1] row_mask:0xf bank_mask:0xf
v_cndmask_b32_e64 v240, v240, v32, s[58:59]
s_add_u32 m0, s75, 0x0
s_add_u32 s74, s74, 0x8000
buffer_load_dwordx4 v1, s[36:39], s74 offen sc1 lds
s_mov_b64 vcc, s[58:59]
v_mov_b32_dpp v33, v225 quad_perm:[2,3,0,1] row_mask:0xf bank_mask:0xf
v_cndmask_b32_dpp v225, v241, v225, vcc quad_perm:[2,3,0,1] row_mask:0xf bank_mask:0xf
v_cndmask_b32_e64 v241, v241, v33, s[58:59]
ds_read_b128 v[72:75], v21 offset:40960
s_waitcnt lgkmcnt(4)
v_mfma_f32_16x16x32_bf16 v[64:67], v[80:83], v[192:195], v[64:67]
s_mov_b64 vcc, s[58:59]
v_mov_b32_dpp v34, v226 quad_perm:[2,3,0,1] row_mask:0xf bank_mask:0xf
v_cndmask_b32_dpp v226, v242, v226, vcc quad_perm:[2,3,0,1] row_mask:0xf bank_mask:0xf
v_cndmask_b32_e64 v242, v242, v34, s[58:59]
s_mov_b64 vcc, s[58:59]
v_mov_b32_dpp v35, v227 quad_perm:[2,3,0,1] row_mask:0xf bank_mask:0xf
v_cndmask_b32_dpp v227, v243, v227, vcc quad_perm:[2,3,0,1] row_mask:0xf bank_mask:0xf
v_cndmask_b32_e64 v243, v243, v35, s[58:59]
s_mov_b64 vcc, s[58:59]
v_mov_b32_dpp v32, v232 quad_perm:[2,3,0,1] row_mask:0xf bank_mask:0xf
v_cndmask_b32_dpp v232, v248, v232, vcc quad_perm:[2,3,0,1] row_mask:0xf bank_mask:0xf
v_cndmask_b32_e64 v248, v248, v32, s[58:59]
v_mfma_f32_16x16x32_bf16 v[68:71], v[80:83], v[196:199], v[68:71]
s_mov_b64 vcc, s[58:59]
v_mov_b32_dpp v33, v233 quad_perm:[2,3,0,1] row_mask:0xf bank_mask:0xf
v_cndmask_b32_dpp v233, v249, v233, vcc quad_perm:[2,3,0,1] row_mask:0xf bank_mask:0xf
v_cndmask_b32_e64 v249, v249, v33, s[58:59]
s_mov_b64 vcc, s[58:59]
v_mov_b32_dpp v34, v234 quad_perm:[2,3,0,1] row_mask:0xf bank_mask:0xf
v_cndmask_b32_dpp v234, v250, v234, vcc quad_perm:[2,3,0,1] row_mask:0xf bank_mask:0xf
v_cndmask_b32_e64 v250, v250, v34, s[58:59]
v_lshrrev_b32 v12, 4, v0
v_lshlrev_b32 v12, 15, v12
v_and_b32 v13, 15, v0
v_lshlrev_b32 v13, 4, v13
v_add_u32 v12, v12, v13
buffer_store_dwordx4 v[108:111], v12, s[36:39], s77 offen sc1
s_mov_b64 vcc, s[58:59]
v_mov_b32_dpp v35, v235 quad_perm:[2,3,0,1] row_mask:0xf bank_mask:0xf
v_cndmask_b32_dpp v235, v251, v235, vcc quad_perm:[2,3,0,1] row_mask:0xf bank_mask:0xf
v_cndmask_b32_e64 v251, v251, v35, s[58:59]
ds_read_b128 v[76:79], v21 offset:41984
s_waitcnt lgkmcnt(4)
v_mfma_f32_16x16x32_bf16 v[64:67], v[84:87], v[200:203], v[64:67]
s_mov_b64 vcc, s[56:57]
v_mov_b32_dpp v32, v228 quad_perm:[1,0,3,2] row_mask:0xf bank_mask:0xf
v_cndmask_b32_dpp v228, v236, v228, vcc quad_perm:[1,0,3,2] row_mask:0xf bank_mask:0xf
v_cndmask_b32_e64 v236, v236, v32, s[56:57]
s_mov_b64 vcc, s[56:57]
v_mov_b32_dpp v33, v229 quad_perm:[1,0,3,2] row_mask:0xf bank_mask:0xf
v_cndmask_b32_dpp v229, v237, v229, vcc quad_perm:[1,0,3,2] row_mask:0xf bank_mask:0xf
v_cndmask_b32_e64 v237, v237, v33, s[56:57]
v_mfma_f32_16x16x32_bf16 v[68:71], v[84:87], v[204:207], v[68:71]
s_mov_b64 vcc, s[56:57]
v_mov_b32_dpp v34, v230 quad_perm:[1,0,3,2] row_mask:0xf bank_mask:0xf
v_cndmask_b32_dpp v230, v238, v230, vcc quad_perm:[1,0,3,2] row_mask:0xf bank_mask:0xf
v_cndmask_b32_e64 v238, v238, v34, s[56:57]
s_mov_b64 vcc, s[56:57]
v_mov_b32_dpp v35, v231 quad_perm:[1,0,3,2] row_mask:0xf bank_mask:0xf
v_cndmask_b32_dpp v231, v239, v231, vcc quad_perm:[1,0,3,2] row_mask:0xf bank_mask:0xf
v_cndmask_b32_e64 v239, v239, v35, s[56:57]
s_mov_b64 vcc, s[56:57]
v_mov_b32_dpp v32, v244 quad_perm:[1,0,3,2] row_mask:0xf bank_mask:0xf
v_cndmask_b32_dpp v244, v252, v244, vcc quad_perm:[1,0,3,2] row_mask:0xf bank_mask:0xf
v_cndmask_b32_e64 v252, v252, v32, s[56:57]
ds_read_b128 v[80:83], v21 offset:43008
s_waitcnt lgkmcnt(4)
v_mfma_f32_16x16x32_bf16 v[64:67], v[88:91], v[208:211], v[64:67]
s_mov_b64 vcc, s[56:57]
v_mov_b32_dpp v33, v245 quad_perm:[1,0,3,2] row_mask:0xf bank_mask:0xf
v_cndmask_b32_dpp v245, v253, v245, vcc quad_perm:[1,0,3,2] row_mask:0xf bank_mask:0xf
v_cndmask_b32_e64 v253, v253, v33, s[56:57]
s_mov_b64 vcc, s[56:57]
v_mov_b32_dpp v34, v246 quad_perm:[1,0,3,2] row_mask:0xf bank_mask:0xf
v_cndmask_b32_dpp v246, v254, v246, vcc quad_perm:[1,0,3,2] row_mask:0xf bank_mask:0xf
v_cndmask_b32_e64 v254, v254, v34, s[56:57]
s_mov_b64 vcc, s[56:57]
v_mov_b32_dpp v35, v247 quad_perm:[1,0,3,2] row_mask:0xf bank_mask:0xf
v_cndmask_b32_dpp v247, v255, v247, vcc quad_perm:[1,0,3,2] row_mask:0xf bank_mask:0xf
v_cndmask_b32_e64 v255, v255, v35, s[56:57]
v_mfma_f32_16x16x32_bf16 v[68:71], v[88:91], v[212:215], v[68:71]
s_mov_b64 vcc, s[58:59]
v_mov_b32_dpp v32, v228 quad_perm:[2,3,0,1] row_mask:0xf bank_mask:0xf
v_cndmask_b32_dpp v228, v244, v228, vcc quad_perm:[2,3,0,1] row_mask:0xf bank_mask:0xf
v_cndmask_b32_e64 v244, v244, v32, s[58:59]
s_add_u32 m0, s75, 0x0
s_nop 0
buffer_load_dwordx4 v1, s[36:39], s74 offen offset:1024 sc1 lds
s_mov_b64 vcc, s[58:59]
v_mov_b32_dpp v33, v229 quad_perm:[2,3,0,1] row_mask:0xf bank_mask:0xf
v_cndmask_b32_dpp v229, v245, v229, vcc quad_perm:[2,3,0,1] row_mask:0xf bank_mask:0xf
v_cndmask_b32_e64 v245, v245, v33, s[58:59]
ds_read_b128 v[84:87], v21 offset:44032
s_waitcnt lgkmcnt(4)
v_mfma_f32_16x16x32_bf16 v[64:67], v[92:95], v[216:219], v[64:67]
s_mov_b64 vcc, s[58:59]
v_mov_b32_dpp v34, v230 quad_perm:[2,3,0,1] row_mask:0xf bank_mask:0xf
v_cndmask_b32_dpp v230, v246, v230, vcc quad_perm:[2,3,0,1] row_mask:0xf bank_mask:0xf
v_cndmask_b32_e64 v246, v246, v34, s[58:59]
s_mov_b64 vcc, s[58:59]
v_mov_b32_dpp v35, v231 quad_perm:[2,3,0,1] row_mask:0xf bank_mask:0xf
v_cndmask_b32_dpp v231, v247, v231, vcc quad_perm:[2,3,0,1] row_mask:0xf bank_mask:0xf
v_cndmask_b32_e64 v247, v247, v35, s[58:59]
s_mov_b64 vcc, s[58:59]
v_mov_b32_dpp v32, v236 quad_perm:[2,3,0,1] row_mask:0xf bank_mask:0xf
v_cndmask_b32_dpp v236, v252, v236, vcc quad_perm:[2,3,0,1] row_mask:0xf bank_mask:0xf
v_cndmask_b32_e64 v252, v252, v32, s[58:59]
v_mfma_f32_16x16x32_bf16 v[68:71], v[92:95], v[220:223], v[68:71]
s_mov_b64 vcc, s[58:59]
v_mov_b32_dpp v33, v237 quad_perm:[2,3,0,1] row_mask:0xf bank_mask:0xf
v_cndmask_b32_dpp v237, v253, v237, vcc quad_perm:[2,3,0,1] row_mask:0xf bank_mask:0xf
v_cndmask_b32_e64 v253, v253, v33, s[58:59]
s_mov_b64 vcc, s[58:59]
v_mov_b32_dpp v34, v238 quad_perm:[2,3,0,1] row_mask:0xf bank_mask:0xf
v_cndmask_b32_dpp v238, v254, v238, vcc quad_perm:[2,3,0,1] row_mask:0xf bank_mask:0xf
v_cndmask_b32_e64 v254, v254, v34, s[58:59]
s_mov_b64 vcc, s[58:59]
v_mov_b32_dpp v35, v239 quad_perm:[2,3,0,1] row_mask:0xf bank_mask:0xf
v_cndmask_b32_dpp v239, v255, v239, vcc quad_perm:[2,3,0,1] row_mask:0xf bank_mask:0xf
v_cndmask_b32_e64 v255, v255, v35, s[58:59]
ds_read_b128 v[88:91], v21 offset:45056
s_waitcnt lgkmcnt(4)
v_mfma_f32_16x16x32_bf16 v[56:59], v[72:75], v[224:227], v[56:59]
v_mfma_f32_16x16x32_bf16 v[60:63], v[72:75], v[228:231], v[60:63]
ds_read_b128 v[92:95], v21 offset:46080
s_waitcnt lgkmcnt(4)
v_mfma_f32_16x16x32_bf16 v[56:59], v[76:79], v[232:235], v[56:59]
v_mfma_f32_16x16x32_bf16 v[60:63], v[76:79], v[236:239], v[60:63]
s_add_u32 m0, s75, 0x0
s_nop 0
buffer_load_dwordx4 v1, s[36:39], s74 offen offset:2048 sc1 lds
ds_read_b128 v[72:75], v21 offset:47104
s_waitcnt lgkmcnt(4)
v_mfma_f32_16x16x32_bf16 v[56:59], v[80:83], v[240:243], v[56:59]
v_mfma_f32_16x16x32_bf16 v[60:63], v[80:83], v[244:247], v[60:63]
ds_read_b128 v[76:79], v21 offset:48128
s_waitcnt lgkmcnt(4)
v_mfma_f32_16x16x32_bf16 v[56:59], v[84:87], v[248:251], v[56:59]
v_mfma_f32_16x16x32_bf16 v[60:63], v[84:87], v[252:255], v[60:63]
ds_read_b128 v[80:83], v22
s_waitcnt lgkmcnt(4)
v_mfma_f32_16x16x32_bf16 v[64:67], v[88:91], v[224:227], v[64:67]
v_mfma_f32_16x16x32_bf16 v[68:71], v[88:91], v[228:231], v[68:71]
s_add_u32 m0, s75, 0x0
s_nop 0
buffer_load_dwordx4 v1, s[36:39], s74 offen offset:3072 sc1 lds
ds_read_b128 v[84:87], v22 offset:1024
s_waitcnt lgkmcnt(4)
v_mfma_f32_16x16x32_bf16 v[64:67], v[92:95], v[232:235], v[64:67]
v_mfma_f32_16x16x32_bf16 v[68:71], v[92:95], v[236:239], v[68:71]
ds_read_b128 v[88:91], v22 offset:2048
s_waitcnt lgkmcnt(4)
v_mfma_f32_16x16x32_bf16 v[64:67], v[72:75], v[240:243], v[64:67]
v_mfma_f32_16x16x32_bf16 v[68:71], v[72:75], v[244:247], v[68:71]
ds_read_b128 v[92:95], v22 offset:3072
s_waitcnt lgkmcnt(4)
v_mfma_f32_16x16x32_bf16 v[64:67], v[76:79], v[248:251], v[64:67]
v_mfma_f32_16x16x32_bf16 v[68:71], v[76:79], v[252:255], v[68:71]
ds_read_b128 v[72:75], v22 offset:4096
s_waitcnt lgkmcnt(4)
v_mfma_f32_16x16x32_bf16 v[40:43], v[80:83], v[128:131], 0
v_mfma_f32_16x16x32_bf16 v[44:47], v[80:83], v[132:135], 0
ds_write_b128 v23, v[60:63] offset:16384
ds_read_b128 v[76:79], v22 offset:5120
s_waitcnt lgkmcnt(5)
v_mfma_f32_16x16x32_bf16 v[40:43], v[84:87], v[136:139], v[40:43]
ds_write_b128 v23, v[68:71] offset:8192
v_mfma_f32_16x16x32_bf16 v[44:47], v[84:87], v[140:143], v[44:47]
ds_read_b128 v[80:83], v22 offset:6144
s_waitcnt lgkmcnt(6)
v_mfma_f32_16x16x32_bf16 v[40:43], v[88:91], v[144:147], v[40:43]
v_mfma_f32_16x16x32_bf16 v[44:47], v[88:91], v[148:151], v[44:47]
ds_read_b128 v[84:87], v22 offset:7168
s_waitcnt lgkmcnt(6)
v_mfma_f32_16x16x32_bf16 v[40:43], v[92:95], v[152:155], v[40:43]
s_waitcnt vmcnt(5) lgkmcnt(2)
s_barrier
v_mfma_f32_16x16x32_bf16 v[44:47], v[92:95], v[156:159], v[44:47]
ds_read_b128 v[96:99], v24 offset:16384
s_cmp_eq_u32 s62, 0
s_cbranch_scc1 .Lfk_rare8
.Lfk_join7:
s_add_u32 m0, s75, 0x8000
s_add_u32 s74, s74, 0x8000
buffer_load_dwordx4 v1, s[36:39], s74 offen sc1 lds
ds_read_b128 v[88:91], v22 offset:8192
v_mfma_f32_16x16x32_bf16 v[40:43], v[72:75], v[160:163], v[40:43]
v_mfma_f32_16x16x32_bf16 v[44:47], v[72:75], v[164:167], v[44:47]
ds_read_b128 v[92:95], v22 offset:9216
v_mfma_f32_16x16x32_bf16 v[40:43], v[76:79], v[168:171], v[40:43]
v_mfma_f32_16x16x32_bf16 v[44:47], v[76:79], v[172:175], v[44:47]
s_waitcnt lgkmcnt(2)
v_add_f32 v56, v56, v96
ds_read_b128 v[72:75], v22 offset:10240
v_mfma_f32_16x16x32_bf16 v[40:43], v[80:83], v[176:179], v[40:43]
v_add_f32 v57, v57, v97
v_add_f32 v58, v58, v98
v_mfma_f32_16x16x32_bf16 v[44:47], v[80:83], v[180:183], v[44:47]
v_add_f32 v59, v59, v99
s_add_u32 m0, s75, 0x8000
s_nop 0
buffer_load_dwordx4 v1, s[36:39], s74 offen offset:1024 sc1 lds
s_cmp_eq_u32 s68, 0
s_cbranch_scc0 .Lfk_else9
v_lshlrev_b32 v96, 16, v16
v_and_b32 v97, s80, v16
v_lshlrev_b32 v98, 16, v17
v_and_b32 v99, s80, v17
v_add_f32 v56, v56, v96
v_add_f32 v57, v57, v97
v_add_f32 v58, v58, v98
v_add_f32 v59, v59, v99
.Lfk_else9:
ds_read_b128 v[76:79], v22 offset:11264
v_mfma_f32_16x16x32_bf16 v[40:43], v[84:87], v[184:187], v[40:43]
v_cvt_pk_bf16_f32 v32, v56, v57
v_cvt_pk_bf16_f32 v33, v58, v59
v_mfma_f32_16x16x32_bf16 v[44:47], v[84:87], v[188:191], v[44:47]
ds_read_b128 v[96:99], v24 offset:8192
v_min_u32 v32, -2, v32
s_cmp_eq_u32 s62, 1
s_cbranch_scc1 .Lfk_rare11
.Lfk_join10:
v_min_u32 v33, -2, v33
ds_read_b128 v[80:83], v22 offset:12288
s_waitcnt lgkmcnt(5)
v_mfma_f32_16x16x32_bf16 v[40:43], v[88:91], v[192:195], v[40:43]
v_mfma_f32_16x16x32_bf16 v[44:47], v[88:91], v[196:199], v[44:47]
ds_read_b128 v[84:87], v22 offset:13312
s_waitcnt lgkmcnt(5)
v_mfma_f32_16x16x32_bf16 v[40:43], v[92:95], v[200:203], v[40:43]
v_mfma_f32_16x16x32_bf16 v[44:47], v[92:95], v[204:207], v[44:47]
s_add_u32 m0, s75, 0x8000
s_nop 0
buffer_load_dwordx4 v1, s[36:39], s74 offen offset:2048 sc1 lds
s_waitcnt lgkmcnt(2)
v_add_f32 v64, v64, v96
ds_read_b128 v[88:91], v22 offset:14336
v_mfma_f32_16x16x32_bf16 v[40:43], v[72:75], v[208:211], v[40:43]
v_add_f32 v65, v65, v97
v_add_f32 v66, v66, v98
v_mfma_f32_16x16x32_bf16 v[44:47], v[72:75], v[212:215], v[44:47]
v_add_f32 v67, v67, v99
s_cmp_eq_u32 s68, 0
s_cbranch_scc0 .Lfk_else12
v_lshlrev_b32 v96, 16, v18
v_and_b32 v97, s80, v18
v_lshlrev_b32 v98, 16, v19
v_and_b32 v99, s80, v19
v_add_f32 v64, v64, v96
v_add_f32 v65, v65, v97
v_add_f32 v66, v66, v98
v_add_f32 v67, v67, v99
.Lfk_else12:
ds_read_b128 v[92:95], v22 offset:15360
v_mfma_f32_16x16x32_bf16 v[40:43], v[76:79], v[216:219], v[40:43]
v_cvt_pk_bf16_f32 v34, v64, v65
v_cvt_pk_bf16_f32 v35, v66, v67
v_mfma_f32_16x16x32_bf16 v[44:47], v[76:79], v[220:223], v[44:47]
v_min_u32 v34, -2, v34
v_min_u32 v35, -2, v35
ds_read_b128 v[72:75], v22 offset:32768
s_waitcnt lgkmcnt(4)
v_mfma_f32_16x16x32_bf16 v[40:43], v[80:83], v[224:227], v[40:43]
s_add_u32 s78, s76, 0x0
s_mov_b64 exec, s[86:87]
buffer_store_dwordx4 v[32:35], v25, s[36:39], s78 offen sc1
s_mov_b64 exec, -1
v_mfma_f32_16x16x32_bf16 v[44:47], v[80:83], v[228:231], v[44:47]
s_add_u32 m0, s75, 0x8000
s_nop 0
buffer_load_dwordx4 v1, s[36:39], s74 offen offset:3072 sc1 lds
ds_read_b128 v[76:79], v22 offset:33792
s_waitcnt lgkmcnt(4)
v_mfma_f32_16x16x32_bf16 v[40:43], v[84:87], v[232:235], v[40:43]
v_mfma_f32_16x16x32_bf16 v[44:47], v[84:87], v[236:239], v[44:47]
ds_read_b128 v[80:83], v22 offset:34816
s_waitcnt lgkmcnt(4)
v_mfma_f32_16x16x32_bf16 v[40:43], v[88:91], v[240:243], v[40:43]
v_mfma_f32_16x16x32_bf16 v[44:47], v[88:91], v[244:247], v[44:47]
v_mov_b32 v108, 0
ds_read_b128 v[84:87], v22 offset:35840
s_waitcnt lgkmcnt(4)
v_mfma_f32_16x16x32_bf16 v[40:43], v[92:95], v[248:251], v[40:43]
v_mov_b32 v109, 0
v_mov_b32 v110, s60
v_mfma_f32_16x16x32_bf16 v[44:47], v[92:95], v[252:255], v[44:47]
v_mov_b32 v111, s60
ds_read_b128 v[88:91], v22 offset:36864
s_waitcnt lgkmcnt(4)
v_mfma_f32_16x16x32_bf16 v[64:67], v[72:75], v[128:131], 0
v_mfma_f32_16x16x32_bf16 v[68:71], v[72:75], v[132:135], 0
ds_read_b128 v[92:95], v22 offset:37888
s_waitcnt lgkmcnt(4)
v_mfma_f32_16x16x32_bf16 v[64:67], v[76:79], v[136:139], v[64:67]
ds_write_b128 v23, v[44:47]
v_mfma_f32_16x16x32_bf16 v[68:71], v[76:79], v[140:143], v[68:71]
ds_read_b128 v[72:75], v22 offset:38912
s_waitcnt lgkmcnt(5)
v_mfma_f32_16x16x32_bf16 v[64:67], v[80:83], v[144:147], v[64:67]
v_mfma_f32_16x16x32_bf16 v[68:71], v[80:83], v[148:151], v[68:71]
s_cmp_eq_u32 s62, 1
s_cbranch_scc1 .Lfk_rare14
.Lfk_join13:
ds_read_b128 v[76:79], v22 offset:39936
s_waitcnt lgkmcnt(5)
v_mfma_f32_16x16x32_bf16 v[64:67], v[84:87], v[152:155], v[64:67]
s_waitcnt vmcnt(5) lgkmcnt(2)
s_barrier
v_mfma_f32_16x16x32_bf16 v[68:71], v[84:87], v[156:159], v[68:71]
ds_read_b128 v[96:99], v24
s_add_u32 m0, s75, 0x10000
s_add_u32 s74, s74, 0x8000
buffer_load_dwordx4 v1, s[36:39], s74 offen sc1 lds
ds_read_b128 v[80:83], v22 offset:40960
v_mfma_f32_16x16x32_bf16 v[64:67], v[88:91], v[160:163], v[64:67]
v_mfma_f32_16x16x32_bf16 v[68:71], v[88:91], v[164:167], v[68:71]
ds_read_b128 v[84:87], v22 offset:41984
v_mfma_f32_16x16x32_bf16 v[64:67], v[92:95], v[168:171], v[64:67]
v_mfma_f32_16x16x32_bf16 v[68:71], v[92:95], v[172:175], v[68:71]
s_waitcnt lgkmcnt(2)
v_add_f32 v40, v40, v96
ds_read_b128 v[88:91], v22 offset:43008
v_mfma_f32_16x16x32_bf16 v[64:67], v[72:75], v[176:179], v[64:67]
v_add_f32 v41, v41, v97
v_add_f32 v42, v42, v98
v_mfma_f32_16x16x32_bf16 v[68:71], v[72:75], v[180:183], v[68:71]
v_add_f32 v43, v43, v99
s_add_u32 m0, s75, 0x10000
s_nop 0
buffer_load_dwordx4 v1, s[36:39], s74 offen offset:1024 sc1 lds
s_cmp_eq_u32 s68, 1
s_cbranch_scc0 .Lfk_else19
v_lshlrev_b32 v96, 16, v16
v_and_b32 v97, s80, v16
v_lshlrev_b32 v98, 16, v17
v_and_b32 v99, s80, v17
v_add_f32 v40, v40, v96
v_add_f32 v41, v41, v97
v_add_f32 v42, v42, v98
v_add_f32 v43, v43, v99
.Lfk_else19:
ds_read_b128 v[92:95], v22 offset:44032
v_mfma_f32_16x16x32_bf16 v[64:67], v[76:79], v[184:187], v[64:67]
v_cvt_pk_bf16_f32 v36, v40, v41
v_cvt_pk_bf16_f32 v37, v42, v43
v_mfma_f32_16x16x32_bf16 v[68:71], v[76:79], v[188:191], v[68:71]
v_lshrrev_b32 v12, 2, v0
v_lshlrev_b32 v12, 8, v12
v_lshrrev_b32 v13, 2, v0
v_xor_b32 v13, s62, v13
v_and_b32 v13, 7, v13
v_lshlrev_b32 v13, 5, v13
v_add_u32 v12, v12, v13
v_and_b32 v13, 3, v0
v_lshlrev_b32 v13, 3, v13
v_add_u32 v12, v12, v13
v_add_u32 v55, 0x26000, v12
v_min_u32 v36, -2, v36
v_min_u32 v37, -2, v37
ds_read_b128 v[72:75], v22 offset:45056
s_waitcnt lgkmcnt(4)
v_mfma_f32_16x16x32_bf16 v[64:67], v[80:83], v[192:195], v[64:67]
ds_read_b64_tr_b16 v[120:121], v55
ds_read_b64_tr_b16 v[122:123], v55 offset:4096
v_mfma_f32_16x16x32_bf16 v[68:71], v[80:83], v[196:199], v[68:71]
ds_read_b128 v[76:79], v22 offset:46080
s_waitcnt lgkmcnt(6)
v_mfma_f32_16x16x32_bf16 v[64:67], v[84:87], v[200:203], v[64:67]
v_mfma_f32_16x16x32_bf16 v[68:71], v[84:87], v[204:207], v[68:71]
s_add_u32 m0, s75, 0x10000
s_nop 0
buffer_load_dwordx4 v1, s[36:39], s74 offen offset:2048 sc1 lds
ds_read_b128 v[80:83], v22 offset:47104
s_waitcnt lgkmcnt(6)
v_mfma_f32_16x16x32_bf16 v[64:67], v[88:91], v[208:211], v[64:67]
v_mfma_f32_16x16x32_bf16 v[68:71], v[88:91], v[212:215], v[68:71]
s_and_b32 s96, s2, 3
s_lshl_b32 s96, s96, 1
s_lshr_b32 s97, s62, 2
s_add_u32 s96, s96, s97
s_lshl_b32 s96, s96, 3
s_lshr_b32 s97, s2, 2
s_and_b32 s97, s97, 7
s_add_u32 s96, s96, s97
s_mul_i32 s96, s96, 7
s_lshr_b32 s97, s2, 5
s_add_u32 s96, s96, s97
s_lshl_b32 s96, s96, 12
s_add_u32 s96, 0x1800000, s96
s_and_b32 s97, s62, 3
s_lshl_b32 s97, s97, 10
s_add_u32 s78, s96, s97
s_waitcnt lgkmcnt(2)
s_cmp_ge_u32 s2, 224
s_cbranch_scc1 .Lfk_else20
buffer_store_dwordx4 v[120:123], v1, s[36:39], s78 offen sc1
.Lfk_else20:
ds_read_b128 v[84:87], v22 offset:48128
v_mfma_f32_16x16x32_bf16 v[64:67], v[92:95], v[216:219], v[64:67]
v_mfma_f32_16x16x32_bf16 v[68:71], v[92:95], v[220:223], v[68:71]
ds_read_b128 v[88:91], v21
v_mfma_f32_16x16x32_bf16 v[64:67], v[72:75], v[224:227], v[64:67]
v_mov_b32 v40, v32
v_mov_b32 v41, v33
v_mov_b32 v42, v34
v_mfma_f32_16x16x32_bf16 v[68:71], v[72:75], v[228:231], v[68:71]
v_mov_b32 v43, v35
s_add_u32 m0, s75, 0x10000
s_nop 0
buffer_load_dwordx4 v1, s[36:39], s74 offen offset:3072 sc1 lds
s_add_u32 s78, s76, 0x0
s_mov_b64 exec, s[86:87]
buffer_load_dwordx4 v[40:43], v27, s[36:39], s78 offen sc1 nt
s_mov_b64 exec, -1
ds_read_b128 v[92:95], v21 offset:1024
s_waitcnt lgkmcnt(4)
v_mfma_f32_16x16x32_bf16 v[64:67], v[76:79], v[232:235], v[64:67]
v_mfma_f32_16x16x32_bf16 v[68:71], v[76:79], v[236:239], v[68:71]
ds_read_b128 v[72:75], v21 offset:2048
s_waitcnt lgkmcnt(4)
v_mfma_f32_16x16x32_bf16 v[64:67], v[80:83], v[240:243], v[64:67]
v_mfma_f32_16x16x32_bf16 v[68:71], v[80:83], v[244:247], v[68:71]
ds_read_b128 v[76:79], v21 offset:3072
s_waitcnt lgkmcnt(4)
v_mfma_f32_16x16x32_bf16 v[64:67], v[84:87], v[248:251], v[64:67]
v_mfma_f32_16x16x32_bf16 v[68:71], v[84:87], v[252:255], v[68:71]
ds_read_b128 v[80:83], v21 offset:4096
s_waitcnt lgkmcnt(4)
v_mfma_f32_16x16x32_bf16 v[56:59], v[88:91], v[128:131], 0
v_mfma_f32_16x16x32_bf16 v[60:63], v[88:91], v[132:135], 0
ds_read_b128 v[84:87], v21 offset:5120
s_waitcnt lgkmcnt(4)
v_mfma_f32_16x16x32_bf16 v[56:59], v[92:95], v[136:139], v[56:59]
s_nop 0
ds_write_b128 v23, v[68:71] offset:8192
v_mfma_f32_16x16x32_bf16 v[60:63], v[92:95], v[140:143], v[60:63]
ds_read_b128 v[88:91], v21 offset:6144
s_waitcnt lgkmcnt(5)
v_mfma_f32_16x16x32_bf16 v[56:59], v[72:75], v[144:147], v[56:59]
v_mfma_f32_16x16x32_bf16 v[60:63], v[72:75], v[148:151], v[60:63]
ds_read_b128 v[92:95], v21 offset:7168
s_waitcnt lgkmcnt(5)
v_mfma_f32_16x16x32_bf16 v[56:59], v[76:79], v[152:155], v[56:59]
s_waitcnt vmcnt(5) lgkmcnt(2)
s_barrier
v_mfma_f32_16x16x32_bf16 v[60:63], v[76:79], v[156:159], v[60:63]
ds_read_b128 v[96:99], v24 offset:8192
s_cmp_eq_u32 s62, 0
s_cbranch_scc1 .Lfk_rare22
.Lfk_join21:
s_add_u32 m0, s75, 0x18000
s_add_u32 s74, s74, 0x8000
buffer_load_dwordx4 v1, s[36:39], s74 offen sc1 lds
ds_read_b128 v[72:75], v21 offset:8192
v_mfma_f32_16x16x32_bf16 v[56:59], v[80:83], v[160:163], v[56:59]
v_mfma_f32_16x16x32_bf16 v[60:63], v[80:83], v[164:167], v[60:63]
ds_read_b128 v[76:79], v21 offset:9216
v_mfma_f32_16x16x32_bf16 v[56:59], v[84:87], v[168:171], v[56:59]
s_cmp_eq_u32 s62, 2
s_cbranch_scc1 .Lfk_rare24
.Lfk_join23:
v_mfma_f32_16x16x32_bf16 v[60:63], v[84:87], v[172:175], v[60:63]
s_waitcnt lgkmcnt(2)
v_add_f32 v64, v64, v96
ds_read_b128 v[80:83], v21 offset:10240
v_mfma_f32_16x16x32_bf16 v[56:59], v[88:91], v[176:179], v[56:59]
v_add_f32 v65, v65, v97
v_add_f32 v66, v66, v98
v_mfma_f32_16x16x32_bf16 v[60:63], v[88:91], v[180:183], v[60:63]
v_add_f32 v67, v67, v99
s_add_u32 m0, s75, 0x18000
s_nop 0
buffer_load_dwordx4 v1, s[36:39], s74 offen offset:1024 sc1 lds
s_cmp_eq_u32 s68, 1
s_cbranch_scc0 .Lfk_else25
v_lshlrev_b32 v96, 16, v18
v_and_b32 v97, s80, v18
v_lshlrev_b32 v98, 16, v19
v_and_b32 v99, s80, v19
v_add_f32 v64, v64, v96
v_add_f32 v65, v65, v97
v_add_f32 v66, v66, v98
v_add_f32 v67, v67, v99
.Lfk_else25:
ds_read_b128 v[84:87], v21 offset:11264
v_mfma_f32_16x16x32_bf16 v[56:59], v[92:95], v[184:187], v[56:59]
v_cvt_pk_bf16_f32 v38, v64, v65
v_cvt_pk_bf16_f32 v39, v66, v67
v_mfma_f32_16x16x32_bf16 v[60:63], v[92:95], v[188:191], v[60:63]
v_min_u32 v38, -2, v38
v_min_u32 v39, -2, v39
ds_read_b128 v[88:91], v21 offset:12288
s_waitcnt lgkmcnt(4)
v_mfma_f32_16x16x32_bf16 v[56:59], v[72:75], v[192:195], v[56:59]
v_mfma_f32_16x16x32_bf16 v[60:63], v[72:75], v[196:199], v[60:63]
ds_read_b128 v[92:95], v21 offset:13312
s_waitcnt lgkmcnt(4)
v_mfma_f32_16x16x32_bf16 v[56:59], v[76:79], v[200:203], v[56:59]
v_mfma_f32_16x16x32_bf16 v[60:63], v[76:79], v[204:207], v[60:63]
s_add_u32 m0, s75, 0x18000
s_nop 0
buffer_load_dwordx4 v1, s[36:39], s74 offen offset:2048 sc1 lds
ds_read_b128 v[72:75], v21 offset:14336
s_waitcnt lgkmcnt(4)
v_mfma_f32_16x16x32_bf16 v[56:59], v[80:83], v[208:211], v[56:59]
s_waitcnt vmcnt(3)
v_max3_u32 v104, v40, v41, v42
v_max_u32 v104, v104, v43
v_cmp_eq_u32 vcc, -1, v104
s_cmp_lg_u64 vcc, 0
s_cbranch_scc1 .Lfk_rare27
.Lfk_join26:
s_add_u32 s78, s76, 0x8000
s_mov_b64 exec, s[86:87]
buffer_store_dwordx4 v[36:39], v25, s[36:39], s78 offen sc1
s_mov_b64 exec, -1
v_mfma_f32_16x16x32_bf16 v[100:103], v[48:51], v[40:43], 0
v_mfma_f32_16x16x32_bf16 v[60:63], v[80:83], v[212:215], v[60:63]
ds_read_b128 v[76:79], v21 offset:15360
s_waitcnt lgkmcnt(4)
v_mfma_f32_16x16x32_bf16 v[56:59], v[84:87], v[216:219], v[56:59]
s_nop 3
v_cvt_pk_bf16_f32 v104, v100, v101
v_min_u32 v105, -2, v104
v_mfma_f32_16x16x32_bf16 v[60:63], v[84:87], v[220:223], v[60:63]
v_min_u32 v104, -2, v104
v_add_f32 v108, v108, v100
v_fmac_f32 v109, v100, v100
ds_read_b128 v[80:83], v21 offset:32768
s_waitcnt lgkmcnt(4)
v_mfma_f32_16x16x32_bf16 v[56:59], v[88:91], v[224:227], v[56:59]
v_permlane16_swap_b32 v104, v105
v_mov_b32 v106, v104
v_mfma_f32_16x16x32_bf16 v[60:63], v[88:91], v[228:231], v[60:63]
v_mov_b32 v107, v105
v_add_f32 v108, v108, v101
s_add_u32 m0, s75, 0x18000
s_nop 0
buffer_load_dwordx4 v1, s[36:39], s74 offen offset:3072 sc1 lds
v_fmac_f32 v109, v101, v101
ds_read_b128 v[84:87], v21 offset:33792
s_waitcnt lgkmcnt(4)
v_mfma_f32_16x16x32_bf16 v[56:59], v[92:95], v[232:235], v[56:59]
v_permlane32_swap_b32 v104, v106
v_permlane32_swap_b32 v105, v107
s_add_u32 s78, s77, 0x0
s_mov_b64 exec, 0xffff
buffer_store_dwordx4 v[104:107], v1, s[36:39], s78 offen sc1
s_mov_b64 exec, -1
v_mfma_f32_16x16x32_bf16 v[60:63], v[92:95], v[236:239], v[60:63]
ds_read_b128 v[88:91], v21 offset:34816
s_waitcnt lgkmcnt(4)
v_mfma_f32_16x16x32_bf16 v[56:59], v[72:75], v[240:243], v[56:59]
v_mfma_f32_16x16x32_bf16 v[60:63], v[72:75], v[244:247], v[60:63]
ds_read_b128 v[92:95], v21 offset:35840
s_waitcnt lgkmcnt(4)
v_mfma_f32_16x16x32_bf16 v[56:59], v[76:79], v[248:251], v[56:59]
v_mfma_f32_16x16x32_bf16 v[60:63], v[76:79], v[252:255], v[60:63]
ds_read_b128 v[72:75], v21 offset:36864
s_waitcnt lgkmcnt(4)
v_mfma_f32_16x16x32_bf16 v[64:67], v[80:83], v[128:131], 0
v_mfma_f32_16x16x32_bf16 v[68:71], v[80:83], v[132:135], 0
ds_read_b128 v[76:79], v21 offset:37888
s_waitcnt lgkmcnt(4)
v_mfma_f32_16x16x32_bf16 v[64:67], v[84:87], v[136:139], v[64:67]
s_nop 0
ds_write_b128 v23, v[60:63]
v_mfma_f32_16x16x32_bf16 v[68:71], v[84:87], v[140:143], v[68:71]
ds_read_b128 v[80:83], v21 offset:38912
s_waitcnt lgkmcnt(5)
v_mfma_f32_16x16x32_bf16 v[64:67], v[88:91], v[144:147], v[64:67]
v_mfma_f32_16x16x32_bf16 v[68:71], v[88:91], v[148:151], v[68:71]
s_and_b32 s96, s2, 2
s_lshl_b32 s96, s96, 1
s_lshr_b32 s97, s64, 3
s_add_u32 s96, s96, s97
s_lshl_b32 s96, s96, 3
s_add_u32 s96, s96, s62
s_mul_i32 s96, s96, 0x7000
s_add_u32 s31, 0x1800000, s96
s_cmp_eq_u32 s62, 2
s_cbranch_scc1 .Lfk_rare31
.Lfk_join30:
ds_read_b128 v[84:87], v21 offset:39936
s_waitcnt lgkmcnt(5)
v_mfma_f32_16x16x32_bf16 v[64:67], v[92:95], v[152:155], v[64:67]
s_waitcnt lgkmcnt(2)
s_barrier
v_mfma_f32_16x16x32_bf16 v[68:71], v[92:95], v[156:159], v[68:71]
ds_read_b128 v[96:99], v24
s_cmp_eq_u32 s62, 0
s_cbranch_scc1 .Lfk_rare37
.Lfk_join36:
s_add_u32 s78, s30, 0x0
s_add_u32 m0, s89, 0x0
s_nop 0
buffer_load_dwordx4 v20, s[36:39], s78 offen sc1 lds
ds_read_b128 v[88:91], v21 offset:40960
v_mfma_f32_16x16x32_bf16 v[64:67], v[72:75], v[160:163], v[64:67]
v_mfma_f32_16x16x32_bf16 v[68:71], v[72:75], v[164:167], v[68:71]
ds_read_b128 v[92:95], v21 offset:41984
v_mfma_f32_16x16x32_bf16 v[64:67], v[76:79], v[168:171], v[64:67]
s_lshl_b32 s96, s82, 3
s_add_u32 s96, s96, s62
s_lshl_b32 s96, s96, 11
s_add_u32 s78, 0x80000, s96
buffer_load_dwordx4 v[120:123], v1, s[36:39], s78 offen sc1
buffer_load_dwordx4 v[124:127], v1, s[36:39], s78 offen offset:1024 sc1
s_cmp_eq_u32 s62, 1
s_cbranch_scc1 .Lfk_rare39
.Lfk_join38:
v_mfma_f32_16x16x32_bf16 v[68:71], v[76:79], v[172:175], v[68:71]
s_waitcnt lgkmcnt(2)
v_add_f32 v56, v56, v96
ds_read_b128 v[72:75], v21 offset:43008
v_mfma_f32_16x16x32_bf16 v[64:67], v[80:83], v[176:179], v[64:67]
v_add_f32 v57, v57, v97
v_add_f32 v58, v58, v98
v_mfma_f32_16x16x32_bf16 v[68:71], v[80:83], v[180:183], v[68:71]
v_add_f32 v59, v59, v99
s_cmp_eq_u32 s68, 2
s_cbranch_scc0 .Lfk_else40
v_lshlrev_b32 v96, 16, v16
v_and_b32 v97, s80, v16
v_lshlrev_b32 v98, 16, v17
v_and_b32 v99, s80, v17
v_add_f32 v56, v56, v96
v_add_f32 v57, v57, v97
v_add_f32 v58, v58, v98
v_add_f32 v59, v59, v99
.Lfk_else40:
ds_read_b128 v[76:79], v21 offset:44032
v_mfma_f32_16x16x32_bf16 v[64:67], v[84:87], v[184:187], v[64:67]
v_cvt_pk_bf16_f32 v56, v56, v57
v_cvt_pk_bf16_f32 v58, v58, v59
v_mfma_f32_16x16x32_bf16 v[68:71], v[84:87], v[188:191], v[68:71]
s_add_u32 s78, s30, 0x2000
s_add_u32 m0, s89, 0x2000
s_nop 0
buffer_load_dwordx4 v20, s[36:39], s78 offen sc1 lds
v_cmp_eq_u32 vcc, v56, v32
s_nop 1
v_addc_co_u32 v32, vcc, 0, v56, vcc
v_cmp_eq_u32 vcc, v58, v33
s_nop 1
v_addc_co_u32 v33, vcc, 0, v58, vcc
ds_read_b128 v[80:83], v21 offset:45056
s_waitcnt lgkmcnt(4)
v_mfma_f32_16x16x32_bf16 v[64:67], v[88:91], v[192:195], v[64:67]
v_mfma_f32_16x16x32_bf16 v[68:71], v[88:91], v[196:199], v[68:71]
ds_read_b128 v[84:87], v21 offset:46080
s_waitcnt lgkmcnt(4)
v_mfma_f32_16x16x32_bf16 v[64:67], v[92:95], v[200:203], v[64:67]
v_mov_b32 v44, v36
v_mov_b32 v45, v37
v_mov_b32 v46, v38
v_mfma_f32_16x16x32_bf16 v[68:71], v[92:95], v[204:207], v[68:71]
v_mov_b32 v47, v39
s_add_u32 s78, s76, 0x8000
s_mov_b64 exec, s[86:87]
buffer_load_dwordx4 v[44:47], v27, s[36:39], s78 offen sc1 nt
s_mov_b64 exec, -1
ds_read_b128 v[88:91], v21 offset:47104
s_waitcnt lgkmcnt(4)
v_mfma_f32_16x16x32_bf16 v[64:67], v[72:75], v[208:211], v[64:67]
v_mfma_f32_16x16x32_bf16 v[68:71], v[72:75], v[212:215], v[68:71]
ds_read_b128 v[92:95], v21 offset:48128
s_waitcnt lgkmcnt(4)
v_mfma_f32_16x16x32_bf16 v[64:67], v[76:79], v[216:219], v[64:67]
s_add_u32 s78, s30, 0x10000
s_add_u32 m0, s89, 0x4000
s_nop 0
buffer_load_dwordx4 v20, s[36:39], s78 offen sc1 lds
v_mfma_f32_16x16x32_bf16 v[68:71], v[76:79], v[220:223], v[68:71]
ds_read_b128 v[72:75], v22
s_waitcnt lgkmcnt(4)
v_mfma_f32_16x16x32_bf16 v[64:67], v[80:83], v[224:227], v[64:67]
v_mfma_f32_16x16x32_bf16 v[68:71], v[80:83], v[228:231], v[68:71]
ds_read_b128 v[76:79], v22 offset:1024
s_waitcnt lgkmcnt(4)
v_mfma_f32_16x16x32_bf16 v[64:67], v[84:87], v[232:235], v[64:67]
v_mfma_f32_16x16x32_bf16 v[68:71], v[84:87], v[236:239], v[68:71]
ds_read_b128 v[80:83], v22 offset:2048
s_waitcnt lgkmcnt(4)
v_mfma_f32_16x16x32_bf16 v[64:67], v[88:91], v[240:243], v[64:67]
v_mfma_f32_16x16x32_bf16 v[68:71], v[88:91], v[244:247], v[68:71]
ds_read_b128 v[84:87], v22 offset:3072
s_waitcnt lgkmcnt(4)
v_mfma_f32_16x16x32_bf16 v[64:67], v[92:95], v[248:251], v[64:67]
s_add_u32 s78, s30, 0x12000
s_add_u32 m0, s89, 0x6000
s_nop 0
buffer_load_dwordx4 v20, s[36:39], s78 offen sc1 lds
v_mfma_f32_16x16x32_bf16 v[68:71], v[92:95], v[252:255], v[68:71]
ds_read_b128 v[88:91], v22 offset:4096
s_waitcnt lgkmcnt(4)
v_mfma_f32_16x16x32_bf16 v[56:59], v[72:75], v[128:131], 0
v_mfma_f32_16x16x32_bf16 v[60:63], v[72:75], v[132:135], 0
ds_read_b128 v[92:95], v22 offset:5120
s_waitcnt lgkmcnt(4)
v_mfma_f32_16x16x32_bf16 v[56:59], v[76:79], v[136:139], v[56:59]
s_nop 0
ds_write_b128 v23, v[68:71] offset:8192
v_mfma_f32_16x16x32_bf16 v[60:63], v[76:79], v[140:143], v[60:63]
ds_read_b128 v[72:75], v22 offset:6144
s_waitcnt lgkmcnt(5)
v_mfma_f32_16x16x32_bf16 v[56:59], v[80:83], v[144:147], v[56:59]
v_mfma_f32_16x16x32_bf16 v[60:63], v[80:83], v[148:151], v[60:63]
s_cmp_eq_u32 s62, 1
s_cbranch_scc1 .Lfk_rare42
.Lfk_join41:
ds_read_b128 v[76:79], v22 offset:7168
s_waitcnt lgkmcnt(5)
v_mfma_f32_16x16x32_bf16 v[56:59], v[84:87], v[152:155], v[56:59]
s_waitcnt vmcnt(8) lgkmcnt(2)
s_barrier
v_mfma_f32_16x16x32_bf16 v[60:63], v[84:87], v[156:159], v[60:63]
ds_read_b128 v[96:99], v24 offset:8192
ds_read_b128 v[80:83], v22 offset:8192
v_mfma_f32_16x16x32_bf16 v[56:59], v[88:91], v[160:163], v[56:59]
v_mfma_f32_16x16x32_bf16 v[60:63], v[88:91], v[164:167], v[60:63]
ds_read_b128 v[84:87], v22 offset:9216
v_mfma_f32_16x16x32_bf16 v[56:59], v[92:95], v[168:171], v[56:59]
v_mfma_f32_16x16x32_bf16 v[60:63], v[92:95], v[172:175], v[60:63]
s_waitcnt lgkmcnt(2)
v_add_f32 v64, v64, v96
ds_read_b128 v[88:91], v22 offset:10240
v_mfma_f32_16x16x32_bf16 v[56:59], v[72:75], v[176:179], v[56:59]
v_add_f32 v65, v65, v97
v_add_f32 v66, v66, v98
v_mfma_f32_16x16x32_bf16 v[60:63], v[72:75], v[180:183], v[60:63]
v_add_f32 v67, v67, v99
s_cmp_eq_u32 s68, 2
s_cbranch_scc0 .Lfk_else47
v_lshlrev_b32 v96, 16, v18
v_and_b32 v97, s80, v18
v_lshlrev_b32 v98, 16, v19
v_and_b32 v99, s80, v19
v_add_f32 v64, v64, v96
v_add_f32 v65, v65, v97
v_add_f32 v66, v66, v98
v_add_f32 v67, v67, v99
.Lfk_else47:
ds_read_b128 v[92:95], v22 offset:11264
v_mfma_f32_16x16x32_bf16 v[56:59], v[76:79], v[184:187], v[56:59]
v_cvt_pk_bf16_f32 v64, v64, v65
v_cvt_pk_bf16_f32 v66, v66, v67
v_mfma_f32_16x16x32_bf16 v[60:63], v[76:79], v[188:191], v[60:63]
v_cmp_eq_u32 vcc, v64, v34
s_nop 1
v_addc_co_u32 v34, vcc, 0, v64, vcc
s_waitcnt vmcnt(2)
v_max3_u32 v104, v44, v45, v46
v_max_u32 v104, v104, v47
v_cmp_eq_u32 vcc, -1, v104
s_cmp_lg_u64 vcc, 0
s_cbranch_scc1 .Lfk_rare49
.Lfk_join48:
v_cmp_eq_u32 vcc, v66, v35
s_nop 1
v_addc_co_u32 v35, vcc, 0, v66, vcc
ds_read_b128 v[72:75], v22 offset:12288
s_waitcnt lgkmcnt(4)
v_mfma_f32_16x16x32_bf16 v[56:59], v[80:83], v[192:195], v[56:59]
v_mfma_f32_16x16x32_bf16 v[100:103], v[48:51], v[44:47], 0
s_add_u32 s78, s76, 0x0
s_mov_b64 exec, s[86:87]
buffer_store_dwordx4 v[32:35], v25, s[36:39], s78 offen sc1
s_mov_b64 exec, -1
v_mfma_f32_16x16x32_bf16 v[60:63], v[80:83], v[196:199], v[60:63]
ds_read_b128 v[76:79], v22 offset:13312
s_waitcnt lgkmcnt(4)
v_mfma_f32_16x16x32_bf16 v[56:59], v[84:87], v[200:203], v[56:59]
v_cvt_pk_bf16_f32 v104, v100, v101
v_min_u32 v105, -2, v104
v_min_u32 v104, -2, v104
v_mfma_f32_16x16x32_bf16 v[60:63], v[84:87], v[204:207], v[60:63]
v_add_f32 v108, v108, v100
v_fmac_f32 v109, v100, v100
v_permlane16_swap_b32 v104, v105
ds_read_b128 v[80:83], v22 offset:14336
s_waitcnt lgkmcnt(4)
v_mfma_f32_16x16x32_bf16 v[56:59], v[88:91], v[208:211], v[56:59]
v_mov_b32 v106, v104
v_mov_b32 v107, v105
v_mfma_f32_16x16x32_bf16 v[60:63], v[88:91], v[212:215], v[60:63]
v_add_f32 v108, v108, v101
v_fmac_f32 v109, v101, v101
v_permlane32_swap_b32 v104, v106
ds_read_b128 v[84:87], v22 offset:15360
s_waitcnt lgkmcnt(4)
v_mfma_f32_16x16x32_bf16 v[56:59], v[92:95], v[216:219], v[56:59]
v_permlane32_swap_b32 v105, v107
s_add_u32 s78, s77, 0x8000
s_mov_b64 exec, 0xffff
buffer_store_dwordx4 v[104:107], v1, s[36:39], s78 offen sc1
s_mov_b64 exec, -1
v_mfma_f32_16x16x32_bf16 v[60:63], v[92:95], v[220:223], v[60:63]
v_add_f32 v120, v120, v124
v_add_f32 v121, v121, v125
ds_read_b128 v[88:91], v22 offset:32768
s_waitcnt lgkmcnt(4)
v_mfma_f32_16x16x32_bf16 v[56:59], v[72:75], v[224:227], v[56:59]
v_add_f32 v122, v122, v126
v_add_f32 v123, v123, v127
v_mfma_f32_16x16x32_bf16 v[60:63], v[72:75], v[228:231], v[60:63]
v_and_b32 v12, 7, v0
v_lshlrev_b32 v12, 4, v12
v_lshrrev_b32 v13, 4, v0
v_and_b32 v13, 1, v13
v_lshlrev_b32 v13, 3, v13
v_add_u32 v12, v12, v13
v_lshrrev_b32 v13, 5, v0
v_lshlrev_b32 v13, 2, v13
v_add_u32 v12, v12, v13
s_lshl_b32 s96, s62, 7
s_add_u32 s96, s96, 0x27400
v_add_u32 v124, s96, v12
v_permlane32_swap_b32 v120, v121
ds_read_b128 v[92:95], v22 offset:33792
s_waitcnt lgkmcnt(4)
v_mfma_f32_16x16x32_bf16 v[56:59], v[76:79], v[232:235], v[56:59]
v_permlane32_swap_b32 v122, v123
v_add_f32 v120, v120, v121
v_mfma_f32_16x16x32_bf16 v[60:63], v[76:79], v[236:239], v[60:63]
v_add_f32 v122, v122, v123
ds_read_b128 v[72:75], v22 offset:34816
s_waitcnt lgkmcnt(4)
v_mfma_f32_16x16x32_bf16 v[56:59], v[80:83], v[240:243], v[56:59]
v_permlane16_swap_b32 v120, v122
v_add_f32 v120, v120, v122
v_mfma_f32_16x16x32_bf16 v[60:63], v[80:83], v[244:247], v[60:63]
s_nop 0
v_add_f32_dpp v120, v120, v120 row_ror:8 row_mask:0xf bank_mask:0xf
ds_read_b128 v[76:79], v22 offset:35840
s_waitcnt lgkmcnt(4)
v_mfma_f32_16x16x32_bf16 v[56:59], v[84:87], v[248:251], v[56:59]
ds_write_b32 v124, v120
v_mfma_f32_16x16x32_bf16 v[60:63], v[84:87], v[252:255], v[60:63]
ds_read_b128 v[80:83], v22 offset:36864
s_waitcnt lgkmcnt(5)
v_mfma_f32_16x16x32_bf16 v[64:67], v[88:91], v[128:131], 0
s_add_u32 m0, s75, 0x8000
s_add_u32 s78, s29, 0x0
buffer_load_dwordx4 v1, s[36:39], s78 offen sc1 lds
buffer_load_dwordx4 v1, s[36:39], s78 offen offset:1024 sc1 lds
s_add_u32 m0, s75, 0x8800
s_add_u32 s78, s29, 0x1000
buffer_load_dwordx4 v1, s[36:39], s78 offen sc1 lds
buffer_load_dwordx4 v1, s[36:39], s78 offen offset:1024 sc1 lds
v_mfma_f32_16x16x32_bf16 v[68:71], v[88:91], v[132:135], 0
ds_read_b128 v[84:87], v22 offset:37888
s_waitcnt lgkmcnt(5)
v_mfma_f32_16x16x32_bf16 v[64:67], v[92:95], v[136:139], v[64:67]
ds_write_b128 v23, v[60:63]
v_mfma_f32_16x16x32_bf16 v[68:71], v[92:95], v[140:143], v[68:71]
ds_read_b128 v[88:91], v22 offset:38912
s_waitcnt lgkmcnt(6)
v_mfma_f32_16x16x32_bf16 v[64:67], v[72:75], v[144:147], v[64:67]
v_mfma_f32_16x16x32_bf16 v[68:71], v[72:75], v[148:151], v[68:71]
ds_read_b128 v[92:95], v22 offset:39936
s_waitcnt lgkmcnt(6)
v_mfma_f32_16x16x32_bf16 v[64:67], v[76:79], v[152:155], v[64:67]
s_waitcnt lgkmcnt(2)
s_barrier
v_mfma_f32_16x16x32_bf16 v[68:71], v[76:79], v[156:159], v[68:71]
ds_read_b128 v[96:99], v24
s_add_u32 s78, s30, 0x20000
s_add_u32 m0, s89, 0x10000
s_nop 0
buffer_load_dwordx4 v20, s[36:39], s78 offen sc1 lds
ds_read_b128 v[72:75], v22 offset:40960
v_mfma_f32_16x16x32_bf16 v[64:67], v[80:83], v[160:163], v[64:67]
v_mfma_f32_16x16x32_bf16 v[68:71], v[80:83], v[164:167], v[68:71]
s_add_u32 s78, s30, 0x22000
s_add_u32 m0, s89, 0x12000
s_nop 0
buffer_load_dwordx4 v20, s[36:39], s78 offen sc1 lds
ds_read_b128 v[76:79], v22 offset:41984
v_mfma_f32_16x16x32_bf16 v[64:67], v[84:87], v[168:171], v[64:67]
v_mfma_f32_16x16x32_bf16 v[68:71], v[84:87], v[172:175], v[68:71]
s_add_u32 s78, s30, 0x30000
s_add_u32 m0, s89, 0x14000
s_nop 0
buffer_load_dwordx4 v20, s[36:39], s78 offen sc1 lds
s_waitcnt lgkmcnt(2)
v_add_f32 v56, v56, v96
ds_read_b128 v[80:83], v22 offset:43008
v_mfma_f32_16x16x32_bf16 v[64:67], v[88:91], v[176:179], v[64:67]
v_add_f32 v57, v57, v97
v_add_f32 v58, v58, v98
v_mfma_f32_16x16x32_bf16 v[68:71], v[88:91], v[180:183], v[68:71]
v_add_f32 v59, v59, v99
s_add_u32 s78, s30, 0x32000
s_add_u32 m0, s89, 0x16000
s_nop 0
buffer_load_dwordx4 v20, s[36:39], s78 offen sc1 lds
s_cmp_eq_u32 s68, 3
s_cbranch_scc0 .Lfk_else52
v_lshlrev_b32 v96, 16, v16
v_and_b32 v97, s80, v16
v_lshlrev_b32 v98, 16, v17
v_and_b32 v99, s80, v17
v_add_f32 v56, v56, v96
v_add_f32 v57, v57, v97
v_add_f32 v58, v58, v98
v_add_f32 v59, v59, v99
.Lfk_else52:
ds_read_b128 v[84:87], v22 offset:44032
v_mfma_f32_16x16x32_bf16 v[64:67], v[92:95], v[184:187], v[64:67]
v_cvt_pk_bf16_f32 v56, v56, v57
v_cvt_pk_bf16_f32 v58, v58, v59
v_mfma_f32_16x16x32_bf16 v[68:71], v[92:95], v[188:191], v[68:71]
v_cmp_eq_u32 vcc, v56, v36
s_nop 1
v_addc_co_u32 v36, vcc, 0, v56, vcc
v_cmp_eq_u32 vcc, v58, v37
s_nop 1
v_addc_co_u32 v37, vcc, 0, v58, vcc
ds_read_b128 v[88:91], v22 offset:45056
s_waitcnt lgkmcnt(4)
v_mfma_f32_16x16x32_bf16 v[64:67], v[72:75], v[192:195], v[64:67]
v_mfma_f32_16x16x32_bf16 v[68:71], v[72:75], v[196:199], v[68:71]
ds_read_b128 v[92:95], v22 offset:46080
s_waitcnt lgkmcnt(4)
v_mfma_f32_16x16x32_bf16 v[64:67], v[76:79], v[200:203], v[64:67]
v_mfma_f32_16x16x32_bf16 v[68:71], v[76:79], v[204:207], v[68:71]
s_waitcnt vmcnt(4)
ds_read_b128 v[160:163], v5 offset:32768
ds_read_b128 v[164:167], v5 offset:33792
ds_read_b128 v[168:171], v5 offset:34816
ds_read_b128 v[172:175], v5 offset:35840
ds_read_b128 v[72:75], v22 offset:47104
s_waitcnt lgkmcnt(8)
v_mfma_f32_16x16x32_bf16 v[64:67], v[80:83], v[208:211], v[64:67]
s_cmp_eq_u32 s62, 3
s_cbranch_scc1 .Lfk_rare54
.Lfk_join53:
s_add_u32 s78, s76, 0x0
s_mov_b64 exec, s[86:87]
buffer_load_dwordx4 v[32:35], v27, s[36:39], s78 offen sc1
s_mov_b64 exec, -1
v_mfma_f32_16x16x32_bf16 v[68:71], v[80:83], v[212:215], v[68:71]
ds_read_b128 v[76:79], v22 offset:48128
s_waitcnt lgkmcnt(8)
v_mfma_f32_16x16x32_bf16 v[64:67], v[84:87], v[216:219], v[64:67]
s_and_b32 s96, s62, 1
s_lshl_b32 s96, s96, 11
v_add_u32 v29, s96, v1
s_lshr_b32 s96, s62, 1
s_lshl_b32 s96, s96, 13
v_add_u32 v31, s96, v1
v_add_u32 v30, 0x10000, v29
v_add_u32 v4, 0x20000, v31
s_add_u32 m0, s75, 0xc000
s_add_u32 s78, s29, 0x800
buffer_load_dwordx4 v1, s[36:39], s78 offen sc1 lds
buffer_load_dwordx4 v1, s[36:39], s78 offen offset:1024 sc1 lds
s_add_u32 m0, s75, 0xc800
s_add_u32 s78, s29, 0x1800
buffer_load_dwordx4 v1, s[36:39], s78 offen sc1 lds
buffer_load_dwordx4 v1, s[36:39], s78 offen offset:1024 sc1 lds
s_waitcnt lgkmcnt(5)
v_max3_u32 v8, v160, v161, v162
s_waitcnt lgkmcnt(4)
v_max3_u32 v8, v8, v163, v164
v_max3_u32 v8, v8, v165, v166
s_waitcnt lgkmcnt(3)
v_max3_u32 v8, v8, v167, v168
v_max3_u32 v8, v8, v169, v170
s_waitcnt lgkmcnt(2)
v_max3_u32 v8, v8, v171, v172
v_max3_u32 v8, v8, v173, v174
v_max_u32 v8, v8, v175
v_cmp_eq_u32 vcc, -1, v8
s_cmp_lg_u64 vcc, 0
s_cbranch_scc1 .Lfk_rare56
.Lfk_join55:
s_barrier
v_mfma_f32_16x16x32_bf16 v[68:71], v[84:87], v[220:223], v[68:71]
ds_read_b128 v[128:131], v29
ds_read_b128 v[144:147], v31 offset:32768
v_mfma_f32_16x16x32_bf16 v[64:67], v[88:91], v[224:227], v[64:67]
ds_read_b128 v[132:135], v29 offset:8192
v_mfma_f32_16x16x32_bf16 v[68:71], v[88:91], v[228:231], v[68:71]
ds_read_b128 v[136:139], v29 offset:1024
v_mfma_f32_16x16x32_bf16 v[64:67], v[92:95], v[232:235], v[64:67]
ds_read_b128 v[140:143], v29 offset:9216
v_mfma_f32_16x16x32_bf16 v[68:71], v[92:95], v[236:239], v[68:71]
ds_read_b128 v[148:151], v31 offset:34816
s_waitcnt lgkmcnt(7)
v_mfma_f32_16x16x32_bf16 v[64:67], v[72:75], v[240:243], v[64:67]
ds_read_b128 v[152:155], v31 offset:36864
v_mfma_f32_16x16x32_bf16 v[68:71], v[72:75], v[244:247], v[68:71]
ds_read_b128 v[156:159], v31 offset:38912
s_waitcnt lgkmcnt(8)
v_mfma_f32_16x16x32_bf16 v[64:67], v[76:79], v[248:251], v[64:67]
v_mfma_f32_16x16x32_bf16 v[68:71], v[76:79], v[252:255], v[68:71]
s_waitcnt lgkmcnt(6)
v_mfma_f32_16x16x32_bf16 v[192:195], v[128:131], v[144:147], 0
ds_read_b128 v[160:163], v29 offset:16384
s_waitcnt lgkmcnt(6)
v_mfma_f32_16x16x32_bf16 v[196:199], v[132:135], v[144:147], 0
ds_read_b128 v[176:179], v31 offset:33792
s_waitcnt lgkmcnt(6)
v_mfma_f32_16x16x32_bf16 v[200:203], v[136:139], v[144:147], 0
ds_write_b128 v23, v[68:71] offset:16384
ds_read_b128 v[164:167], v29 offset:24576
s_waitcnt lgkmcnt(7)
v_mfma_f32_16x16x32_bf16 v[204:207], v[140:143], v[144:147], 0
ds_read_b128 v[168:171], v29 offset:17408
s_waitcnt lgkmcnt(7)
v_mfma_f32_16x16x32_bf16 v[208:211], v[128:131], v[148:151], 0
ds_read_b128 v[172:175], v29 offset:25600
v_mfma_f32_16x16x32_bf16 v[212:215], v[132:135], v[148:151], 0
ds_read_b128 v[180:183], v31 offset:35840
v_mfma_f32_16x16x32_bf16 v[216:219], v[136:139], v[148:151], 0
s_cmp_eq_u32 s62, 3
s_cbranch_scc1 .Lfk_rare60
.Lfk_join59:
ds_read_b128 v[184:187], v31 offset:37888
v_mfma_f32_16x16x32_bf16 v[220:223], v[140:143], v[148:151], 0
s_waitcnt lgkmcnt(5)
s_barrier
ds_read_b128 v[96:99], v24 offset:16384
ds_read_b128 v[188:191], v31 offset:39936
v_mfma_f32_16x16x32_bf16 v[224:227], v[128:131], v[152:155], 0
s_add_u32 s78, s30, 0x4000
s_add_u32 m0, s89, 0x18000
s_nop 0
buffer_load_dwordx4 v20, s[36:39], s78 offen sc1 lds
v_mfma_f32_16x16x32_bf16 v[228:231], v[132:135], v[152:155], 0
v_mfma_f32_16x16x32_bf16 v[232:235], v[136:139], v[152:155], 0
s_waitcnt lgkmcnt(1)
v_add_f32 v64, v64, v96
v_add_f32 v65, v65, v97
s_add_u32 s78, s30, 0x6000
s_add_u32 m0, s89, 0x1a000
s_nop 0
buffer_load_dwordx4 v20, s[36:39], s78 offen sc1 lds
v_add_f32 v66, v66, v98
v_mfma_f32_16x16x32_bf16 v[236:239], v[140:143], v[152:155], 0
v_add_f32 v67, v67, v99
s_cmp_eq_u32 s68, 3
s_cbranch_scc0 .Lfk_else65
v_lshlrev_b32 v96, 16, v18
v_and_b32 v97, s80, v18
v_lshlrev_b32 v98, 16, v19
v_and_b32 v99, s80, v19
v_add_f32 v64, v64, v96
v_add_f32 v65, v65, v97
v_add_f32 v66, v66, v98
v_add_f32 v67, v67, v99
.Lfk_else65:
v_cvt_pk_bf16_f32 v64, v64, v65
v_mfma_f32_16x16x32_bf16 v[240:243], v[128:131], v[156:159], 0
v_cvt_pk_bf16_f32 v66, v66, v67
v_cmp_eq_u32 vcc, v64, v38
s_nop 1
v_addc_co_u32 v38, vcc, 0, v64, vcc
v_cmp_eq_u32 vcc, v66, v39
s_nop 1
v_addc_co_u32 v39, vcc, 0, v66, vcc
v_mfma_f32_16x16x32_bf16 v[244:247], v[132:135], v[156:159], 0
s_add_u32 s78, s30, 0x14000
s_add_u32 m0, s89, 0x1c000
s_nop 0
buffer_load_dwordx4 v20, s[36:39], s78 offen sc1 lds
v_mfma_f32_16x16x32_bf16 v[248:251], v[136:139], v[156:159], 0
v_mfma_f32_16x16x32_bf16 v[252:255], v[140:143], v[156:159], 0
s_add_u32 s78, s30, 0x16000
s_add_u32 m0, s89, 0x1e000
s_nop 0
buffer_load_dwordx4 v20, s[36:39], s78 offen sc1 lds
v_mfma_f32_16x16x32_bf16 v[192:195], v[160:163], v[176:179], v[192:195]
s_setprio 3
s_add_u32 m0, s75, 0x20000
s_add_u32 s78, s29, 0x8000
buffer_load_dwordx4 v1, s[36:39], s78 offen sc1 lds
buffer_load_dwordx4 v1, s[36:39], s78 offen offset:1024 sc1 lds
s_add_u32 m0, s75, 0x20800
s_add_u32 s78, s29, 0x9000
buffer_load_dwordx4 v1, s[36:39], s78 offen sc1 lds
buffer_load_dwordx4 v1, s[36:39], s78 offen offset:1024 sc1 lds
s_waitcnt vmcnt(8)
ds_read_b128 v[128:131], v5 offset:49152
ds_read_b128 v[132:135], v5 offset:50176
ds_read_b128 v[136:139], v5 offset:51200
ds_read_b128 v[140:143], v5 offset:52224
v_cmp_eq_u32 vcc, v32, v40
v_cmp_eq_u32 s[100:101], v33, v41
s_or_b64 vcc, vcc, s[100:101]
v_cmp_eq_u32 s[100:101], v34, v42
s_or_b64 vcc, vcc, s[100:101]
v_cmp_eq_u32 s[100:101], v35, v43
s_or_b64 vcc, vcc, s[100:101]
s_cbranch_scc1 .Lfk_rare67
.Lfk_join66:
v_mfma_f32_16x16x32_bf16 v[100:103], v[48:51], v[32:35], 0
s_nop 7
v_cvt_pk_bf16_f32 v104, v100, v101
v_min_u32 v105, -2, v104
v_min_u32 v104, -2, v104
v_add_f32 v108, v108, v100
v_fmac_f32 v109, v100, v100
v_permlane16_swap_b32 v104, v105
v_mov_b32 v106, v104
v_mov_b32 v107, v105
v_add_f32 v108, v108, v101
v_fmac_f32 v109, v101, v101
v_permlane32_swap_b32 v104, v106
v_permlane32_swap_b32 v105, v107
s_add_u32 s78, s77, 0x10000
s_mov_b64 exec, 0xffff
buffer_store_dwordx4 v[104:107], v1, s[36:39], s78 offen sc1
s_mov_b64 exec, -1
s_add_u32 s78, s76, 0x8000
s_mov_b64 exec, s[86:87]
buffer_store_dwordx4 v[36:39], v25, s[36:39], s78 offen sc1
s_mov_b64 exec, -1
s_add_u32 s78, s31, 0x0
buffer_load_dwordx4 v[32:35], v1, s[36:39], s78 offen sc1
s_add_u32 s78, s31, 0x0
buffer_load_dwordx4 v[56:59], v1, s[36:39], s78 offen offset:2048 sc1
s_waitcnt lgkmcnt(3)
v_max3_u32 v8, v128, v129, v130
s_waitcnt lgkmcnt(2)
v_max3_u32 v8, v8, v131, v132
v_max3_u32 v8, v8, v133, v134
s_waitcnt lgkmcnt(1)
v_max3_u32 v8, v8, v135, v136
v_max3_u32 v8, v8, v137, v138
s_waitcnt lgkmcnt(0)
v_max3_u32 v8, v8, v139, v140
v_max3_u32 v8, v8, v141, v142
v_max_u32 v8, v8, v143
v_cmp_eq_u32 vcc, -1, v8
s_cmp_lg_u64 vcc, 0
s_cbranch_scc1 .Lfk_rare71
.Lfk_join70:
s_setprio 1
s_barrier
ds_read_b128 v[128:131], v30
v_mfma_f32_16x16x32_bf16 v[196:199], v[164:167], v[176:179], v[196:199]
ds_read_b128 v[144:147], v31 offset:49152
v_mfma_f32_16x16x32_bf16 v[200:203], v[168:171], v[176:179], v[200:203]
ds_read_b128 v[132:135], v30 offset:8192
v_mfma_f32_16x16x32_bf16 v[204:207], v[172:175], v[176:179], v[204:207]
ds_read_b128 v[136:139], v30 offset:1024
v_mfma_f32_16x16x32_bf16 v[208:211], v[160:163], v[180:183], v[208:211]
ds_read_b128 v[140:143], v30 offset:9216
v_mfma_f32_16x16x32_bf16 v[212:215], v[164:167], v[180:183], v[212:215]
ds_read_b128 v[148:151], v31 offset:51200
v_mfma_f32_16x16x32_bf16 v[216:219], v[168:171], v[180:183], v[216:219]
ds_read_b128 v[152:155], v31 offset:53248
v_mfma_f32_16x16x32_bf16 v[220:223], v[172:175], v[180:183], v[220:223]
ds_read_b128 v[156:159], v31 offset:55296
v_mfma_f32_16x16x32_bf16 v[224:227], v[160:163], v[184:187], v[224:227]
v_mfma_f32_16x16x32_bf16 v[228:231], v[164:167], v[184:187], v[228:231]
v_mfma_f32_16x16x32_bf16 v[232:235], v[168:171], v[184:187], v[232:235]
v_mfma_f32_16x16x32_bf16 v[236:239], v[172:175], v[184:187], v[236:239]
s_lshl_b32 s78, s2, 3
s_add_u32 s78, s78, s62
s_cmp_eq_u32 s78, 0
s_cbranch_scc1 .Lfk_rare75
.Lfk_join74:
v_mfma_f32_16x16x32_bf16 v[240:243], v[160:163], v[188:191], v[240:243]
v_mfma_f32_16x16x32_bf16 v[244:247], v[164:167], v[188:191], v[244:247]
v_mfma_f32_16x16x32_bf16 v[248:251], v[168:171], v[188:191], v[248:251]
v_mfma_f32_16x16x32_bf16 v[252:255], v[172:175], v[188:191], v[252:255]
s_waitcnt lgkmcnt(6)
v_mfma_f32_16x16x32_bf16 v[192:195], v[128:131], v[144:147], v[192:195]
ds_read_b128 v[160:163], v30 offset:16384
s_waitcnt lgkmcnt(6)
v_mfma_f32_16x16x32_bf16 v[196:199], v[132:135], v[144:147], v[196:199]
ds_read_b128 v[176:179], v31 offset:50176
s_waitcnt lgkmcnt(6)
v_mfma_f32_16x16x32_bf16 v[200:203], v[136:139], v[144:147], v[200:203]
ds_read_b128 v[164:167], v30 offset:24576
s_waitcnt lgkmcnt(6)
v_mfma_f32_16x16x32_bf16 v[204:207], v[140:143], v[144:147], v[204:207]
ds_read_b128 v[168:171], v30 offset:17408
s_waitcnt lgkmcnt(6)
v_mfma_f32_16x16x32_bf16 v[208:211], v[128:131], v[148:151], v[208:211]
ds_read_b128 v[172:175], v30 offset:25600
v_mfma_f32_16x16x32_bf16 v[212:215], v[132:135], v[148:151], v[212:215]
ds_read_b128 v[180:183], v31 offset:52224
v_mfma_f32_16x16x32_bf16 v[216:219], v[136:139], v[148:151], v[216:219]
ds_read_b128 v[184:187], v31 offset:54272
v_mfma_f32_16x16x32_bf16 v[220:223], v[140:143], v[148:151], v[220:223]
ds_read_b128 v[188:191], v31 offset:56320
s_waitcnt lgkmcnt(9)
v_mfma_f32_16x16x32_bf16 v[224:227], v[128:131], v[152:155], v[224:227]
v_mfma_f32_16x16x32_bf16 v[228:231], v[132:135], v[152:155], v[228:231]
v_mfma_f32_16x16x32_bf16 v[232:235], v[136:139], v[152:155], v[232:235]
v_mfma_f32_16x16x32_bf16 v[236:239], v[140:143], v[152:155], v[236:239]
s_waitcnt lgkmcnt(8)
v_mfma_f32_16x16x32_bf16 v[240:243], v[128:131], v[156:159], v[240:243]
v_mfma_f32_16x16x32_bf16 v[244:247], v[132:135], v[156:159], v[244:247]
v_mfma_f32_16x16x32_bf16 v[248:251], v[136:139], v[156:159], v[248:251]
v_mfma_f32_16x16x32_bf16 v[252:255], v[140:143], v[156:159], v[252:255]
s_waitcnt lgkmcnt(6)
v_mfma_f32_16x16x32_bf16 v[192:195], v[160:163], v[176:179], v[192:195]
s_setprio 3
s_add_u32 s78, s76, 0x8000
s_mov_b64 exec, s[86:87]
buffer_load_dwordx4 v[36:39], v27, s[36:39], s78 offen sc1
s_mov_b64 exec, -1
s_add_u32 m0, s75, 0x8000
s_add_u32 s78, s29, 0x8800
buffer_load_dwordx4 v1, s[36:39], s78 offen sc1 lds
buffer_load_dwordx4 v1, s[36:39], s78 offen offset:1024 sc1 lds
s_add_u32 m0, s75, 0x8800
s_add_u32 s78, s29, 0x9800
buffer_load_dwordx4 v1, s[36:39], s78 offen sc1 lds
buffer_load_dwordx4 v1, s[36:39], s78 offen offset:1024 sc1 lds
s_waitcnt vmcnt(9)
ds_read_b128 v[128:131], v6
ds_read_b128 v[132:135], v6 offset:1024
ds_read_b128 v[136:139], v6 offset:2048
ds_read_b128 v[140:143], v6 offset:3072
s_add_u32 s78, s31, 0x0
buffer_load_dwordx4 v[60:63], v1, s[36:39], s78 offen offset:3072 sc1
s_add_u32 s78, s31, 0x1000
buffer_load_dwordx4 v[72:75], v1, s[36:39], s78 offen sc1
s_waitcnt lgkmcnt(3)
v_max3_u32 v8, v128, v129, v130
s_waitcnt lgkmcnt(2)
v_max3_u32 v8, v8, v131, v132
v_max3_u32 v8, v8, v133, v134
s_waitcnt lgkmcnt(1)
v_max3_u32 v8, v8, v135, v136
v_max3_u32 v8, v8, v137, v138
s_waitcnt lgkmcnt(0)
v_max3_u32 v8, v8, v139, v140
v_max3_u32 v8, v8, v141, v142
v_max_u32 v8, v8, v143
v_cmp_eq_u32 vcc, -1, v8
s_cmp_lg_u64 vcc, 0
s_cbranch_scc1 .Lfk_rare77
.Lfk_join76:
s_setprio 1
s_barrier
ds_read_b128 v[128:131], v30 offset:32768
v_mfma_f32_16x16x32_bf16 v[196:199], v[164:167], v[176:179], v[196:199]
ds_read_b128 v[144:147], v4
v_mfma_f32_16x16x32_bf16 v[200:203], v[168:171], v[176:179], v[200:203]
ds_read_b128 v[132:135], v30 offset:40960
v_mfma_f32_16x16x32_bf16 v[204:207], v[172:175], v[176:179], v[204:207]
ds_read_b128 v[136:139], v30 offset:33792
v_mfma_f32_16x16x32_bf16 v[208:211], v[160:163], v[180:183], v[208:211]
ds_read_b128 v[140:143], v30 offset:41984
v_mfma_f32_16x16x32_bf16 v[212:215], v[164:167], v[180:183], v[212:215]
ds_read_b128 v[148:151], v4 offset:2048
v_mfma_f32_16x16x32_bf16 v[216:219], v[168:171], v[180:183], v[216:219]
ds_read_b128 v[152:155], v4 offset:4096
v_mfma_f32_16x16x32_bf16 v[220:223], v[172:175], v[180:183], v[220:223]
ds_read_b128 v[156:159], v4 offset:6144
v_mfma_f32_16x16x32_bf16 v[224:227], v[160:163], v[184:187], v[224:227]
v_mfma_f32_16x16x32_bf16 v[228:231], v[164:167], v[184:187], v[228:231]
v_mfma_f32_16x16x32_bf16 v[232:235], v[168:171], v[184:187], v[232:235]
v_mfma_f32_16x16x32_bf16 v[236:239], v[172:175], v[184:187], v[236:239]
v_mfma_f32_16x16x32_bf16 v[240:243], v[160:163], v[188:191], v[240:243]
v_mfma_f32_16x16x32_bf16 v[244:247], v[164:167], v[188:191], v[244:247]
v_mfma_f32_16x16x32_bf16 v[248:251], v[168:171], v[188:191], v[248:251]
v_mfma_f32_16x16x32_bf16 v[252:255], v[172:175], v[188:191], v[252:255]
s_waitcnt lgkmcnt(6)
v_mfma_f32_16x16x32_bf16 v[192:195], v[128:131], v[144:147], v[192:195]
ds_read_b128 v[160:163], v30 offset:49152
s_waitcnt lgkmcnt(6)
v_mfma_f32_16x16x32_bf16 v[196:199], v[132:135], v[144:147], v[196:199]
ds_read_b128 v[176:179], v4 offset:1024
s_waitcnt lgkmcnt(6)
v_mfma_f32_16x16x32_bf16 v[200:203], v[136:139], v[144:147], v[200:203]
ds_read_b128 v[164:167], v30 offset:57344
s_waitcnt lgkmcnt(6)
v_mfma_f32_16x16x32_bf16 v[204:207], v[140:143], v[144:147], v[204:207]
ds_read_b128 v[168:171], v30 offset:50176
s_waitcnt lgkmcnt(6)
v_mfma_f32_16x16x32_bf16 v[208:211], v[128:131], v[148:151], v[208:211]
ds_read_b128 v[172:175], v30 offset:58368
v_mfma_f32_16x16x32_bf16 v[212:215], v[132:135], v[148:151], v[212:215]
ds_read_b128 v[180:183], v4 offset:3072
v_mfma_f32_16x16x32_bf16 v[216:219], v[136:139], v[148:151], v[216:219]
ds_read_b128 v[184:187], v4 offset:5120
v_mfma_f32_16x16x32_bf16 v[220:223], v[140:143], v[148:151], v[220:223]
ds_read_b128 v[188:191], v4 offset:7168
s_waitcnt lgkmcnt(9)
v_mfma_f32_16x16x32_bf16 v[224:227], v[128:131], v[152:155], v[224:227]
v_mfma_f32_16x16x32_bf16 v[228:231], v[132:135], v[152:155], v[228:231]
v_mfma_f32_16x16x32_bf16 v[232:235], v[136:139], v[152:155], v[232:235]
v_mfma_f32_16x16x32_bf16 v[236:239], v[140:143], v[152:155], v[236:239]
s_waitcnt lgkmcnt(8)
v_mfma_f32_16x16x32_bf16 v[240:243], v[128:131], v[156:159], v[240:243]
v_mfma_f32_16x16x32_bf16 v[244:247], v[132:135], v[156:159], v[244:247]
v_mfma_f32_16x16x32_bf16 v[248:251], v[136:139], v[156:159], v[248:251]
v_mfma_f32_16x16x32_bf16 v[252:255], v[140:143], v[156:159], v[252:255]
s_waitcnt lgkmcnt(6)
v_mfma_f32_16x16x32_bf16 v[192:195], v[160:163], v[176:179], v[192:195]
s_setprio 3
s_waitcnt vmcnt(6)
v_cmp_eq_u32 vcc, v36, v44
v_cmp_eq_u32 s[100:101], v37, v45
s_or_b64 vcc, vcc, s[100:101]
v_cmp_eq_u32 s[100:101], v38, v46
s_or_b64 vcc, vcc, s[100:101]
v_cmp_eq_u32 s[100:101], v39, v47
s_or_b64 vcc, vcc, s[100:101]
s_cbranch_scc1 .Lfk_rare81
.Lfk_join80:
v_mfma_f32_16x16x32_bf16 v[100:103], v[48:51], v[36:39], 0
s_nop 7
v_cvt_pk_bf16_f32 v104, v100, v101
v_min_u32 v105, -2, v104
v_min_u32 v104, -2, v104
v_add_f32 v108, v108, v100
v_fmac_f32 v109, v100, v100
v_permlane16_swap_b32 v104, v105
v_mov_b32 v106, v104
v_mov_b32 v107, v105
v_add_f32 v108, v108, v101
v_fmac_f32 v109, v101, v101
v_permlane32_swap_b32 v104, v106
v_permlane32_swap_b32 v105, v107
s_add_u32 s78, s77, 0x18000
s_mov_b64 exec, 0xffff
buffer_store_dwordx4 v[104:107], v1, s[36:39], s78 offen sc1
s_mov_b64 exec, -1
v_permlane32_swap_b32 v108, v109
v_add_f32 v108, v108, v109
v_mov_b32 v109, v108
s_nop 1
v_permlane16_swap_b32 v108, v109
v_add_f32 v108, v108, v109
v_mov_b32 v109, v108
s_nop 1
v_permlane32_swap_b32 v108, v109
s_lshl_b32 s96, s69, 3
s_add_u32 s96, s96, s88
s_lshl_b32 s96, s96, 4
s_lshl_b32 s97, s67, 2
s_add_u32 s96, s96, s97
s_add_u32 s96, s96, s68
s_lshl_b32 s96, s96, 8
s_add_u32 s78, 0xe00000, s96
s_mov_b64 exec, 0xffff
buffer_store_dwordx4 v[108:111], v1, s[36:39], s78 offen sc1
s_mov_b64 exec, -1
s_add_u32 m0, s75, 0xc000
s_add_u32 s78, s29, 0x10000
buffer_load_dwordx4 v1, s[36:39], s78 offen sc1 lds
buffer_load_dwordx4 v1, s[36:39], s78 offen offset:1024 sc1 lds
s_add_u32 m0, s75, 0xc800
s_add_u32 s78, s29, 0x11000
buffer_load_dwordx4 v1, s[36:39], s78 offen sc1 lds
buffer_load_dwordx4 v1, s[36:39], s78 offen offset:1024 sc1 lds
s_waitcnt vmcnt(8)
ds_read_b128 v[128:131], v5 offset:32768
ds_read_b128 v[132:135], v5 offset:33792
ds_read_b128 v[136:139], v5 offset:34816
ds_read_b128 v[140:143], v5 offset:35840
s_add_u32 s78, s31, 0x0
buffer_load_dwordx4 v[36:39], v1, s[36:39], s78 offen offset:1024 sc1
s_add_u32 s78, s31, 0x1000
buffer_load_dwordx4 v[76:79], v1, s[36:39], s78 offen offset:1024 sc1
s_waitcnt lgkmcnt(3)
v_max3_u32 v8, v128, v129, v130
s_waitcnt lgkmcnt(2)
v_max3_u32 v8, v8, v131, v132
v_max3_u32 v8, v8, v133, v134
s_waitcnt lgkmcnt(1)
v_max3_u32 v8, v8, v135, v136
v_max3_u32 v8, v8, v137, v138
s_waitcnt lgkmcnt(0)
v_max3_u32 v8, v8, v139, v140
v_max3_u32 v8, v8, v141, v142
v_max_u32 v8, v8, v143
v_cmp_eq_u32 vcc, -1, v8
s_cmp_lg_u64 vcc, 0
s_cbranch_scc1 .Lfk_rare85
.Lfk_join84:
s_setprio 1
s_barrier
ds_read_b128 v[128:131], v29
v_mfma_f32_16x16x32_bf16 v[196:199], v[164:167], v[176:179], v[196:199]
ds_read_b128 v[144:147], v31 offset:32768
v_mfma_f32_16x16x32_bf16 v[200:203], v[168:171], v[176:179], v[200:203]
ds_read_b128 v[132:135], v29 offset:8192
v_mfma_f32_16x16x32_bf16 v[204:207], v[172:175], v[176:179], v[204:207]
ds_read_b128 v[136:139], v29 offset:1024
v_mfma_f32_16x16x32_bf16 v[208:211], v[160:163], v[180:183], v[208:211]
ds_read_b128 v[140:143], v29 offset:9216
v_mfma_f32_16x16x32_bf16 v[212:215], v[164:167], v[180:183], v[212:215]
ds_read_b128 v[148:151], v31 offset:34816
v_mfma_f32_16x16x32_bf16 v[216:219], v[168:171], v[180:183], v[216:219]
ds_read_b128 v[152:155], v31 offset:36864
v_mfma_f32_16x16x32_bf16 v[220:223], v[172:175], v[180:183], v[220:223]
ds_read_b128 v[156:159], v31 offset:38912
v_mfma_f32_16x16x32_bf16 v[224:227], v[160:163], v[184:187], v[224:227]
v_mfma_f32_16x16x32_bf16 v[228:231], v[164:167], v[184:187], v[228:231]
v_mfma_f32_16x16x32_bf16 v[232:235], v[168:171], v[184:187], v[232:235]
v_mfma_f32_16x16x32_bf16 v[236:239], v[172:175], v[184:187], v[236:239]
v_mfma_f32_16x16x32_bf16 v[240:243], v[160:163], v[188:191], v[240:243]
v_mfma_f32_16x16x32_bf16 v[244:247], v[164:167], v[188:191], v[244:247]
v_mfma_f32_16x16x32_bf16 v[248:251], v[168:171], v[188:191], v[248:251]
v_mfma_f32_16x16x32_bf16 v[252:255], v[172:175], v[188:191], v[252:255]
s_waitcnt lgkmcnt(6)
v_mfma_f32_16x16x32_bf16 v[192:195], v[128:131], v[144:147], v[192:195]
ds_read_b128 v[160:163], v29 offset:16384
s_waitcnt lgkmcnt(6)
v_mfma_f32_16x16x32_bf16 v[196:199], v[132:135], v[144:147], v[196:199]
ds_read_b128 v[176:179], v31 offset:33792
s_waitcnt lgkmcnt(6)
v_mfma_f32_16x16x32_bf16 v[200:203], v[136:139], v[144:147], v[200:203]
ds_read_b128 v[164:167], v29 offset:24576
s_waitcnt lgkmcnt(6)
v_mfma_f32_16x16x32_bf16 v[204:207], v[140:143], v[144:147], v[204:207]
ds_read_b128 v[168:171], v29 offset:17408
s_waitcnt lgkmcnt(6)
v_mfma_f32_16x16x32_bf16 v[208:211], v[128:131], v[148:151], v[208:211]
ds_read_b128 v[172:175], v29 offset:25600
v_mfma_f32_16x16x32_bf16 v[212:215], v[132:135], v[148:151], v[212:215]
ds_read_b128 v[180:183], v31 offset:35840
v_mfma_f32_16x16x32_bf16 v[216:219], v[136:139], v[148:151], v[216:219]
ds_read_b128 v[184:187], v31 offset:37888
v_mfma_f32_16x16x32_bf16 v[220:223], v[140:143], v[148:151], v[220:223]
ds_read_b128 v[188:191], v31 offset:39936
s_waitcnt lgkmcnt(9)
v_mfma_f32_16x16x32_bf16 v[224:227], v[128:131], v[152:155], v[224:227]
v_mfma_f32_16x16x32_bf16 v[228:231], v[132:135], v[152:155], v[228:231]
v_mfma_f32_16x16x32_bf16 v[232:235], v[136:139], v[152:155], v[232:235]
v_mfma_f32_16x16x32_bf16 v[236:239], v[140:143], v[152:155], v[236:239]
s_waitcnt lgkmcnt(8)
v_mfma_f32_16x16x32_bf16 v[240:243], v[128:131], v[156:159], v[240:243]
v_mfma_f32_16x16x32_bf16 v[244:247], v[132:135], v[156:159], v[244:247]
v_mfma_f32_16x16x32_bf16 v[248:251], v[136:139], v[156:159], v[248:251]
v_mfma_f32_16x16x32_bf16 v[252:255], v[140:143], v[156:159], v[252:255]
s_waitcnt lgkmcnt(6)
v_mfma_f32_16x16x32_bf16 v[192:195], v[160:163], v[176:179], v[192:195]
s_setprio 3
s_add_u32 m0, s75, 0x20000
s_add_u32 s78, s29, 0x10800
buffer_load_dwordx4 v1, s[36:39], s78 offen sc1 lds
buffer_load_dwordx4 v1, s[36:39], s78 offen offset:1024 sc1 lds
s_add_u32 m0, s75, 0x20800
s_add_u32 s78, s29, 0x11800
buffer_load_dwordx4 v1, s[36:39], s78 offen sc1 lds
buffer_load_dwordx4 v1, s[36:39], s78 offen offset:1024 sc1 lds
s_waitcnt vmcnt(6)
ds_read_b128 v[128:131], v5 offset:49152
ds_read_b128 v[132:135], v5 offset:50176
ds_read_b128 v[136:139], v5 offset:51200
ds_read_b128 v[140:143], v5 offset:52224
s_add_u32 s78, s31, 0x1000
buffer_load_dwordx4 v[80:83], v1, s[36:39], s78 offen offset:2048 sc1
s_add_u32 s78, s31, 0x1000
buffer_load_dwordx4 v[84:87], v1, s[36:39], s78 offen offset:3072 sc1
s_waitcnt lgkmcnt(3)
v_max3_u32 v8, v128, v129, v130
s_waitcnt lgkmcnt(2)
v_max3_u32 v8, v8, v131, v132
v_max3_u32 v8, v8, v133, v134
s_waitcnt lgkmcnt(1)
v_max3_u32 v8, v8, v135, v136
v_max3_u32 v8, v8, v137, v138
s_waitcnt lgkmcnt(0)
v_max3_u32 v8, v8, v139, v140
v_max3_u32 v8, v8, v141, v142
v_max_u32 v8, v8, v143
v_cmp_eq_u32 vcc, -1, v8
s_cmp_lg_u64 vcc, 0
s_cbranch_scc1 .Lfk_rare89
.Lfk_join88:
s_setprio 1
s_barrier
ds_read_b128 v[128:131], v30
v_mfma_f32_16x16x32_bf16 v[196:199], v[164:167], v[176:179], v[196:199]
ds_read_b128 v[144:147], v31 offset:49152
v_mfma_f32_16x16x32_bf16 v[200:203], v[168:171], v[176:179], v[200:203]
s_lshl_b32 s96, s83, 3
s_lshr_b32 s97, s62, 1
s_lshl_b32 s97, s97, 2
s_add_u32 s96, s96, s97
s_lshl_b32 s96, s96, 3
s_lshr_b32 s97, s82, 1
s_add_u32 s96, s96, s97
s_mul_i32 s96, s96, 0x1c00
s_add_u32 s96, 0x700000, s96
s_and_b32 s97, s82, 1
s_mul_i32 s97, s97, 3
s_and_b32 s98, s62, 1
s_add_u32 s97, s97, s98
s_lshl_b32 s97, s97, 10
s_add_u32 s32, s96, s97
ds_read_b128 v[132:135], v30 offset:8192
v_mfma_f32_16x16x32_bf16 v[204:207], v[172:175], v[176:179], v[204:207]
ds_read_b128 v[136:139], v30 offset:1024
v_mfma_f32_16x16x32_bf16 v[208:211], v[160:163], v[180:183], v[208:211]
ds_read_b128 v[140:143], v30 offset:9216
v_mfma_f32_16x16x32_bf16 v[212:215], v[164:167], v[180:183], v[212:215]
ds_read_b128 v[148:151], v31 offset:51200
v_mfma_f32_16x16x32_bf16 v[216:219], v[168:171], v[180:183], v[216:219]
ds_read_b128 v[152:155], v31 offset:53248
v_mfma_f32_16x16x32_bf16 v[220:223], v[172:175], v[180:183], v[220:223]
ds_read_b128 v[156:159], v31 offset:55296
v_mfma_f32_16x16x32_bf16 v[224:227], v[160:163], v[184:187], v[224:227]
v_lshrrev_b32 v12, 4, v0
v_lshlrev_b32 v12, 5, v12
s_and_b32 s96, s62, 1
s_lshl_b32 s96, s96, 8
v_add_u32 v12, s96, v12
v_add_u32 v20, 0x27400, v12
v_mfma_f32_16x16x32_bf16 v[228:231], v[164:167], v[184:187], v[228:231]
v_mfma_f32_16x16x32_bf16 v[232:235], v[168:171], v[184:187], v[232:235]
v_mfma_f32_16x16x32_bf16 v[236:239], v[172:175], v[184:187], v[236:239]
v_mfma_f32_16x16x32_bf16 v[240:243], v[160:163], v[188:191], v[240:243]
v_lshlrev_b32 v12, 1, v3
s_lshr_b32 s96, s62, 1
s_lshl_b32 s96, s96, 10
v_add_u32 v12, s96, v12
v_add_u32 v21, 0x27800, v12
v_mfma_f32_16x16x32_bf16 v[244:247], v[164:167], v[188:191], v[244:247]
v_mfma_f32_16x16x32_bf16 v[248:251], v[168:171], v[188:191], v[248:251]
v_mfma_f32_16x16x32_bf16 v[252:255], v[172:175], v[188:191], v[252:255]
s_waitcnt lgkmcnt(6)
v_mfma_f32_16x16x32_bf16 v[192:195], v[128:131], v[144:147], v[192:195]
ds_read_b128 v[160:163], v30 offset:16384
s_waitcnt lgkmcnt(6)
v_mfma_f32_16x16x32_bf16 v[196:199], v[132:135], v[144:147], v[196:199]
ds_read_b128 v[176:179], v31 offset:50176
s_waitcnt lgkmcnt(6)
v_mfma_f32_16x16x32_bf16 v[200:203], v[136:139], v[144:147], v[200:203]
ds_read_b128 v[164:167], v30 offset:24576
s_waitcnt lgkmcnt(6)
v_mfma_f32_16x16x32_bf16 v[204:207], v[140:143], v[144:147], v[204:207]
ds_read_b128 v[168:171], v30 offset:17408
s_waitcnt lgkmcnt(6)
v_mfma_f32_16x16x32_bf16 v[208:211], v[128:131], v[148:151], v[208:211]
ds_read_b128 v[172:175], v30 offset:25600
v_mfma_f32_16x16x32_bf16 v[212:215], v[132:135], v[148:151], v[212:215]
ds_read_b128 v[180:183], v31 offset:52224
v_mfma_f32_16x16x32_bf16 v[216:219], v[136:139], v[148:151], v[216:219]
ds_read_b128 v[184:187], v31 offset:54272
v_mfma_f32_16x16x32_bf16 v[220:223], v[140:143], v[148:151], v[220:223]
ds_read_b128 v[188:191], v31 offset:56320
s_waitcnt lgkmcnt(9)
v_mfma_f32_16x16x32_bf16 v[224:227], v[128:131], v[152:155], v[224:227]
v_mfma_f32_16x16x32_bf16 v[228:231], v[132:135], v[152:155], v[228:231]
v_mfma_f32_16x16x32_bf16 v[232:235], v[136:139], v[152:155], v[232:235]
v_mfma_f32_16x16x32_bf16 v[236:239], v[140:143], v[152:155], v[236:239]
s_waitcnt lgkmcnt(8)
v_mfma_f32_16x16x32_bf16 v[240:243], v[128:131], v[156:159], v[240:243]
v_mfma_f32_16x16x32_bf16 v[244:247], v[132:135], v[156:159], v[244:247]
v_mfma_f32_16x16x32_bf16 v[248:251], v[136:139], v[156:159], v[248:251]
v_mfma_f32_16x16x32_bf16 v[252:255], v[140:143], v[156:159], v[252:255]
s_waitcnt lgkmcnt(6)
v_mfma_f32_16x16x32_bf16 v[192:195], v[160:163], v[176:179], v[192:195]
s_setprio 3
s_add_u32 m0, s75, 0x8000
s_add_u32 s78, s29, 0x18000
buffer_load_dwordx4 v1, s[36:39], s78 offen sc1 lds
buffer_load_dwordx4 v1, s[36:39], s78 offen offset:1024 sc1 lds
s_add_u32 m0, s75, 0x8800
s_add_u32 s78, s29, 0x19000
buffer_load_dwordx4 v1, s[36:39], s78 offen sc1 lds
buffer_load_dwordx4 v1, s[36:39], s78 offen offset:1024 sc1 lds
s_waitcnt vmcnt(6)
ds_read_b128 v[128:131], v6
ds_read_b128 v[132:135], v6 offset:1024
ds_read_b128 v[136:139], v6 offset:2048
ds_read_b128 v[140:143], v6 offset:3072
s_add_u32 s78, s31, 0x2000
buffer_load_dwordx4 v[52:55], v1, s[36:39], s78 offen sc1
s_add_u32 s78, s31, 0x2000
buffer_load_dwordx4 v[64:67], v1, s[36:39], s78 offen offset:1024 sc1
s_waitcnt lgkmcnt(3)
v_max3_u32 v8, v128, v129, v130
s_waitcnt lgkmcnt(2)
v_max3_u32 v8, v8, v131, v132
v_max3_u32 v8, v8, v133, v134
s_waitcnt lgkmcnt(1)
v_max3_u32 v8, v8, v135, v136
v_max3_u32 v8, v8, v137, v138
s_waitcnt lgkmcnt(0)
v_max3_u32 v8, v8, v139, v140
v_max3_u32 v8, v8, v141, v142
v_max_u32 v8, v8, v143
v_cmp_eq_u32 vcc, -1, v8
s_cmp_lg_u64 vcc, 0
s_cbranch_scc1 .Lfk_rare93
.Lfk_join92:
s_setprio 1
s_barrier
ds_read_b128 v[128:131], v30 offset:32768
v_mfma_f32_16x16x32_bf16 v[196:199], v[164:167], v[176:179], v[196:199]
ds_read_b128 v[144:147], v4
v_mfma_f32_16x16x32_bf16 v[200:203], v[168:171], v[176:179], v[200:203]
ds_read_b128 v[132:135], v30 offset:40960
v_mfma_f32_16x16x32_bf16 v[204:207], v[172:175], v[176:179], v[204:207]
ds_read_b128 v[136:139], v30 offset:33792
v_mfma_f32_16x16x32_bf16 v[208:211], v[160:163], v[180:183], v[208:211]
ds_read_b128 v[140:143], v30 offset:41984
v_mfma_f32_16x16x32_bf16 v[212:215], v[164:167], v[180:183], v[212:215]
ds_read_b128 v[148:151], v4 offset:2048
v_mfma_f32_16x16x32_bf16 v[216:219], v[168:171], v[180:183], v[216:219]
ds_read_b128 v[152:155], v4 offset:4096
v_mfma_f32_16x16x32_bf16 v[220:223], v[172:175], v[180:183], v[220:223]
ds_read_b128 v[156:159], v4 offset:6144
v_mfma_f32_16x16x32_bf16 v[224:227], v[160:163], v[184:187], v[224:227]
v_mfma_f32_16x16x32_bf16 v[228:231], v[164:167], v[184:187], v[228:231]
v_mfma_f32_16x16x32_bf16 v[232:235], v[168:171], v[184:187], v[232:235]
v_mfma_f32_16x16x32_bf16 v[236:239], v[172:175], v[184:187], v[236:239]
v_mfma_f32_16x16x32_bf16 v[240:243], v[160:163], v[188:191], v[240:243]
v_mfma_f32_16x16x32_bf16 v[244:247], v[164:167], v[188:191], v[244:247]
v_mfma_f32_16x16x32_bf16 v[248:251], v[168:171], v[188:191], v[248:251]
v_mfma_f32_16x16x32_bf16 v[252:255], v[172:175], v[188:191], v[252:255]
s_waitcnt lgkmcnt(6)
v_mfma_f32_16x16x32_bf16 v[192:195], v[128:131], v[144:147], v[192:195]
ds_read_b128 v[160:163], v30 offset:49152
s_waitcnt lgkmcnt(6)
v_mfma_f32_16x16x32_bf16 v[196:199], v[132:135], v[144:147], v[196:199]
ds_read_b128 v[176:179], v4 offset:1024
s_waitcnt lgkmcnt(6)
v_mfma_f32_16x16x32_bf16 v[200:203], v[136:139], v[144:147], v[200:203]
ds_read_b128 v[164:167], v30 offset:57344
s_waitcnt lgkmcnt(6)
v_mfma_f32_16x16x32_bf16 v[204:207], v[140:143], v[144:147], v[204:207]
ds_read_b128 v[168:171], v30 offset:50176
s_waitcnt lgkmcnt(6)
v_mfma_f32_16x16x32_bf16 v[208:211], v[128:131], v[148:151], v[208:211]
ds_read_b128 v[172:175], v30 offset:58368
v_mfma_f32_16x16x32_bf16 v[212:215], v[132:135], v[148:151], v[212:215]
ds_read_b128 v[180:183], v4 offset:3072
v_mfma_f32_16x16x32_bf16 v[216:219], v[136:139], v[148:151], v[216:219]
ds_read_b128 v[184:187], v4 offset:5120
v_mfma_f32_16x16x32_bf16 v[220:223], v[140:143], v[148:151], v[220:223]
ds_read_b128 v[188:191], v4 offset:7168
s_waitcnt lgkmcnt(9)
v_mfma_f32_16x16x32_bf16 v[224:227], v[128:131], v[152:155], v[224:227]
v_mfma_f32_16x16x32_bf16 v[228:231], v[132:135], v[152:155], v[228:231]
v_mfma_f32_16x16x32_bf16 v[232:235], v[136:139], v[152:155], v[232:235]
v_mfma_f32_16x16x32_bf16 v[236:239], v[140:143], v[152:155], v[236:239]
s_waitcnt lgkmcnt(8)
v_mfma_f32_16x16x32_bf16 v[240:243], v[128:131], v[156:159], v[240:243]
v_mfma_f32_16x16x32_bf16 v[244:247], v[132:135], v[156:159], v[244:247]
v_mfma_f32_16x16x32_bf16 v[248:251], v[136:139], v[156:159], v[248:251]
v_mfma_f32_16x16x32_bf16 v[252:255], v[140:143], v[156:159], v[252:255]
s_waitcnt lgkmcnt(6)
v_mfma_f32_16x16x32_bf16 v[192:195], v[160:163], v[176:179], v[192:195]
s_setprio 3
s_add_u32 m0, s75, 0xc000
s_add_u32 s78, s29, 0x18800
buffer_load_dwordx4 v1, s[36:39], s78 offen sc1 lds
buffer_load_dwordx4 v1, s[36:39], s78 offen offset:1024 sc1 lds
s_add_u32 m0, s75, 0xc800
s_add_u32 s78, s29, 0x19800
buffer_load_dwordx4 v1, s[36:39], s78 offen sc1 lds
buffer_load_dwordx4 v1, s[36:39], s78 offen offset:1024 sc1 lds
s_waitcnt vmcnt(6)
ds_read_b128 v[128:131], v5 offset:32768
ds_read_b128 v[132:135], v5 offset:33792
ds_read_b128 v[136:139], v5 offset:34816
ds_read_b128 v[140:143], v5 offset:35840
s_waitcnt lgkmcnt(3)
v_max3_u32 v8, v128, v129, v130
s_waitcnt lgkmcnt(2)
v_max3_u32 v8, v8, v131, v132
v_max3_u32 v8, v8, v133, v134
s_waitcnt lgkmcnt(1)
v_max3_u32 v8, v8, v135, v136
v_max3_u32 v8, v8, v137, v138
s_waitcnt lgkmcnt(0)
v_max3_u32 v8, v8, v139, v140
v_max3_u32 v8, v8, v141, v142
v_max_u32 v8, v8, v143
v_cmp_eq_u32 vcc, -1, v8
s_cmp_lg_u64 vcc, 0
s_cbranch_scc1 .Lfk_rare97
.Lfk_join96:
s_lshl_b32 s96, s83, 3
s_add_u32 s96, s96, s62
s_lshl_b32 s96, s96, 12
s_add_u32 s78, 0xe00000, s96
buffer_load_dwordx4 v[96:99], v1, s[36:39], s78 offen sc1
buffer_load_dwordx4 v[100:103], v1, s[36:39], s78 offen offset:1024 sc1
buffer_load_dwordx4 v[104:107], v1, s[36:39], s78 offen offset:2048 sc1
buffer_load_dwordx4 v[108:111], v1, s[36:39], s78 offen offset:3072 sc1
s_setprio 1
s_barrier
ds_read_b128 v[128:131], v29
v_mfma_f32_16x16x32_bf16 v[196:199], v[164:167], v[176:179], v[196:199]
ds_read_b128 v[144:147], v31 offset:32768
v_mfma_f32_16x16x32_bf16 v[200:203], v[168:171], v[176:179], v[200:203]
ds_read_b128 v[132:135], v29 offset:8192
v_mfma_f32_16x16x32_bf16 v[204:207], v[172:175], v[176:179], v[204:207]
ds_read_b128 v[136:139], v29 offset:1024
v_mfma_f32_16x16x32_bf16 v[208:211], v[160:163], v[180:183], v[208:211]
ds_read_b128 v[140:143], v29 offset:9216
v_mfma_f32_16x16x32_bf16 v[212:215], v[164:167], v[180:183], v[212:215]
ds_read_b128 v[148:151], v31 offset:34816
v_mfma_f32_16x16x32_bf16 v[216:219], v[168:171], v[180:183], v[216:219]
ds_read_b128 v[152:155], v31 offset:36864
v_mfma_f32_16x16x32_bf16 v[220:223], v[172:175], v[180:183], v[220:223]
ds_read_b128 v[156:159], v31 offset:38912
v_mfma_f32_16x16x32_bf16 v[224:227], v[160:163], v[184:187], v[224:227]
v_mfma_f32_16x16x32_bf16 v[228:231], v[164:167], v[184:187], v[228:231]
v_mfma_f32_16x16x32_bf16 v[232:235], v[168:171], v[184:187], v[232:235]
v_mfma_f32_16x16x32_bf16 v[236:239], v[172:175], v[184:187], v[236:239]
v_mfma_f32_16x16x32_bf16 v[240:243], v[160:163], v[188:191], v[240:243]
v_mfma_f32_16x16x32_bf16 v[244:247], v[164:167], v[188:191], v[244:247]
v_mfma_f32_16x16x32_bf16 v[248:251], v[168:171], v[188:191], v[248:251]
v_mfma_f32_16x16x32_bf16 v[252:255], v[172:175], v[188:191], v[252:255]
s_waitcnt lgkmcnt(6)
v_mfma_f32_16x16x32_bf16 v[192:195], v[128:131], v[144:147], v[192:195]
ds_read_b128 v[160:163], v29 offset:16384
s_waitcnt lgkmcnt(6)
v_mfma_f32_16x16x32_bf16 v[196:199], v[132:135], v[144:147], v[196:199]
ds_read_b128 v[176:179], v31 offset:33792
s_waitcnt lgkmcnt(6)
v_mfma_f32_16x16x32_bf16 v[200:203], v[136:139], v[144:147], v[200:203]
ds_read_b128 v[164:167], v29 offset:24576
s_waitcnt lgkmcnt(6)
v_mfma_f32_16x16x32_bf16 v[204:207], v[140:143], v[144:147], v[204:207]
ds_read_b128 v[168:171], v29 offset:17408
s_waitcnt lgkmcnt(6)
v_mfma_f32_16x16x32_bf16 v[208:211], v[128:131], v[148:151], v[208:211]
ds_read_b128 v[172:175], v29 offset:25600
v_mfma_f32_16x16x32_bf16 v[212:215], v[132:135], v[148:151], v[212:215]
ds_read_b128 v[180:183], v31 offset:35840
v_mfma_f32_16x16x32_bf16 v[216:219], v[136:139], v[148:151], v[216:219]
ds_read_b128 v[184:187], v31 offset:37888
v_mfma_f32_16x16x32_bf16 v[220:223], v[140:143], v[148:151], v[220:223]
ds_read_b128 v[188:191], v31 offset:39936
s_waitcnt lgkmcnt(9)
v_mfma_f32_16x16x32_bf16 v[224:227], v[128:131], v[152:155], v[224:227]
v_mfma_f32_16x16x32_bf16 v[228:231], v[132:135], v[152:155], v[228:231]
v_mfma_f32_16x16x32_bf16 v[232:235], v[136:139], v[152:155], v[232:235]
v_mfma_f32_16x16x32_bf16 v[236:239], v[140:143], v[152:155], v[236:239]
s_waitcnt lgkmcnt(8)
v_mfma_f32_16x16x32_bf16 v[240:243], v[128:131], v[156:159], v[240:243]
v_mfma_f32_16x16x32_bf16 v[244:247], v[132:135], v[156:159], v[244:247]
v_mfma_f32_16x16x32_bf16 v[248:251], v[136:139], v[156:159], v[248:251]
v_mfma_f32_16x16x32_bf16 v[252:255], v[140:143], v[156:159], v[252:255]
s_waitcnt lgkmcnt(6)
v_mfma_f32_16x16x32_bf16 v[192:195], v[160:163], v[176:179], v[192:195]
s_setprio 3
s_waitcnt vmcnt(4)
ds_read_b128 v[128:131], v5 offset:49152
ds_read_b128 v[132:135], v5 offset:50176
ds_read_b128 v[136:139], v5 offset:51200
ds_read_b128 v[140:143], v5 offset:52224
s_waitcnt vmcnt(3)
v_cmp_ne_u32 vcc, s60, v98
s_waitcnt vmcnt(2)
v_cmp_ne_u32 s[100:101], s60, v102
s_or_b64 vcc, vcc, s[100:101]
s_waitcnt vmcnt(1)
v_cmp_ne_u32 s[100:101], s60, v106
s_or_b64 vcc, vcc, s[100:101]
s_waitcnt vmcnt(0)
v_cmp_ne_u32 s[100:101], s60, v110
s_or_b64 vcc, vcc, s[100:101]
s_cbranch_scc1 .Lfk_rare101
.Lfk_join100:
v_add_f32 v96, v96, v100
v_add_f32 v97, v97, v101
v_add_f32 v96, v96, v104
v_add_f32 v97, v97, v105
v_add_f32 v96, v96, v108
v_add_f32 v97, v97, v109
v_mov_b32 v98, v96
v_mov_b32 v99, v97
s_nop 0
v_permlane32_swap_b32 v96, v98
v_permlane32_swap_b32 v97, v99
v_add_f32 v96, v96, v98
v_add_f32 v97, v97, v99
v_mov_b32 v98, v96
v_mov_b32 v99, v97
s_nop 0
v_permlane16_swap_b32 v96, v98
v_permlane16_swap_b32 v97, v99
v_add_f32 v96, v96, v98
v_add_f32 v97, v97, v99
v_mul_f32 v96, 0xbb000000, v96
v_mul_f32 v97, 0x3b000000, v97
v_fma_f32 v97, -v96, v96, v97
v_max_f32 v97, 0, v97
v_add_f32 v97, 0x3727c5ac, v97
v_rsq_f32 v98, v97
v_sqrt_f32 v97, v97
v_lshlrev_b32 v12, 1, v3
s_lshl_b32 s96, s62, 8
v_add_u32 v12, s96, v12
v_add_u32 v100, 0x27800, v12
v_mul_f32 v98, 0xbfb8aa3b, v98
ds_write_b128 v100, v[96:99]
s_add_u32 s78, s31, 0x2000
buffer_load_dwordx4 v[68:71], v1, s[36:39], s78 offen offset:2048 sc1
s_add_u32 s78, s31, 0x2000
buffer_load_dwordx4 v[40:43], v1, s[36:39], s78 offen offset:3072 sc1
s_add_u32 s78, s31, 0x3000
buffer_load_dwordx4 v[88:91], v1, s[36:39], s78 offen sc1
s_add_u32 s78, s31, 0x3000
buffer_load_dwordx4 v[92:95], v1, s[36:39], s78 offen offset:1024 sc1
s_add_u32 s78, s31, 0x3000
buffer_load_dwordx4 v[44:47], v1, s[36:39], s78 offen offset:2048 sc1
s_waitcnt lgkmcnt(4)
v_max3_u32 v8, v128, v129, v130
s_waitcnt lgkmcnt(3)
v_max3_u32 v8, v8, v131, v132
v_max3_u32 v8, v8, v133, v134
s_waitcnt lgkmcnt(2)
v_max3_u32 v8, v8, v135, v136
v_max3_u32 v8, v8, v137, v138
s_waitcnt lgkmcnt(1)
v_max3_u32 v8, v8, v139, v140
v_max3_u32 v8, v8, v141, v142
v_max_u32 v8, v8, v143
v_cmp_eq_u32 vcc, -1, v8
s_cmp_lg_u64 vcc, 0
s_cbranch_scc1 .Lfk_rare105
.Lfk_join104:
s_setprio 1
s_waitcnt lgkmcnt(0)
s_barrier
ds_read_b128 v[128:131], v30
v_mfma_f32_16x16x32_bf16 v[196:199], v[164:167], v[176:179], v[196:199]
ds_read_b128 v[144:147], v31 offset:49152
v_mfma_f32_16x16x32_bf16 v[200:203], v[168:171], v[176:179], v[200:203]
ds_read_b128 v[132:135], v30 offset:8192
v_mfma_f32_16x16x32_bf16 v[204:207], v[172:175], v[176:179], v[204:207]
ds_read_b128 v[136:139], v30 offset:1024
v_mfma_f32_16x16x32_bf16 v[208:211], v[160:163], v[180:183], v[208:211]
ds_read_b128 v[140:143], v30 offset:9216
v_mfma_f32_16x16x32_bf16 v[212:215], v[164:167], v[180:183], v[212:215]
ds_read_b128 v[148:151], v31 offset:51200
v_mfma_f32_16x16x32_bf16 v[216:219], v[168:171], v[180:183], v[216:219]
ds_read_b128 v[152:155], v31 offset:53248
v_mfma_f32_16x16x32_bf16 v[220:223], v[172:175], v[180:183], v[220:223]
ds_read_b128 v[156:159], v31 offset:55296
v_mfma_f32_16x16x32_bf16 v[224:227], v[160:163], v[184:187], v[224:227]
v_mfma_f32_16x16x32_bf16 v[228:231], v[164:167], v[184:187], v[228:231]
v_mfma_f32_16x16x32_bf16 v[232:235], v[168:171], v[184:187], v[232:235]
v_mfma_f32_16x16x32_bf16 v[236:239], v[172:175], v[184:187], v[236:239]
v_mfma_f32_16x16x32_bf16 v[240:243], v[160:163], v[188:191], v[240:243]
v_mfma_f32_16x16x32_bf16 v[244:247], v[164:167], v[188:191], v[244:247]
v_mfma_f32_16x16x32_bf16 v[248:251], v[168:171], v[188:191], v[248:251]
v_mfma_f32_16x16x32_bf16 v[252:255], v[172:175], v[188:191], v[252:255]
s_waitcnt lgkmcnt(6)
v_mfma_f32_16x16x32_bf16 v[192:195], v[128:131], v[144:147], v[192:195]
ds_read_b128 v[160:163], v30 offset:16384
s_waitcnt lgkmcnt(6)
v_mfma_f32_16x16x32_bf16 v[196:199], v[132:135], v[144:147], v[196:199]
ds_read_b128 v[176:179], v31 offset:50176
s_waitcnt lgkmcnt(6)
v_mfma_f32_16x16x32_bf16 v[200:203], v[136:139], v[144:147], v[200:203]
ds_read_b128 v[164:167], v30 offset:24576
s_waitcnt lgkmcnt(6)
v_mfma_f32_16x16x32_bf16 v[204:207], v[140:143], v[144:147], v[204:207]
ds_read_b128 v[168:171], v30 offset:17408
s_waitcnt lgkmcnt(6)
v_mfma_f32_16x16x32_bf16 v[208:211], v[128:131], v[148:151], v[208:211]
ds_read_b128 v[172:175], v30 offset:25600
v_mfma_f32_16x16x32_bf16 v[212:215], v[132:135], v[148:151], v[212:215]
ds_read_b128 v[180:183], v31 offset:52224
v_mfma_f32_16x16x32_bf16 v[216:219], v[136:139], v[148:151], v[216:219]
s_add_u32 s78, s31, 0x3000
buffer_load_dwordx4 v[48:51], v1, s[36:39], s78 offen offset:3072 sc1
s_add_u32 s78, s31, 0x4000
buffer_load_dwordx4 v[112:115], v1, s[36:39], s78 offen sc1
s_add_u32 s78, s31, 0x4000
buffer_load_dwordx4 v[116:119], v1, s[36:39], s78 offen offset:1024 sc1
s_add_u32 s78, s31, 0x4000
buffer_load_dwordx4 v[120:123], v1, s[36:39], s78 offen offset:2048 sc1
s_add_u32 s78, s31, 0x4000
buffer_load_dwordx4 v[124:127], v1, s[36:39], s78 offen offset:3072 sc1
s_add_u32 s78, s31, 0x5000
buffer_load_dwordx4 v[96:99], v1, s[36:39], s78 offen sc1
s_add_u32 s78, s31, 0x5000
buffer_load_dwordx4 v[100:103], v1, s[36:39], s78 offen offset:1024 sc1
ds_read_b128 v[184:187], v31 offset:54272
v_mfma_f32_16x16x32_bf16 v[220:223], v[140:143], v[148:151], v[220:223]
ds_read_b128 v[188:191], v31 offset:56320
s_waitcnt lgkmcnt(9)
v_mfma_f32_16x16x32_bf16 v[224:227], v[128:131], v[152:155], v[224:227]
v_mfma_f32_16x16x32_bf16 v[228:231], v[132:135], v[152:155], v[228:231]
v_mfma_f32_16x16x32_bf16 v[232:235], v[136:139], v[152:155], v[232:235]
v_mfma_f32_16x16x32_bf16 v[236:239], v[140:143], v[152:155], v[236:239]
s_waitcnt lgkmcnt(8)
v_mfma_f32_16x16x32_bf16 v[240:243], v[128:131], v[156:159], v[240:243]
v_mfma_f32_16x16x32_bf16 v[244:247], v[132:135], v[156:159], v[244:247]
v_mfma_f32_16x16x32_bf16 v[248:251], v[136:139], v[156:159], v[248:251]
v_mfma_f32_16x16x32_bf16 v[252:255], v[140:143], v[156:159], v[252:255]
s_waitcnt lgkmcnt(6)
v_mfma_f32_16x16x32_bf16 v[192:195], v[160:163], v[176:179], v[192:195]
ds_read_b128 v[144:147], v21
s_waitcnt lgkmcnt(6)
v_mfma_f32_16x16x32_bf16 v[196:199], v[164:167], v[176:179], v[196:199]
ds_read_b128 v[128:131], v20
s_waitcnt lgkmcnt(6)
v_mfma_f32_16x16x32_bf16 v[200:203], v[168:171], v[176:179], v[200:203]
ds_read_b128 v[132:135], v20 offset:16
s_waitcnt lgkmcnt(6)
v_mfma_f32_16x16x32_bf16 v[204:207], v[172:175], v[176:179], v[204:207]
ds_read_b128 v[136:139], v20 offset:128
s_waitcnt lgkmcnt(6)
v_mfma_f32_16x16x32_bf16 v[208:211], v[160:163], v[180:183], v[208:211]
ds_read_b128 v[140:143], v20 offset:144
v_mfma_f32_16x16x32_bf16 v[212:215], v[164:167], v[180:183], v[212:215]
ds_read_b128 v[148:151], v21 offset:256
v_mfma_f32_16x16x32_bf16 v[216:219], v[168:171], v[180:183], v[216:219]
ds_read_b128 v[152:155], v21 offset:512
v_mfma_f32_16x16x32_bf16 v[220:223], v[172:175], v[180:183], v[220:223]
ds_read_b128 v[156:159], v21 offset:768
s_waitcnt lgkmcnt(9)
v_mfma_f32_16x16x32_bf16 v[224:227], v[160:163], v[184:187], v[224:227]
v_mfma_f32_16x16x32_bf16 v[228:231], v[164:167], v[184:187], v[228:231]
v_mfma_f32_16x16x32_bf16 v[232:235], v[168:171], v[184:187], v[232:235]
v_mfma_f32_16x16x32_bf16 v[236:239], v[172:175], v[184:187], v[236:239]
s_waitcnt lgkmcnt(8)
v_mfma_f32_16x16x32_bf16 v[240:243], v[160:163], v[188:191], v[240:243]
v_mfma_f32_16x16x32_bf16 v[244:247], v[164:167], v[188:191], v[244:247]
v_mfma_f32_16x16x32_bf16 v[248:251], v[168:171], v[188:191], v[248:251]
v_mfma_f32_16x16x32_bf16 v[252:255], v[172:175], v[188:191], v[252:255]
s_waitcnt lgkmcnt(7)
v_mov_b32 v12, v145
s_waitcnt lgkmcnt(6)
v_pk_fma_f32 v[192:193], v[144:145], v[128:129], v[192:193] op_sel_hi:[0,1,1]
v_pk_fma_f32 v[194:195], v[144:145], v[130:131], v[194:195] op_sel_hi:[0,1,1]
v_pk_mul_f32 v[192:193], v[192:193], v[146:147] op_sel_hi:[1,0]
v_pk_mul_f32 v[194:195], v[194:195], v[146:147] op_sel_hi:[1,0]
s_waitcnt lgkmcnt(5)
v_pk_fma_f32 v[196:197], v[144:145], v[132:133], v[196:197] op_sel_hi:[0,1,1]
v_pk_fma_f32 v[198:199], v[144:145], v[134:135], v[198:199] op_sel_hi:[0,1,1]
v_exp_f32 v192, v192
v_exp_f32 v193, v193
v_exp_f32 v194, v194
v_exp_f32 v195, v195
v_pk_fma_f32 v[192:193], v[192:193], v[12:13], v[12:13] op_sel_hi:[1,0,0]
v_pk_fma_f32 v[194:195], v[194:195], v[12:13], v[12:13] op_sel_hi:[1,0,0]
v_rcp_f32 v192, v192
v_rcp_f32 v193, v193
v_rcp_f32 v194, v194
v_rcp_f32 v195, v195
v_pk_mul_f32 v[192:193], v[192:193], v[196:197]
v_pk_mul_f32 v[194:195], v[194:195], v[198:199]
v_cvt_pk_bf16_f32 v168, v192, v193
v_cvt_pk_bf16_f32 v169, v194, v195
s_waitcnt lgkmcnt(4)
v_pk_fma_f32 v[200:201], v[144:145], v[136:137], v[200:201] op_sel_hi:[0,1,1]
v_pk_fma_f32 v[202:203], v[144:145], v[138:139], v[202:203] op_sel_hi:[0,1,1]
v_pk_mul_f32 v[200:201], v[200:201], v[146:147] op_sel_hi:[1,0]
v_pk_mul_f32 v[202:203], v[202:203], v[146:147] op_sel_hi:[1,0]
s_waitcnt lgkmcnt(3)
v_pk_fma_f32 v[204:205], v[144:145], v[140:141], v[204:205] op_sel_hi:[0,1,1]
v_pk_fma_f32 v[206:207], v[144:145], v[142:143], v[206:207] op_sel_hi:[0,1,1]
v_exp_f32 v200, v200
v_exp_f32 v201, v201
v_exp_f32 v202, v202
v_exp_f32 v203, v203
v_pk_fma_f32 v[200:201], v[200:201], v[12:13], v[12:13] op_sel_hi:[1,0,0]
v_pk_fma_f32 v[202:203], v[202:203], v[12:13], v[12:13] op_sel_hi:[1,0,0]
v_rcp_f32 v200, v200
v_rcp_f32 v201, v201
v_rcp_f32 v202, v202
v_rcp_f32 v203, v203
v_pk_mul_f32 v[200:201], v[200:201], v[204:205]
v_pk_mul_f32 v[202:203], v[202:203], v[206:207]
v_cvt_pk_bf16_f32 v170, v200, v201
v_cvt_pk_bf16_f32 v171, v202, v203
buffer_store_dwordx4 v[168:171], v1, s[36:39], s32 offen sc1
s_add_u32 s32, s32, 0xe000
s_waitcnt lgkmcnt(2)
v_mov_b32 v12, v149
v_pk_fma_f32 v[208:209], v[148:149], v[128:129], v[208:209] op_sel_hi:[0,1,1]
v_pk_fma_f32 v[210:211], v[148:149], v[130:131], v[210:211] op_sel_hi:[0,1,1]
v_pk_mul_f32 v[208:209], v[208:209], v[150:151] op_sel_hi:[1,0]
v_pk_mul_f32 v[210:211], v[210:211], v[150:151] op_sel_hi:[1,0]
v_pk_fma_f32 v[212:213], v[148:149], v[132:133], v[212:213] op_sel_hi:[0,1,1]
v_pk_fma_f32 v[214:215], v[148:149], v[134:135], v[214:215] op_sel_hi:[0,1,1]
v_exp_f32 v208, v208
v_exp_f32 v209, v209
v_exp_f32 v210, v210
v_exp_f32 v211, v211
v_pk_fma_f32 v[208:209], v[208:209], v[12:13], v[12:13] op_sel_hi:[1,0,0]
v_pk_fma_f32 v[210:211], v[210:211], v[12:13], v[12:13] op_sel_hi:[1,0,0]
v_rcp_f32 v208, v208
v_rcp_f32 v209, v209
v_rcp_f32 v210, v210
v_rcp_f32 v211, v211
v_pk_mul_f32 v[208:209], v[208:209], v[212:213]
v_pk_mul_f32 v[210:211], v[210:211], v[214:215]
v_cvt_pk_bf16_f32 v172, v208, v209
v_cvt_pk_bf16_f32 v173, v210, v211
v_pk_fma_f32 v[216:217], v[148:149], v[136:137], v[216:217] op_sel_hi:[0,1,1]
v_pk_fma_f32 v[218:219], v[148:149], v[138:139], v[218:219] op_sel_hi:[0,1,1]
v_pk_mul_f32 v[216:217], v[216:217], v[150:151] op_sel_hi:[1,0]
v_pk_mul_f32 v[218:219], v[218:219], v[150:151] op_sel_hi:[1,0]
v_pk_fma_f32 v[220:221], v[148:149], v[140:141], v[220:221] op_sel_hi:[0,1,1]
v_pk_fma_f32 v[222:223], v[148:149], v[142:143], v[222:223] op_sel_hi:[0,1,1]
v_exp_f32 v216, v216
v_exp_f32 v217, v217
v_exp_f32 v218, v218
v_exp_f32 v219, v219
v_pk_fma_f32 v[216:217], v[216:217], v[12:13], v[12:13] op_sel_hi:[1,0,0]
v_pk_fma_f32 v[218:219], v[218:219], v[12:13], v[12:13] op_sel_hi:[1,0,0]
v_rcp_f32 v216, v216
v_rcp_f32 v217, v217
v_rcp_f32 v218, v218
v_rcp_f32 v219, v219
v_pk_mul_f32 v[216:217], v[216:217], v[220:221]
v_pk_mul_f32 v[218:219], v[218:219], v[222:223]
v_cvt_pk_bf16_f32 v174, v216, v217
v_cvt_pk_bf16_f32 v175, v218, v219
buffer_store_dwordx4 v[172:175], v1, s[36:39], s32 offen sc1
s_add_u32 s32, s32, 0xe000
s_waitcnt lgkmcnt(1)
v_mov_b32 v12, v153
v_pk_fma_f32 v[224:225], v[152:153], v[128:129], v[224:225] op_sel_hi:[0,1,1]
v_pk_fma_f32 v[226:227], v[152:153], v[130:131], v[226:227] op_sel_hi:[0,1,1]
v_pk_mul_f32 v[224:225], v[224:225], v[154:155] op_sel_hi:[1,0]
v_pk_mul_f32 v[226:227], v[226:227], v[154:155] op_sel_hi:[1,0]
v_pk_fma_f32 v[228:229], v[152:153], v[132:133], v[228:229] op_sel_hi:[0,1,1]
v_pk_fma_f32 v[230:231], v[152:153], v[134:135], v[230:231] op_sel_hi:[0,1,1]
v_exp_f32 v224, v224
v_exp_f32 v225, v225
v_exp_f32 v226, v226
v_exp_f32 v227, v227
v_pk_fma_f32 v[224:225], v[224:225], v[12:13], v[12:13] op_sel_hi:[1,0,0]
v_pk_fma_f32 v[226:227], v[226:227], v[12:13], v[12:13] op_sel_hi:[1,0,0]
v_rcp_f32 v224, v224
v_rcp_f32 v225, v225
v_rcp_f32 v226, v226
v_rcp_f32 v227, v227
v_pk_mul_f32 v[224:225], v[224:225], v[228:229]
v_pk_mul_f32 v[226:227], v[226:227], v[230:231]
v_cvt_pk_bf16_f32 v176, v224, v225
v_cvt_pk_bf16_f32 v177, v226, v227
v_pk_fma_f32 v[232:233], v[152:153], v[136:137], v[232:233] op_sel_hi:[0,1,1]
v_pk_fma_f32 v[234:235], v[152:153], v[138:139], v[234:235] op_sel_hi:[0,1,1]
v_pk_mul_f32 v[232:233], v[232:233], v[154:155] op_sel_hi:[1,0]
v_pk_mul_f32 v[234:235], v[234:235], v[154:155] op_sel_hi:[1,0]
v_pk_fma_f32 v[236:237], v[152:153], v[140:141], v[236:237] op_sel_hi:[0,1,1]
v_pk_fma_f32 v[238:239], v[152:153], v[142:143], v[238:239] op_sel_hi:[0,1,1]
v_exp_f32 v232, v232
v_exp_f32 v233, v233
v_exp_f32 v234, v234
v_exp_f32 v235, v235
v_pk_fma_f32 v[232:233], v[232:233], v[12:13], v[12:13] op_sel_hi:[1,0,0]
v_pk_fma_f32 v[234:235], v[234:235], v[12:13], v[12:13] op_sel_hi:[1,0,0]
v_rcp_f32 v232, v232
v_rcp_f32 v233, v233
v_rcp_f32 v234, v234
v_rcp_f32 v235, v235
v_pk_mul_f32 v[232:233], v[232:233], v[236:237]
v_pk_mul_f32 v[234:235], v[234:235], v[238:239]
v_cvt_pk_bf16_f32 v178, v232, v233
v_cvt_pk_bf16_f32 v179, v234, v235
buffer_store_dwordx4 v[176:179], v1, s[36:39], s32 offen sc1
s_add_u32 s32, s32, 0xe000
s_waitcnt lgkmcnt(0)
v_mov_b32 v12, v157
v_pk_fma_f32 v[240:241], v[156:157], v[128:129], v[240:241] op_sel_hi:[0,1,1]
v_pk_fma_f32 v[242:243], v[156:157], v[130:131], v[242:243] op_sel_hi:[0,1,1]
v_pk_mul_f32 v[240:241], v[240:241], v[158:159] op_sel_hi:[1,0]
v_pk_mul_f32 v[242:243], v[242:243], v[158:159] op_sel_hi:[1,0]
v_pk_fma_f32 v[244:245], v[156:157], v[132:133], v[244:245] op_sel_hi:[0,1,1]
v_pk_fma_f32 v[246:247], v[156:157], v[134:135], v[246:247] op_sel_hi:[0,1,1]
v_exp_f32 v240, v240
v_exp_f32 v241, v241
v_exp_f32 v242, v242
v_exp_f32 v243, v243
v_pk_fma_f32 v[240:241], v[240:241], v[12:13], v[12:13] op_sel_hi:[1,0,0]
v_pk_fma_f32 v[242:243], v[242:243], v[12:13], v[12:13] op_sel_hi:[1,0,0]
v_rcp_f32 v240, v240
v_rcp_f32 v241, v241
v_rcp_f32 v242, v242
v_rcp_f32 v243, v243
v_pk_mul_f32 v[240:241], v[240:241], v[244:245]
v_pk_mul_f32 v[242:243], v[242:243], v[246:247]
v_cvt_pk_bf16_f32 v180, v240, v241
v_cvt_pk_bf16_f32 v181, v242, v243
v_pk_fma_f32 v[248:249], v[156:157], v[136:137], v[248:249] op_sel_hi:[0,1,1]
v_pk_fma_f32 v[250:251], v[156:157], v[138:139], v[250:251] op_sel_hi:[0,1,1]
v_pk_mul_f32 v[248:249], v[248:249], v[158:159] op_sel_hi:[1,0]
v_pk_mul_f32 v[250:251], v[250:251], v[158:159] op_sel_hi:[1,0]
v_pk_fma_f32 v[252:253], v[156:157], v[140:141], v[252:253] op_sel_hi:[0,1,1]
v_pk_fma_f32 v[254:255], v[156:157], v[142:143], v[254:255] op_sel_hi:[0,1,1]
v_exp_f32 v248, v248
v_exp_f32 v249, v249
v_exp_f32 v250, v250
v_exp_f32 v251, v251
v_pk_fma_f32 v[248:249], v[248:249], v[12:13], v[12:13] op_sel_hi:[1,0,0]
v_pk_fma_f32 v[250:251], v[250:251], v[12:13], v[12:13] op_sel_hi:[1,0,0]
v_rcp_f32 v248, v248
v_rcp_f32 v249, v249
v_rcp_f32 v250, v250
v_rcp_f32 v251, v251
v_pk_mul_f32 v[248:249], v[248:249], v[252:253]
v_pk_mul_f32 v[250:251], v[250:251], v[254:255]
v_cvt_pk_bf16_f32 v182, v248, v249
v_cvt_pk_bf16_f32 v183, v250, v251
buffer_store_dwordx4 v[180:183], v1, s[36:39], s32 offen sc1
s_and_b32 s96, s2, 2
s_lshl_b32 s96, s96, 1
s_lshr_b32 s97, s64, 3
s_add_u32 s81, s96, s97
s_lshl_b32 s96, s83, 3
s_and_b32 s97, s63, 1
s_lshl_b32 s97, s97, 2
s_add_u32 s34, s96, s97
s_lshl_b32 s96, s34, 3
s_add_u32 s96, s96, s62
s_mul_i32 s96, s96, 0x1c00
s_add_u32 s70, 0x700000, s96
s_add_u32 s71, s70, 0xe000
s_add_u32 s72, s70, 0x1c000
s_add_u32 s73, s70, 0x2a000
s_lshl_b32 s96, s34, 14
s_lshl_b32 s97, s81, 7
s_add_u32 s34, s96, s97
v_lshrrev_b32 v12, 3, v0
v_and_b32 v12, 1, v12
v_lshlrev_b32 v12, 15, v12
v_lshrrev_b32 v13, 1, v0
v_and_b32 v13, 3, v13
v_lshlrev_b32 v13, 8, v13
v_add_u32 v12, v12, v13
s_and_b32 s96, s62, 3
s_lshl_b32 s96, s96, 2
v_lshrrev_b32 v13, 4, v0
v_add_u32 v13, s96, v13
v_lshlrev_b32 v13, 4, v13
v_add_u32 v12, v12, v13
v_and_b32 v13, 1, v0
v_lshlrev_b32 v13, 3, v13
v_add_u32 v7, v12, v13
s_lshr_b32 s35, s62, 2
s_lshl_b32 s96, s83, 2
s_and_b32 s97, s81, 1
s_lshl_b32 s97, s97, 1
s_add_u32 s96, s96, s97
s_lshl_b32 s96, s96, 3
s_and_b32 s97, s63, 1
s_lshl_b32 s97, s97, 2
s_add_u32 s96, s96, s97
s_mul_i32 s97, s35, 3
s_add_u32 s96, s96, s97
s_lshl_b32 s96, s96, 12
s_add_u32 s96, 0x500000, s96
s_lshr_b32 s97, s81, 1
s_lshl_b32 s97, s97, 10
s_add_u32 s78, s96, s97
buffer_load_dwordx2 v[24:25], v7, s[36:39], s78 offen sc1
s_lshl_b32 s96, s83, 2
s_and_b32 s97, s81, 1
s_lshl_b32 s97, s97, 1
s_add_u32 s96, s96, s97
s_lshl_b32 s96, s96, 3
s_and_b32 s97, s63, 1
s_lshl_b32 s97, s97, 2
s_add_u32 s96, s96, s97
s_add_u32 s96, s96, 1
s_lshl_b32 s96, s96, 12
s_add_u32 s96, 0x500000, s96
s_lshr_b32 s97, s81, 1
s_lshl_b32 s97, s97, 10
s_add_u32 s78, s96, s97
buffer_load_dwordx2 v[26:27], v7, s[36:39], s78 offen sc1
s_lshl_b32 s96, s83, 2
s_and_b32 s97, s81, 1
s_lshl_b32 s97, s97, 1
s_add_u32 s96, s96, s97
s_lshl_b32 s96, s96, 3
s_and_b32 s97, s63, 1
s_lshl_b32 s97, s97, 2
s_add_u32 s96, s96, s97
s_add_u32 s96, s96, 2
s_lshl_b32 s96, s96, 12
s_add_u32 s96, 0x500000, s96
s_lshr_b32 s97, s81, 1
s_lshl_b32 s97, s97, 10
s_add_u32 s78, s96, s97
buffer_load_dwordx2 v[2:3], v7, s[36:39], s78 offen sc1
s_and_b32 s96, s62, 3
s_lshl_b32 s96, s96, 2
v_lshrrev_b32 v12, 4, v0
v_add_u32 v12, s96, v12
v_lshlrev_b32 v12, 10, v12
v_and_b32 v13, 15, v0
v_lshlrev_b32 v13, 3, v13
v_add_u32 v7, v12, v13
v_and_b32 v12, 15, v0
v_mul_u32_u24 v12, 0x110, v12
v_lshrrev_b32 v13, 4, v0
v_lshlrev_b32 v13, 4, v13
v_add_u32 v12, v12, v13
s_mul_i32 s96, s62, 0x2200
v_add_u32 v5, s96, v12
s_and_b32 s96, s62, 3
s_lshl_b32 s96, s96, 2
v_lshrrev_b32 v12, 4, v0
v_add_u32 v12, s96, v12
v_mul_u32_u24 v12, 0x110, v12
v_and_b32 v13, 15, v0
v_lshlrev_b32 v13, 4, v13
v_add_u32 v12, v12, v13
s_mul_i32 s96, s35, 0x12100
v_add_u32 v6, s96, v12
s_lshl_b32 s96, s83, 4
s_add_u32 s96, s96, s82
s_lshl_b32 s96, s96, 3
s_add_u32 s96, s96, s62
s_lshl_b32 s96, s96, 7
s_add_u32 s79, 0x40000, s96
s_waitcnt vmcnt(3)
s_mov_b64 exec, 1
buffer_store_dwordx2 v[10:11], off, s[36:39], s79 sc1
s_mov_b64 exec, -1
s_add_u32 s78, s31, 0x5000
buffer_load_dwordx4 v[104:107], v1, s[36:39], s78 offen offset:2048 sc1
s_add_u32 s78, s31, 0x5000
buffer_load_dwordx4 v[108:111], v1, s[36:39], s78 offen offset:3072 sc1
s_add_u32 s78, s31, 0x6000
buffer_load_dwordx4 v[240:243], v1, s[36:39], s78 offen sc1
s_add_u32 s78, s31, 0x6000
buffer_load_dwordx4 v[244:247], v1, s[36:39], s78 offen offset:1024 sc1
s_add_u32 s78, s31, 0x6000
buffer_load_dwordx4 v[248:251], v1, s[36:39], s78 offen offset:2048 sc1
s_add_u32 s78, s31, 0x6000
buffer_load_dwordx4 v[252:255], v1, s[36:39], s78 offen offset:3072 sc1
s_barrier
s_sleep 10
s_branch .Lfk_end108
.Lfk_else2:
v_lshrrev_b32 v12, 4, v0
v_lshlrev_b32 v12, 6, v12
v_and_b32 v13, 48, v1
v_add_u32 v5, v12, v13
s_lshl_b32 s96, s67, 7
s_and_b32 s97, s62, 3
s_lshl_b32 s97, s97, 5
s_add_u32 s96, s96, s97
v_lshrrev_b32 v12, 4, v0
v_add_u32 v12, s96, v12
v_lshlrev_b32 v12, 10, v12
v_and_b32 v13, 15, v0
v_lshrrev_b32 v14, 4, v0
v_lshlrev_b32 v14, 2, v14
v_xor_b32 v13, v13, v14
v_lshlrev_b32 v13, 4, v13
v_add_u32 v120, v12, v13
v_xor_b32 v121, 16, v120
v_add_u32 v121, 0x1c00, v121
v_xor_b32 v122, 32, v120
v_add_u32 v122, 0x3800, v122
v_xor_b32 v123, 48, v120
v_add_u32 v123, 0x5400, v123
s_and_b32 s96, s62, 3
s_mul_i32 s96, s96, 0x6000
s_add_u32 s84, 0x10000, s96
s_lshl_b32 s96, s67, 7
s_and_b32 s97, s62, 3
s_lshl_b32 s97, s97, 5
s_add_u32 s96, s96, s97
v_and_b32 v12, 12, v0
v_lshlrev_b32 v12, 1, v12
v_add_u32 v12, s96, v12
v_lshlrev_b32 v12, 10, v12
v_add_u32 v4, v12, v5
s_waitcnt lgkmcnt(0)
s_mov_b32 s24, s4
s_and_b32 s25, s5, 0xffff
s_mov_b32 s26, 0x200000
s_mov_b32 s27, 0x20000
s_mov_b32 s36, s22
s_and_b32 s37, s23, 0xffff
s_mov_b32 s38, 0x1e00000
s_mov_b32 s39, 0x20000
s_cmp_eq_u32 s65, 1
s_cselect_b32 s32, s12, s10
s_cselect_b32 s33, s13, s11
s_and_b32 s33, s33, 0xffff
s_mov_b32 s34, 0x80000
s_mov_b32 s35, 0x20000
s_and_b32 s68, s2, 3
s_lshr_b32 s96, s63, 2
s_lshl_b32 s96, s96, 3
s_and_b32 s97, s64, 7
s_add_u32 s69, s96, s97
s_cmp_eq_u32 s65, 1
s_cselect_b32 s28, s8, s6
s_cselect_b32 s29, s9, s7
s_and_b32 s29, s29, 0xffff
s_mov_b32 s30, 0x800000
s_mov_b32 s31, 0x20000
s_lshl_b32 s96, s69, 7
s_and_b32 s97, s62, 3
s_lshl_b32 s97, s97, 5
s_add_u32 s96, s96, s97
v_and_b32 v12, 12, v0
v_add_u32 v12, s96, v12
v_lshlrev_b32 v12, 12, v12
v_add_u32 v6, v12, v5
s_and_b32 s80, s67, 2
s_mov_b32 s56, 0x55555555
s_mov_b32 s57, 0x55555555
s_mov_b32 s58, 0x33333333
s_mov_b32 s59, 0x33333333
s_mov_b32 s40, s14
s_and_b32 s41, s15, 0xffff
s_mov_b32 s42, 0x1c0000
s_mov_b32 s43, 0x20000
s_mov_b32 s52, s16
s_and_b32 s53, s17, 0xffff
s_mov_b32 s54, 0x1c0000
s_mov_b32 s55, 0x20000
s_mov_b32 s44, s18
s_and_b32 s45, s19, 0xffff
s_mov_b32 s46, 0x1c0000
s_mov_b32 s47, 0x20000
s_mov_b32 s48, s20
s_and_b32 s49, s21, 0xffff
s_mov_b32 s50, 0x200000
s_mov_b32 s51, 0x20000
v_lshlrev_b32 v2, 3, v0
v_and_b32 v3, 0x78, v2
s_add_u32 s83, s69, 0
s_lshl_b32 s96, s68, 2
s_add_u32 s82, s96, s67
s_cmp_eq_u32 s62, 7
s_cselect_b32 s78, 0x10000000, 0
v_add_u32 v20, s78, v1
s_lshl_b32 s89, s62, 10
s_and_b32 s96, s62, 3
s_lshl_b32 s96, s96, 1
s_lshr_b32 s97, s62, 2
s_add_u32 s88, s96, s97
s_and_b32 s96, s68, 1
s_lshl_b32 s96, s96, 1
s_lshr_b32 s97, s68, 1
s_add_u32 s96, s96, s97
s_lshl_b32 s96, s96, 2
s_add_u32 s96, s96, s67
s_lshl_b32 s96, s96, 18
s_add_u32 s96, 0x100000, s96
s_xor_b32 s97, s62, s80
s_lshl_b32 s97, s97, 12
s_add_u32 s74, s96, s97
s_lshl_b32 s75, s62, 12
s_lshl_b32 s96, s69, 2
s_add_u32 s96, s96, s67
s_lshl_b32 s96, s96, 5
s_add_u32 s96, s96, s88
s_lshl_b32 s96, s96, 12
s_add_u32 s76, 0x1000000, s96
s_lshl_b32 s96, s69, 5
s_add_u32 s96, s96, s88
s_lshl_b32 s96, s96, 12
s_add_u32 s96, 0x500000, s96
s_lshl_b32 s97, s67, 10
s_add_u32 s96, s96, s97
s_lshl_b32 s97, s68, 8
s_add_u32 s77, s96, s97
s_lshl_b32 s78, s68, 4
s_lshl_b64 s[86:87], 0xffff, s78
s_not_b64 s[86:87], s[86:87]
s_lshr_b32 s96, s62, 2
s_lshl_b32 s96, s96, 14
v_add_u32 v21, s96, v1
v_add_u32 v22, 0x10000, v21
s_lshl_b32 s96, s62, 10
v_add_u32 v12, s96, v1
v_add_u32 v23, 0x20000, v12
v_xor_b32 v24, 0x1000, v23
v_lshrrev_b32 v12, 4, v0
v_lshlrev_b32 v12, 10, v12
v_and_b32 v13, 15, v0
v_lshlrev_b32 v13, 4, v13
v_add_u32 v12, v12, v13
s_lshl_b32 s96, s68, 8
v_add_u32 v25, s96, v12
s_lshl_b32 s96, s68, 10
v_add_u32 v27, s96, v1
v_and_b32 v12, 15, v0
v_lshlrev_b32 v12, 8, v12
v_lshrrev_b32 v13, 4, v0
v_xor_b32 v13, v13, v0
v_and_b32 v13, 3, v13
v_lshlrev_b32 v13, 6, v13
v_add_u32 v12, v12, v13
v_and_b32 v13, 12, v0
v_lshlrev_b32 v13, 2, v13
v_add_u32 v112, v12, v13
v_xor_b32 v113, 16, v112
v_xor_b32 v114, 32, v112
v_xor_b32 v115, 48, v112
v_add_u32 v116, s84, v112
v_add_u32 v117, s84, v113
v_add_u32 v118, s84, v114
v_add_u32 v119, s84, v115
s_lshl_b32 s96, s65, 1
s_lshr_b32 s97, s66, 4
s_add_u32 s96, s96, s97
s_lshl_b32 s96, s96, 2
s_add_u32 s96, s96, s67
s_lshl_b32 s96, s96, 2
s_and_b32 s97, s62, 3
s_add_u32 s96, s96, s97
s_lshl_b32 s96, s96, 16
s_add_u32 s96, 0x100000, s96
s_lshr_b32 s97, s66, 1
s_and_b32 s97, s97, 7
s_lshl_b32 s97, s97, 12
s_add_u32 s96, s96, s97
s_and_b32 s97, s66, 1
s_lshl_b32 s97, s97, 9
s_add_u32 s79, s96, s97
v_lshrrev_b32 v12, 4, v0
v_lshlrev_b32 v12, 10, v12
v_and_b32 v13, 15, v0
v_lshlrev_b32 v13, 4, v13
v_add_u32 v7, v12, v13
s_add_u32 m0, s84, 0x0
s_nop 0
buffer_load_dwordx4 v120, s[32:35], s71 offen lds
s_add_u32 m0, s84, 0x0
s_nop 0
buffer_load_dwordx4 v121, s[32:35], s71 offen offset:1024 lds
s_add_u32 m0, s84, 0x0
s_nop 0
buffer_load_dwordx4 v122, s[32:35], s71 offen offset:2048 lds
s_add_u32 m0, s84, 0x0
s_nop 0
buffer_load_dwordx4 v123, s[32:35], s71 offen offset:3072 lds
s_add_u32 m0, s84, 0x1000
s_add_u32 s78, s71, 0x1000
buffer_load_dwordx4 v120, s[32:35], s78 offen lds
s_add_u32 m0, s84, 0x1000
s_add_u32 s78, s71, 0x1000
buffer_load_dwordx4 v121, s[32:35], s78 offen offset:1024 lds
s_add_u32 m0, s84, 0x1000
s_add_u32 s78, s71, 0x1000
buffer_load_dwordx4 v122, s[32:35], s78 offen offset:2048 lds
s_add_u32 m0, s84, 0x1000
s_add_u32 s78, s71, 0x1000
buffer_load_dwordx4 v123, s[32:35], s78 offen offset:3072 lds
s_add_u32 m0, s84, 0x2000
s_nop 0
buffer_load_dwordx4 v120, s[32:35], s72 offen lds
buffer_load_dwordx2 v[10:11], off, s[36:39], 0 sc1
s_add_u32 m0, s84, 0x2000
s_nop 0
buffer_load_dwordx4 v121, s[32:35], s72 offen offset:1024 lds
s_add_u32 m0, s84, 0x2000
s_nop 0
buffer_load_dwordx4 v122, s[32:35], s72 offen offset:2048 lds
s_add_u32 m0, s84, 0x2000
s_nop 0
buffer_load_dwordx4 v123, s[32:35], s72 offen offset:3072 lds
s_add_u32 m0, s84, 0x3000
s_add_u32 s78, s72, 0x1000
buffer_load_dwordx4 v120, s[32:35], s78 offen lds
s_add_u32 m0, s84, 0x3000
s_add_u32 s78, s72, 0x1000
buffer_load_dwordx4 v121, s[32:35], s78 offen offset:1024 lds
s_add_u32 m0, s84, 0x3000
s_add_u32 s78, s72, 0x1000
buffer_load_dwordx4 v122, s[32:35], s78 offen offset:2048 lds
s_add_u32 m0, s84, 0x3000
s_add_u32 s78, s72, 0x1000
buffer_load_dwordx4 v123, s[32:35], s78 offen offset:3072 lds
buffer_load_dwordx4 v[224:227], v4, s[32:35], s70 offen
buffer_load_dwordx4 v[228:231], v4, s[32:35], s70 offen offset:1024
buffer_load_dwordx4 v[232:235], v4, s[32:35], s70 offen offset:2048
buffer_load_dwordx4 v[236:239], v4, s[32:35], s70 offen offset:3072
s_add_u32 s78, s70, 0x1000
buffer_load_dwordx4 v[240:243], v4, s[32:35], s78 offen
s_add_u32 s78, s70, 0x1000
buffer_load_dwordx4 v[244:247], v4, s[32:35], s78 offen offset:1024
s_add_u32 s78, s70, 0x1000
buffer_load_dwordx4 v[248:251], v4, s[32:35], s78 offen offset:2048
s_add_u32 s78, s70, 0x1000
buffer_load_dwordx4 v[252:255], v4, s[32:35], s78 offen offset:3072
s_add_u32 m0, s84, 0x4000
s_nop 0
buffer_load_dwordx4 v120, s[32:35], s73 offen lds
s_add_u32 m0, s84, 0x4000
s_nop 0
buffer_load_dwordx4 v121, s[32:35], s73 offen offset:1024 lds
s_add_u32 m0, s84, 0x4000
s_nop 0
buffer_load_dwordx4 v122, s[32:35], s73 offen offset:2048 lds
s_add_u32 m0, s84, 0x4000
s_nop 0
buffer_load_dwordx4 v123, s[32:35], s73 offen offset:3072 lds
s_add_u32 m0, s84, 0x5000
s_add_u32 s78, s73, 0x1000
buffer_load_dwordx4 v120, s[32:35], s78 offen lds
s_add_u32 m0, s84, 0x5000
s_add_u32 s78, s73, 0x1000
buffer_load_dwordx4 v121, s[32:35], s78 offen offset:1024 lds
s_add_u32 m0, s84, 0x5000
s_add_u32 s78, s73, 0x1000
buffer_load_dwordx4 v122, s[32:35], s78 offen offset:2048 lds
s_add_u32 m0, s84, 0x5000
s_add_u32 s78, s73, 0x1000
buffer_load_dwordx4 v123, s[32:35], s78 offen offset:3072 lds
s_cmp_eq_u32 s62, 6
s_cbranch_scc0 .Lfk_else109
v_and_b32 v12, 7, v0
v_lshlrev_b32 v12, 7, v12
s_lshl_b32 s96, s83, 4
s_add_u32 s96, s96, s82
s_lshl_b32 s96, s96, 10
s_add_u32 s96, s96, 0x40000
v_add_u32 v109, s96, v12
s_lshl_b32 s96, s69, 4
s_lshl_b32 s97, s67, 2
s_add_u32 s96, s96, s97
s_add_u32 s96, s96, s68
s_lshl_b32 s96, s96, 7
s_add_u32 s78, 0xd0000, s96
v_writelane_b32 v109, s78, 8
s_lshl_b32 s96, s69, 12
s_add_u32 s96, 0x10000, s96
s_lshl_b32 s97, s67, 2
s_add_u32 s97, s97, s68
s_lshl_b32 s97, s97, 7
s_add_u32 s78, s96, s97
v_writelane_b32 v109, s78, 9
s_lshl_b32 s96, s82, 12
s_add_u32 s96, 0x10800, s96
s_lshl_b32 s97, s83, 7
s_add_u32 s78, s96, s97
v_writelane_b32 v109, s78, 10
s_and_b32 s96, s2, 3
s_lshl_b32 s96, s96, 6
s_lshr_b32 s97, s2, 2
s_and_b32 s97, s97, 7
s_lshl_b32 s97, s97, 3
s_add_u32 s96, s96, s97
s_lshr_b32 s97, s2, 5
s_add_u32 s96, s96, s97
s_lshl_b32 s96, s96, 7
s_add_u32 s78, 0xc0000, s96
v_writelane_b32 v109, s78, 11
s_lshl_b32 s96, s65, 2
s_add_u32 s96, s96, s67
s_lshl_b32 s96, s96, 4
s_lshr_b32 s97, s66, 1
s_add_u32 s96, s96, s97
s_lshl_b32 s96, s96, 7
s_add_u32 s78, 0x20000, s96
v_writelane_b32 v109, s78, 12
buffer_load_dword v9, v109, s[36:39], 0 offen sc1
.Lfk_else109:
s_lshl_b32 s96, s69, 7
s_lshl_b32 s97, s88, 4
s_add_u32 s96, s96, s97
v_and_b32 v12, 15, v0
v_add_u32 v12, s96, v12
v_lshlrev_b32 v12, 10, v12
v_lshrrev_b32 v13, 4, v0
v_lshlrev_b32 v13, 4, v13
v_add_u32 v12, v12, v13
s_lshl_b32 s96, s67, 8
s_lshl_b32 s97, s68, 6
s_add_u32 s96, s96, s97
v_add_u32 v12, s96, v12
buffer_load_dwordx4 v[16:19], v12, s[24:27], 0 offen
s_waitcnt vmcnt(26)
ds_read_b128 v[200:203], v116
ds_read_b128 v[204:207], v116 offset:4096
ds_read_b128 v[208:211], v117
ds_read_b128 v[212:215], v117 offset:4096
s_setprio 3
s_barrier
ds_read_b128 v[64:67], v112 offset:16384
ds_read_b128 v[68:71], v112 offset:20480
ds_read_b128 v[72:75], v112 offset:24576
ds_read_b128 v[76:79], v112 offset:28672
ds_read_b128 v[80:83], v113 offset:16384
ds_read_b128 v[84:87], v113 offset:20480
ds_read_b128 v[88:91], v113 offset:24576
ds_read_b128 v[92:95], v113 offset:28672
s_waitcnt lgkmcnt(7)
v_mfma_f32_16x16x32_bf16 v[32:35], v[64:67], v[200:203], 0
ds_read_b128 v[216:219], v118
s_waitcnt lgkmcnt(7)
v_mfma_f32_16x16x32_bf16 v[40:43], v[68:71], v[200:203], 0
ds_read_b128 v[96:99], v114 offset:16384
s_waitcnt lgkmcnt(7)
v_mfma_f32_16x16x32_bf16 v[48:51], v[72:75], v[200:203], 0
ds_read_b128 v[100:103], v114 offset:20480
s_waitcnt lgkmcnt(7)
v_mfma_f32_16x16x32_bf16 v[56:59], v[76:79], v[200:203], 0
ds_read_b128 v[104:107], v114 offset:24576
v_mfma_f32_16x16x32_bf16 v[36:39], v[64:67], v[204:207], 0
ds_read_b128 v[108:111], v114 offset:28672
v_mfma_f32_16x16x32_bf16 v[44:47], v[68:71], v[204:207], 0
ds_read_b128 v[220:223], v118 offset:4096
v_mfma_f32_16x16x32_bf16 v[52:55], v[72:75], v[204:207], 0
v_mfma_f32_16x16x32_bf16 v[60:63], v[76:79], v[204:207], 0
s_waitcnt lgkmcnt(9)
v_mfma_f32_16x16x32_bf16 v[32:35], v[80:83], v[208:211], v[32:35]
ds_read_b128 v[200:203], v119
s_waitcnt lgkmcnt(9)
v_mfma_f32_16x16x32_bf16 v[40:43], v[84:87], v[208:211], v[40:43]
ds_read_b128 v[64:67], v115 offset:16384
s_waitcnt lgkmcnt(9)
v_mfma_f32_16x16x32_bf16 v[48:51], v[88:91], v[208:211], v[48:51]
ds_read_b128 v[68:71], v115 offset:20480
s_waitcnt lgkmcnt(9)
v_mfma_f32_16x16x32_bf16 v[56:59], v[92:95], v[208:211], v[56:59]
ds_read_b128 v[72:75], v115 offset:24576
v_mfma_f32_16x16x32_bf16 v[36:39], v[80:83], v[212:215], v[36:39]
ds_read_b128 v[76:79], v115 offset:28672
v_mfma_f32_16x16x32_bf16 v[44:47], v[84:87], v[212:215], v[44:47]
ds_read_b128 v[204:207], v119 offset:4096
v_mfma_f32_16x16x32_bf16 v[52:55], v[88:91], v[212:215], v[52:55]
v_mfma_f32_16x16x32_bf16 v[60:63], v[92:95], v[212:215], v[60:63]
s_waitcnt lgkmcnt(10)
v_mfma_f32_16x16x32_bf16 v[32:35], v[96:99], v[216:219], v[32:35]
s_waitcnt vmcnt(17)
ds_read_b128 v[208:211], v116 offset:8192
s_mov_b64 vcc, s[56:57]
s_waitcnt vmcnt(16)
v_mov_b32_dpp v12, v224 quad_perm:[1,0,3,2] row_mask:0xf bank_mask:0xf
s_waitcnt lgkmcnt(10)
v_mfma_f32_16x16x32_bf16 v[40:43], v[100:103], v[216:219], v[40:43]
ds_read_b128 v[80:83], v112 offset:32768
s_waitcnt vmcnt(15)
v_cndmask_b32_dpp v224, v228, v224, vcc quad_perm:[1,0,3,2] row_mask:0xf bank_mask:0xf
v_cndmask_b32_e64 v228, v228, v12, s[56:57]
s_waitcnt lgkmcnt(10)
v_mfma_f32_16x16x32_bf16 v[48:51], v[104:107], v[216:219], v[48:51]
ds_read_b128 v[84:87], v112 offset:36864
v_mov_b32_dpp v13, v225 quad_perm:[1,0,3,2] row_mask:0xf bank_mask:0xf
v_cndmask_b32_dpp v225, v229, v225, vcc quad_perm:[1,0,3,2] row_mask:0xf bank_mask:0xf
s_waitcnt lgkmcnt(10)
v_mfma_f32_16x16x32_bf16 v[56:59], v[108:111], v[216:219], v[56:59]
ds_read_b128 v[88:91], v112 offset:40960
v_cndmask_b32_e64 v229, v229, v13, s[56:57]
v_mov_b32_dpp v12, v226 quad_perm:[1,0,3,2] row_mask:0xf bank_mask:0xf
s_waitcnt lgkmcnt(10)
v_mfma_f32_16x16x32_bf16 v[36:39], v[96:99], v[220:223], v[36:39]
ds_read_b128 v[92:95], v112 offset:45056
v_cndmask_b32_dpp v226, v230, v226, vcc quad_perm:[1,0,3,2] row_mask:0xf bank_mask:0xf
v_cndmask_b32_e64 v230, v230, v12, s[56:57]
v_mfma_f32_16x16x32_bf16 v[44:47], v[100:103], v[220:223], v[44:47]
ds_read_b128 v[212:215], v116 offset:12288
v_mov_b32_dpp v13, v227 quad_perm:[1,0,3,2] row_mask:0xf bank_mask:0xf
v_cndmask_b32_dpp v227, v231, v227, vcc quad_perm:[1,0,3,2] row_mask:0xf bank_mask:0xf
v_mfma_f32_16x16x32_bf16 v[52:55], v[104:107], v[220:223], v[52:55]
v_cndmask_b32_e64 v231, v231, v13, s[56:57]
s_waitcnt vmcnt(14)
v_mov_b32_dpp v12, v232 quad_perm:[1,0,3,2] row_mask:0xf bank_mask:0xf
v_mfma_f32_16x16x32_bf16 v[60:63], v[108:111], v[220:223], v[60:63]
s_waitcnt vmcnt(13)
v_cndmask_b32_dpp v232, v236, v232, vcc quad_perm:[1,0,3,2] row_mask:0xf bank_mask:0xf
v_cndmask_b32_e64 v236, v236, v12, s[56:57]
s_waitcnt lgkmcnt(10)
v_mfma_f32_16x16x32_bf16 v[32:35], v[64:67], v[200:203], v[32:35]
ds_read_b128 v[216:219], v117 offset:8192
v_mov_b32_dpp v13, v233 quad_perm:[1,0,3,2] row_mask:0xf bank_mask:0xf
v_cndmask_b32_dpp v233, v237, v233, vcc quad_perm:[1,0,3,2] row_mask:0xf bank_mask:0xf
s_waitcnt lgkmcnt(10)
v_mfma_f32_16x16x32_bf16 v[40:43], v[68:71], v[200:203], v[40:43]
ds_read_b128 v[96:99], v113 offset:32768
v_cndmask_b32_e64 v237, v237, v13, s[56:57]
v_mov_b32_dpp v12, v234 quad_perm:[1,0,3,2] row_mask:0xf bank_mask:0xf
s_waitcnt lgkmcnt(10)
v_mfma_f32_16x16x32_bf16 v[48:51], v[72:75], v[200:203], v[48:51]
ds_read_b128 v[100:103], v113 offset:36864
v_cndmask_b32_dpp v234, v238, v234, vcc quad_perm:[1,0,3,2] row_mask:0xf bank_mask:0xf
v_cndmask_b32_e64 v238, v238, v12, s[56:57]
s_waitcnt lgkmcnt(10)
v_mfma_f32_16x16x32_bf16 v[56:59], v[76:79], v[200:203], v[56:59]
ds_read_b128 v[104:107], v113 offset:40960
v_mov_b32_dpp v13, v235 quad_perm:[1,0,3,2] row_mask:0xf bank_mask:0xf
v_cndmask_b32_dpp v235, v239, v235, vcc quad_perm:[1,0,3,2] row_mask:0xf bank_mask:0xf
s_waitcnt lgkmcnt(10)
v_mfma_f32_16x16x32_bf16 v[36:39], v[64:67], v[204:207], v[36:39]
ds_read_b128 v[108:111], v113 offset:45056
v_cndmask_b32_e64 v239, v239, v13, s[56:57]
s_mov_b64 vcc, s[58:59]
v_mfma_f32_16x16x32_bf16 v[44:47], v[68:71], v[204:207], v[44:47]
ds_read_b128 v[220:223], v117 offset:12288
v_mov_b32_dpp v12, v224 quad_perm:[2,3,0,1] row_mask:0xf bank_mask:0xf
v_cndmask_b32_dpp v224, v232, v224, vcc quad_perm:[2,3,0,1] row_mask:0xf bank_mask:0xf
v_mfma_f32_16x16x32_bf16 v[52:55], v[72:75], v[204:207], v[52:55]
v_cndmask_b32_e64 v232, v232, v12, s[58:59]
v_mov_b32_dpp v13, v225 quad_perm:[2,3,0,1] row_mask:0xf bank_mask:0xf
v_mfma_f32_16x16x32_bf16 v[60:63], v[76:79], v[204:207], v[60:63]
v_cndmask_b32_dpp v225, v233, v225, vcc quad_perm:[2,3,0,1] row_mask:0xf bank_mask:0xf
v_cndmask_b32_e64 v233, v233, v13, s[58:59]
s_waitcnt lgkmcnt(10)
v_mfma_f32_16x16x32_bf16 v[32:35], v[80:83], v[208:211], v[32:35]
ds_read_b128 v[200:203], v118 offset:8192
v_mov_b32_dpp v12, v226 quad_perm:[2,3,0,1] row_mask:0xf bank_mask:0xf
v_cndmask_b32_dpp v226, v234, v226, vcc quad_perm:[2,3,0,1] row_mask:0xf bank_mask:0xf
s_waitcnt lgkmcnt(10)
v_mfma_f32_16x16x32_bf16 v[40:43], v[84:87], v[208:211], v[40:43]
ds_read_b128 v[64:67], v114 offset:32768
v_cndmask_b32_e64 v234, v234, v12, s[58:59]
v_mov_b32_dpp v13, v227 quad_perm:[2,3,0,1] row_mask:0xf bank_mask:0xf
s_waitcnt lgkmcnt(10)
v_mfma_f32_16x16x32_bf16 v[48:51], v[88:91], v[208:211], v[48:51]
ds_read_b128 v[68:71], v114 offset:36864
v_cndmask_b32_dpp v227, v235, v227, vcc quad_perm:[2,3,0,1] row_mask:0xf bank_mask:0xf
v_cndmask_b32_e64 v235, v235, v13, s[58:59]
s_waitcnt lgkmcnt(10)
v_mfma_f32_16x16x32_bf16 v[56:59], v[92:95], v[208:211], v[56:59]
ds_read_b128 v[72:75], v114 offset:40960
v_mov_b32_dpp v12, v228 quad_perm:[2,3,0,1] row_mask:0xf bank_mask:0xf
v_cndmask_b32_dpp v228, v236, v228, vcc quad_perm:[2,3,0,1] row_mask:0xf bank_mask:0xf
s_waitcnt lgkmcnt(10)
v_mfma_f32_16x16x32_bf16 v[36:39], v[80:83], v[212:215], v[36:39]
ds_read_b128 v[76:79], v114 offset:45056
v_cndmask_b32_e64 v236, v236, v12, s[58:59]
v_mov_b32_dpp v13, v229 quad_perm:[2,3,0,1] row_mask:0xf bank_mask:0xf
v_mfma_f32_16x16x32_bf16 v[44:47], v[84:87], v[212:215], v[44:47]
ds_read_b128 v[204:207], v118 offset:12288
v_cndmask_b32_dpp v229, v237, v229, vcc quad_perm:[2,3,0,1] row_mask:0xf bank_mask:0xf
v_cndmask_b32_e64 v237, v237, v13, s[58:59]
v_mfma_f32_16x16x32_bf16 v[52:55], v[88:91], v[212:215], v[52:55]
v_mov_b32_dpp v12, v230 quad_perm:[2,3,0,1] row_mask:0xf bank_mask:0xf
v_cndmask_b32_dpp v230, v238, v230, vcc quad_perm:[2,3,0,1] row_mask:0xf bank_mask:0xf
v_mfma_f32_16x16x32_bf16 v[60:63], v[92:95], v[212:215], v[60:63]
v_cndmask_b32_e64 v238, v238, v12, s[58:59]
v_mov_b32_dpp v13, v231 quad_perm:[2,3,0,1] row_mask:0xf bank_mask:0xf
s_waitcnt lgkmcnt(10)
v_mfma_f32_16x16x32_bf16 v[32:35], v[96:99], v[216:219], v[32:35]
ds_read_b128 v[208:211], v119 offset:8192
v_cndmask_b32_dpp v231, v239, v231, vcc quad_perm:[2,3,0,1] row_mask:0xf bank_mask:0xf
s_waitcnt lgkmcnt(10)
v_mfma_f32_16x16x32_bf16 v[40:43], v[100:103], v[216:219], v[40:43]
ds_read_b128 v[80:83], v115 offset:32768
v_cndmask_b32_e64 v239, v239, v13, s[58:59]
s_waitcnt lgkmcnt(10)
v_mfma_f32_16x16x32_bf16 v[48:51], v[104:107], v[216:219], v[48:51]
ds_read_b128 v[84:87], v115 offset:36864
s_mov_b64 vcc, s[56:57]
s_waitcnt lgkmcnt(10)
v_mfma_f32_16x16x32_bf16 v[56:59], v[108:111], v[216:219], v[56:59]
ds_read_b128 v[88:91], v115 offset:40960
s_waitcnt vmcnt(12)
v_mov_b32_dpp v12, v240 quad_perm:[1,0,3,2] row_mask:0xf bank_mask:0xf
s_waitcnt lgkmcnt(10)
v_mfma_f32_16x16x32_bf16 v[36:39], v[96:99], v[220:223], v[36:39]
ds_read_b128 v[92:95], v115 offset:45056
s_waitcnt vmcnt(11)
v_cndmask_b32_dpp v240, v244, v240, vcc quad_perm:[1,0,3,2] row_mask:0xf bank_mask:0xf
v_mfma_f32_16x16x32_bf16 v[44:47], v[100:103], v[220:223], v[44:47]
ds_read_b128 v[212:215], v119 offset:12288
v_cndmask_b32_e64 v244, v244, v12, s[56:57]
v_mfma_f32_16x16x32_bf16 v[52:55], v[104:107], v[220:223], v[52:55]
v_mov_b32_dpp v13, v241 quad_perm:[1,0,3,2] row_mask:0xf bank_mask:0xf
v_mfma_f32_16x16x32_bf16 v[60:63], v[108:111], v[220:223], v[60:63]
v_cndmask_b32_dpp v241, v245, v241, vcc quad_perm:[1,0,3,2] row_mask:0xf bank_mask:0xf
s_waitcnt lgkmcnt(10)
v_mfma_f32_16x16x32_bf16 v[32:35], v[64:67], v[200:203], v[32:35]
s_waitcnt vmcnt(1)
ds_read_b128 v[216:219], v116 offset:16384
v_cndmask_b32_e64 v245, v245, v13, s[56:57]
s_waitcnt lgkmcnt(10)
v_mfma_f32_16x16x32_bf16 v[40:43], v[68:71], v[200:203], v[40:43]
ds_read_b128 v[96:99], v112 offset:49152
v_mov_b32_dpp v12, v242 quad_perm:[1,0,3,2] row_mask:0xf bank_mask:0xf
s_waitcnt lgkmcnt(10)
v_mfma_f32_16x16x32_bf16 v[48:51], v[72:75], v[200:203], v[48:51]
ds_read_b128 v[100:103], v112 offset:53248
v_cndmask_b32_dpp v242, v246, v242, vcc quad_perm:[1,0,3,2] row_mask:0xf bank_mask:0xf
s_waitcnt lgkmcnt(10)
v_mfma_f32_16x16x32_bf16 v[56:59], v[76:79], v[200:203], v[56:59]
ds_read_b128 v[104:107], v112 offset:57344
v_cndmask_b32_e64 v246, v246, v12, s[56:57]
s_waitcnt lgkmcnt(10)
v_mfma_f32_16x16x32_bf16 v[36:39], v[64:67], v[204:207], v[36:39]
ds_read_b128 v[108:111], v112 offset:61440
v_mov_b32_dpp v13, v243 quad_perm:[1,0,3,2] row_mask:0xf bank_mask:0xf
v_mfma_f32_16x16x32_bf16 v[44:47], v[68:71], v[204:207], v[44:47]
ds_read_b128 v[220:223], v116 offset:20480
v_cndmask_b32_dpp v243, v247, v243, vcc quad_perm:[1,0,3,2] row_mask:0xf bank_mask:0xf
v_mfma_f32_16x16x32_bf16 v[52:55], v[72:75], v[204:207], v[52:55]
v_cndmask_b32_e64 v247, v247, v13, s[56:57]
v_mfma_f32_16x16x32_bf16 v[60:63], v[76:79], v[204:207], v[60:63]
v_mov_b32_dpp v12, v248 quad_perm:[1,0,3,2] row_mask:0xf bank_mask:0xf
s_waitcnt lgkmcnt(10)
v_mfma_f32_16x16x32_bf16 v[32:35], v[80:83], v[208:211], v[32:35]
ds_read_b128 v[200:203], v117 offset:16384
v_cndmask_b32_dpp v248, v252, v248, vcc quad_perm:[1,0,3,2] row_mask:0xf bank_mask:0xf
s_waitcnt lgkmcnt(10)
v_mfma_f32_16x16x32_bf16 v[40:43], v[84:87], v[208:211], v[40:43]
ds_read_b128 v[64:67], v113 offset:49152
v_cndmask_b32_e64 v252, v252, v12, s[56:57]
s_waitcnt lgkmcnt(10)
v_mfma_f32_16x16x32_bf16 v[48:51], v[88:91], v[208:211], v[48:51]
ds_read_b128 v[68:71], v113 offset:53248
v_mov_b32_dpp v13, v249 quad_perm:[1,0,3,2] row_mask:0xf bank_mask:0xf
s_waitcnt lgkmcnt(10)
v_mfma_f32_16x16x32_bf16 v[56:59], v[92:95], v[208:211], v[56:59]
ds_read_b128 v[72:75], v113 offset:57344
v_cndmask_b32_dpp v249, v253, v249, vcc quad_perm:[1,0,3,2] row_mask:0xf bank_mask:0xf
s_waitcnt lgkmcnt(10)
v_mfma_f32_16x16x32_bf16 v[36:39], v[80:83], v[212:215], v[36:39]
ds_read_b128 v[76:79], v113 offset:61440
v_cndmask_b32_e64 v253, v253, v13, s[56:57]
v_mfma_f32_16x16x32_bf16 v[44:47], v[84:87], v[212:215], v[44:47]
ds_read_b128 v[204:207], v117 offset:20480
v_mov_b32_dpp v12, v250 quad_perm:[1,0,3,2] row_mask:0xf bank_mask:0xf
v_mfma_f32_16x16x32_bf16 v[52:55], v[88:91], v[212:215], v[52:55]
v_cndmask_b32_dpp v250, v254, v250, vcc quad_perm:[1,0,3,2] row_mask:0xf bank_mask:0xf
v_mfma_f32_16x16x32_bf16 v[60:63], v[92:95], v[212:215], v[60:63]
v_cndmask_b32_e64 v254, v254, v12, s[56:57]
s_waitcnt lgkmcnt(10)
v_mfma_f32_16x16x32_bf16 v[32:35], v[96:99], v[216:219], v[32:35]
ds_read_b128 v[208:211], v118 offset:16384
v_mov_b32_dpp v13, v251 quad_perm:[1,0,3,2] row_mask:0xf bank_mask:0xf
s_waitcnt lgkmcnt(10)
v_mfma_f32_16x16x32_bf16 v[40:43], v[100:103], v[216:219], v[40:43]
ds_read_b128 v[80:83], v114 offset:49152
v_cndmask_b32_dpp v251, v255, v251, vcc quad_perm:[1,0,3,2] row_mask:0xf bank_mask:0xf
s_waitcnt lgkmcnt(10)
v_mfma_f32_16x16x32_bf16 v[48:51], v[104:107], v[216:219], v[48:51]
ds_read_b128 v[84:87], v114 offset:53248
v_cndmask_b32_e64 v255, v255, v13, s[56:57]
s_waitcnt lgkmcnt(10)
v_mfma_f32_16x16x32_bf16 v[56:59], v[108:111], v[216:219], v[56:59]
ds_read_b128 v[88:91], v114 offset:57344
s_mov_b64 vcc, s[58:59]
s_waitcnt lgkmcnt(10)
v_mfma_f32_16x16x32_bf16 v[36:39], v[96:99], v[220:223], v[36:39]
ds_read_b128 v[92:95], v114 offset:61440
v_mov_b32_dpp v12, v240 quad_perm:[2,3,0,1] row_mask:0xf bank_mask:0xf
v_mfma_f32_16x16x32_bf16 v[44:47], v[100:103], v[220:223], v[44:47]
ds_read_b128 v[212:215], v118 offset:20480
v_cndmask_b32_dpp v240, v248, v240, vcc quad_perm:[2,3,0,1] row_mask:0xf bank_mask:0xf
v_mfma_f32_16x16x32_bf16 v[52:55], v[104:107], v[220:223], v[52:55]
v_cndmask_b32_e64 v248, v248, v12, s[58:59]
v_mfma_f32_16x16x32_bf16 v[60:63], v[108:111], v[220:223], v[60:63]
v_mov_b32_dpp v13, v241 quad_perm:[2,3,0,1] row_mask:0xf bank_mask:0xf
s_waitcnt lgkmcnt(10)
v_mfma_f32_16x16x32_bf16 v[32:35], v[64:67], v[200:203], v[32:35]
ds_read_b128 v[216:219], v119 offset:16384
v_cndmask_b32_dpp v241, v249, v241, vcc quad_perm:[2,3,0,1] row_mask:0xf bank_mask:0xf
s_waitcnt lgkmcnt(10)
v_mfma_f32_16x16x32_bf16 v[40:43], v[68:71], v[200:203], v[40:43]
ds_read_b128 v[96:99], v115 offset:49152
v_cndmask_b32_e64 v249, v249, v13, s[58:59]
s_waitcnt lgkmcnt(10)
v_mfma_f32_16x16x32_bf16 v[48:51], v[72:75], v[200:203], v[48:51]
ds_read_b128 v[100:103], v115 offset:53248
v_mov_b32_dpp v12, v242 quad_perm:[2,3,0,1] row_mask:0xf bank_mask:0xf
s_waitcnt lgkmcnt(10)
v_mfma_f32_16x16x32_bf16 v[56:59], v[76:79], v[200:203], v[56:59]
ds_read_b128 v[104:107], v115 offset:57344
v_cndmask_b32_dpp v242, v250, v242, vcc quad_perm:[2,3,0,1] row_mask:0xf bank_mask:0xf
s_waitcnt lgkmcnt(10)
v_mfma_f32_16x16x32_bf16 v[36:39], v[64:67], v[204:207], v[36:39]
ds_read_b128 v[108:111], v115 offset:61440
v_cndmask_b32_e64 v250, v250, v12, s[58:59]
v_mfma_f32_16x16x32_bf16 v[44:47], v[68:71], v[204:207], v[44:47]
ds_read_b128 v[220:223], v119 offset:20480
v_mov_b32_dpp v13, v243 quad_perm:[2,3,0,1] row_mask:0xf bank_mask:0xf
v_mfma_f32_16x16x32_bf16 v[52:55], v[72:75], v[204:207], v[52:55]
v_cndmask_b32_dpp v243, v251, v243, vcc quad_perm:[2,3,0,1] row_mask:0xf bank_mask:0xf
v_mfma_f32_16x16x32_bf16 v[60:63], v[76:79], v[204:207], v[60:63]
v_cndmask_b32_e64 v251, v251, v13, s[58:59]
s_waitcnt lgkmcnt(10)
v_mfma_f32_16x16x32_bf16 v[32:35], v[80:83], v[208:211], v[32:35]
v_mov_b32_dpp v12, v244 quad_perm:[2,3,0,1] row_mask:0xf bank_mask:0xf
s_waitcnt lgkmcnt(9)
v_mfma_f32_16x16x32_bf16 v[40:43], v[84:87], v[208:211], v[40:43]
ds_read_b128 v[64:67], v112
v_cndmask_b32_dpp v244, v252, v244, vcc quad_perm:[2,3,0,1] row_mask:0xf bank_mask:0xf
s_waitcnt lgkmcnt(9)
v_mfma_f32_16x16x32_bf16 v[48:51], v[88:91], v[208:211], v[48:51]
ds_read_b128 v[68:71], v112 offset:4096
v_cndmask_b32_e64 v252, v252, v12, s[58:59]
s_waitcnt lgkmcnt(9)
v_mfma_f32_16x16x32_bf16 v[56:59], v[92:95], v[208:211], v[56:59]
ds_read_b128 v[72:75], v112 offset:8192
v_mov_b32_dpp v13, v245 quad_perm:[2,3,0,1] row_mask:0xf bank_mask:0xf
s_waitcnt lgkmcnt(9)
v_mfma_f32_16x16x32_bf16 v[36:39], v[80:83], v[212:215], v[36:39]
ds_read_b128 v[76:79], v112 offset:12288
v_cndmask_b32_dpp v245, v253, v245, vcc quad_perm:[2,3,0,1] row_mask:0xf bank_mask:0xf
v_mfma_f32_16x16x32_bf16 v[44:47], v[84:87], v[212:215], v[44:47]
v_cndmask_b32_e64 v253, v253, v13, s[58:59]
v_mfma_f32_16x16x32_bf16 v[52:55], v[88:91], v[212:215], v[52:55]
v_mov_b32_dpp v12, v246 quad_perm:[2,3,0,1] row_mask:0xf bank_mask:0xf
v_mfma_f32_16x16x32_bf16 v[60:63], v[92:95], v[212:215], v[60:63]
v_cndmask_b32_dpp v246, v254, v246, vcc quad_perm:[2,3,0,1] row_mask:0xf bank_mask:0xf
s_waitcnt lgkmcnt(8)
v_mfma_f32_16x16x32_bf16 v[32:35], v[96:99], v[216:219], v[32:35]
v_cndmask_b32_e64 v254, v254, v12, s[58:59]
s_waitcnt lgkmcnt(7)
v_mfma_f32_16x16x32_bf16 v[40:43], v[100:103], v[216:219], v[40:43]
ds_read_b128 v[80:83], v113
v_mov_b32_dpp v13, v247 quad_perm:[2,3,0,1] row_mask:0xf bank_mask:0xf
s_waitcnt lgkmcnt(7)
v_mfma_f32_16x16x32_bf16 v[48:51], v[104:107], v[216:219], v[48:51]
ds_read_b128 v[84:87], v113 offset:4096
v_cndmask_b32_dpp v247, v255, v247, vcc quad_perm:[2,3,0,1] row_mask:0xf bank_mask:0xf
s_waitcnt lgkmcnt(7)
v_mfma_f32_16x16x32_bf16 v[56:59], v[108:111], v[216:219], v[56:59]
ds_read_b128 v[88:91], v113 offset:8192
v_cndmask_b32_e64 v255, v255, v13, s[58:59]
s_waitcnt lgkmcnt(7)
v_mfma_f32_16x16x32_bf16 v[36:39], v[96:99], v[220:223], v[36:39]
ds_read_b128 v[92:95], v113 offset:12288
v_mfma_f32_16x16x32_bf16 v[44:47], v[100:103], v[220:223], v[44:47]
v_mfma_f32_16x16x32_bf16 v[52:55], v[104:107], v[220:223], v[52:55]
v_mfma_f32_16x16x32_bf16 v[60:63], v[108:111], v[220:223], v[60:63]
s_waitcnt lgkmcnt(7)
v_mfma_f32_16x16x32_bf16 v[32:35], v[64:67], v[224:227], v[32:35]
s_waitcnt lgkmcnt(6)
v_mfma_f32_16x16x32_bf16 v[40:43], v[68:71], v[224:227], v[40:43]
ds_read_b128 v[96:99], v114
s_waitcnt lgkmcnt(6)
v_mfma_f32_16x16x32_bf16 v[48:51], v[72:75], v[224:227], v[48:51]
ds_read_b128 v[100:103], v114 offset:4096
s_waitcnt lgkmcnt(6)
v_mfma_f32_16x16x32_bf16 v[56:59], v[76:79], v[224:227], v[56:59]
ds_read_b128 v[104:107], v114 offset:8192
v_mfma_f32_16x16x32_bf16 v[36:39], v[64:67], v[240:243], v[36:39]
ds_read_b128 v[108:111], v114 offset:12288
v_mfma_f32_16x16x32_bf16 v[44:47], v[68:71], v[240:243], v[44:47]
v_mfma_f32_16x16x32_bf16 v[52:55], v[72:75], v[240:243], v[52:55]
v_mfma_f32_16x16x32_bf16 v[60:63], v[76:79], v[240:243], v[60:63]
s_waitcnt lgkmcnt(7)
v_mfma_f32_16x16x32_bf16 v[32:35], v[80:83], v[228:231], v[32:35]
s_waitcnt lgkmcnt(6)
v_mfma_f32_16x16x32_bf16 v[40:43], v[84:87], v[228:231], v[40:43]
ds_read_b128 v[64:67], v115
s_waitcnt lgkmcnt(6)
v_mfma_f32_16x16x32_bf16 v[48:51], v[88:91], v[228:231], v[48:51]
ds_read_b128 v[68:71], v115 offset:4096
s_waitcnt lgkmcnt(6)
v_mfma_f32_16x16x32_bf16 v[56:59], v[92:95], v[228:231], v[56:59]
ds_read_b128 v[72:75], v115 offset:8192
v_mfma_f32_16x16x32_bf16 v[36:39], v[80:83], v[244:247], v[36:39]
ds_read_b128 v[76:79], v115 offset:12288
v_mfma_f32_16x16x32_bf16 v[44:47], v[84:87], v[244:247], v[44:47]
v_mfma_f32_16x16x32_bf16 v[52:55], v[88:91], v[244:247], v[52:55]
v_mfma_f32_16x16x32_bf16 v[60:63], v[92:95], v[244:247], v[60:63]
s_waitcnt lgkmcnt(7)
v_mfma_f32_16x16x32_bf16 v[32:35], v[96:99], v[232:235], v[32:35]
s_waitcnt lgkmcnt(6)
v_mfma_f32_16x16x32_bf16 v[40:43], v[100:103], v[232:235], v[40:43]
s_waitcnt lgkmcnt(5)
v_mfma_f32_16x16x32_bf16 v[48:51], v[104:107], v[232:235], v[48:51]
s_waitcnt lgkmcnt(4)
v_mfma_f32_16x16x32_bf16 v[56:59], v[108:111], v[232:235], v[56:59]
v_mfma_f32_16x16x32_bf16 v[36:39], v[96:99], v[248:251], v[36:39]
v_mfma_f32_16x16x32_bf16 v[44:47], v[100:103], v[248:251], v[44:47]
v_mfma_f32_16x16x32_bf16 v[52:55], v[104:107], v[248:251], v[52:55]
v_mfma_f32_16x16x32_bf16 v[60:63], v[108:111], v[248:251], v[60:63]
s_waitcnt lgkmcnt(3)
v_mfma_f32_16x16x32_bf16 v[32:35], v[64:67], v[236:239], v[32:35]
v_readfirstlane_b32 s60, v10
v_readfirstlane_b32 s61, v11
s_add_u32 s60, s60, 1
s_addc_u32 s61, s61, 0
v_mov_b32 v10, s60
v_mov_b32 v11, s61
s_waitcnt lgkmcnt(2)
v_mfma_f32_16x16x32_bf16 v[40:43], v[68:71], v[236:239], v[40:43]
s_waitcnt lgkmcnt(1)
v_mfma_f32_16x16x32_bf16 v[48:51], v[72:75], v[236:239], v[48:51]
s_waitcnt lgkmcnt(0)
v_mfma_f32_16x16x32_bf16 v[56:59], v[76:79], v[236:239], v[56:59]
v_mfma_f32_16x16x32_bf16 v[36:39], v[64:67], v[252:255], v[36:39]
v_mfma_f32_16x16x32_bf16 v[44:47], v[68:71], v[252:255], v[44:47]
v_mfma_f32_16x16x32_bf16 v[52:55], v[72:75], v[252:255], v[52:55]
v_mfma_f32_16x16x32_bf16 v[60:63], v[76:79], v[252:255], v[60:63]
s_add_u32 s78, s79, 0x8000
v_cvt_pk_bf16_f32 v64, v32, v33
v_cvt_pk_bf16_f32 v65, v34, v35
v_cvt_pk_bf16_f32 v66, v40, v41
v_cvt_pk_bf16_f32 v67, v42, v43
buffer_store_dwordx4 v[64:67], v7, s[36:39], s79 offen sc1
v_cvt_pk_bf16_f32 v68, v48, v49
v_cvt_pk_bf16_f32 v69, v50, v51
v_cvt_pk_bf16_f32 v70, v56, v57
v_cvt_pk_bf16_f32 v71, v58, v59
buffer_store_dwordx4 v[68:71], v7, s[36:39], s79 offen offset:256 sc1
v_cvt_pk_bf16_f32 v72, v36, v37
v_cvt_pk_bf16_f32 v73, v38, v39
v_cvt_pk_bf16_f32 v74, v44, v45
v_cvt_pk_bf16_f32 v75, v46, v47
buffer_store_dwordx4 v[72:75], v7, s[36:39], s78 offen sc1
v_cvt_pk_bf16_f32 v76, v52, v53
v_cvt_pk_bf16_f32 v77, v54, v55
v_cvt_pk_bf16_f32 v78, v60, v61
v_cvt_pk_bf16_f32 v79, v62, v63
buffer_store_dwordx4 v[76:79], v7, s[36:39], s78 offen offset:256 sc1
s_lshr_b32 s96, s68, 1
s_lshl_b32 s96, s96, 11
s_and_b32 s97, s62, 4
s_xor_b32 s97, s97, s80
s_lshl_b32 s97, s97, 8
s_add_u32 s96, s96, s97
s_lshr_b32 s97, s62, 2
s_lshl_b32 s97, s97, 16
s_add_u32 s81, s96, s97
buffer_load_dwordx4 v[128:131], v6, s[28:31], s81 offen
s_add_u32 s81, s81, 0x1000
buffer_load_dwordx4 v[136:139], v6, s[28:31], s81 offen
s_add_u32 s81, s81, 0x1000
buffer_load_dwordx4 v[144:147], v6, s[28:31], s81 offen
s_add_u32 s81, s81, 0x1000
buffer_load_dwordx4 v[152:155], v6, s[28:31], s81 offen
s_lshr_b32 s96, s68, 1
s_lshl_b32 s96, s96, 11
s_and_b32 s97, s62, 4
s_xor_b32 s97, s97, s80
s_lshl_b32 s97, s97, 8
s_add_u32 s96, s96, s97
s_lshr_b32 s97, s62, 2
s_xor_b32 s97, s97, 1
s_lshl_b32 s97, s97, 16
s_add_u32 s81, s96, s97
buffer_load_dwordx4 v[132:135], v6, s[28:31], s81 offen
s_add_u32 s81, s81, 0x1000
buffer_load_dwordx4 v[140:143], v6, s[28:31], s81 offen
s_add_u32 s81, s81, 0x1000
buffer_load_dwordx4 v[148:151], v6, s[28:31], s81 offen
s_add_u32 s81, s81, 0x1000
buffer_load_dwordx4 v[156:159], v6, s[28:31], s81 offen
s_lshl_b32 s96, s65, 2
s_add_u32 s96, s96, s67
s_lshl_b32 s96, s96, 4
s_lshr_b32 s97, s66, 1
s_add_u32 s96, s96, s97
s_lshl_b32 s96, s96, 7
s_add_u32 s96, 0x20000, s96
s_and_b32 s97, s66, 1
s_lshl_b32 s97, s97, 2
s_and_b32 s98, s62, 3
s_add_u32 s97, s97, s98
s_lshl_b32 s97, s97, 3
s_add_u32 s78, s96, s97
s_waitcnt vmcnt(8)
s_mov_b64 exec, 1
buffer_store_dwordx2 v[10:11], off, s[36:39], s78 sc1
s_mov_b64 exec, -1
s_setprio 0
s_barrier
s_setprio 1
s_lshr_b32 s96, s68, 1
s_lshl_b32 s96, s96, 11
s_and_b32 s97, s62, 4
s_add_u32 s97, s97, 1
s_xor_b32 s97, s97, s80
s_lshl_b32 s97, s97, 8
s_add_u32 s96, s96, s97
s_lshr_b32 s97, s62, 2
s_lshl_b32 s97, s97, 16
s_add_u32 s81, s96, s97
buffer_load_dwordx4 v[160:163], v6, s[28:31], s81 offen
s_add_u32 s81, s81, 0x1000
buffer_load_dwordx4 v[168:171], v6, s[28:31], s81 offen
s_add_u32 s81, s81, 0x1000
buffer_load_dwordx4 v[176:179], v6, s[28:31], s81 offen
s_add_u32 s81, s81, 0x1000
buffer_load_dwordx4 v[184:187], v6, s[28:31], s81 offen
s_lshr_b32 s96, s68, 1
s_lshl_b32 s96, s96, 11
s_and_b32 s97, s62, 4
s_add_u32 s97, s97, 1
s_xor_b32 s97, s97, s80
s_lshl_b32 s97, s97, 8
s_add_u32 s96, s96, s97
s_lshr_b32 s97, s62, 2
s_xor_b32 s97, s97, 1
s_lshl_b32 s97, s97, 16
s_add_u32 s81, s96, s97
buffer_load_dwordx4 v[164:167], v6, s[28:31], s81 offen
s_add_u32 s81, s81, 0x1000
buffer_load_dwordx4 v[172:175], v6, s[28:31], s81 offen
s_add_u32 s81, s81, 0x1000
buffer_load_dwordx4 v[180:183], v6, s[28:31], s81 offen
s_add_u32 s81, s81, 0x1000
buffer_load_dwordx4 v[188:191], v6, s[28:31], s81 offen
s_mov_b64 vcc, s[56:57]
s_waitcnt vmcnt(16)
v_mov_b32_dpp v12, v128 quad_perm:[1,0,3,2] row_mask:0xf bank_mask:0xf
s_waitcnt vmcnt(15)
v_cndmask_b32_dpp v128, v136, v128, vcc quad_perm:[1,0,3,2] row_mask:0xf bank_mask:0xf
v_cndmask_b32_e64 v136, v136, v12, s[56:57]
v_mov_b32_dpp v13, v129 quad_perm:[1,0,3,2] row_mask:0xf bank_mask:0xf
v_cndmask_b32_dpp v129, v137, v129, vcc quad_perm:[1,0,3,2] row_mask:0xf bank_mask:0xf
v_cndmask_b32_e64 v137, v137, v13, s[56:57]
v_mov_b32_dpp v12, v130 quad_perm:[1,0,3,2] row_mask:0xf bank_mask:0xf
v_cndmask_b32_dpp v130, v138, v130, vcc quad_perm:[1,0,3,2] row_mask:0xf bank_mask:0xf
v_cndmask_b32_e64 v138, v138, v12, s[56:57]
v_mov_b32_dpp v13, v131 quad_perm:[1,0,3,2] row_mask:0xf bank_mask:0xf
v_cndmask_b32_dpp v131, v139, v131, vcc quad_perm:[1,0,3,2] row_mask:0xf bank_mask:0xf
v_cndmask_b32_e64 v139, v139, v13, s[56:57]
s_waitcnt vmcnt(14)
v_mov_b32_dpp v12, v144 quad_perm:[1,0,3,2] row_mask:0xf bank_mask:0xf
s_waitcnt vmcnt(13)
v_cndmask_b32_dpp v144, v152, v144, vcc quad_perm:[1,0,3,2] row_mask:0xf bank_mask:0xf
v_cndmask_b32_e64 v152, v152, v12, s[56:57]
v_mov_b32_dpp v13, v145 quad_perm:[1,0,3,2] row_mask:0xf bank_mask:0xf
v_cndmask_b32_dpp v145, v153, v145, vcc quad_perm:[1,0,3,2] row_mask:0xf bank_mask:0xf
v_cndmask_b32_e64 v153, v153, v13, s[56:57]
v_mov_b32_dpp v12, v146 quad_perm:[1,0,3,2] row_mask:0xf bank_mask:0xf
v_cndmask_b32_dpp v146, v154, v146, vcc quad_perm:[1,0,3,2] row_mask:0xf bank_mask:0xf
v_cndmask_b32_e64 v154, v154, v12, s[56:57]
v_mov_b32_dpp v13, v147 quad_perm:[1,0,3,2] row_mask:0xf bank_mask:0xf
v_cndmask_b32_dpp v147, v155, v147, vcc quad_perm:[1,0,3,2] row_mask:0xf bank_mask:0xf
v_cndmask_b32_e64 v155, v155, v13, s[56:57]
s_mov_b64 vcc, s[58:59]
v_mov_b32_dpp v12, v128 quad_perm:[2,3,0,1] row_mask:0xf bank_mask:0xf
v_cndmask_b32_dpp v128, v144, v128, vcc quad_perm:[2,3,0,1] row_mask:0xf bank_mask:0xf
v_cndmask_b32_e64 v144, v144, v12, s[58:59]
v_mov_b32_dpp v13, v129 quad_perm:[2,3,0,1] row_mask:0xf bank_mask:0xf
v_cndmask_b32_dpp v129, v145, v129, vcc quad_perm:[2,3,0,1] row_mask:0xf bank_mask:0xf
v_cndmask_b32_e64 v145, v145, v13, s[58:59]
v_mov_b32_dpp v12, v130 quad_perm:[2,3,0,1] row_mask:0xf bank_mask:0xf
v_cndmask_b32_dpp v130, v146, v130, vcc quad_perm:[2,3,0,1] row_mask:0xf bank_mask:0xf
v_cndmask_b32_e64 v146, v146, v12, s[58:59]
v_mov_b32_dpp v13, v131 quad_perm:[2,3,0,1] row_mask:0xf bank_mask:0xf
v_cndmask_b32_dpp v131, v147, v131, vcc quad_perm:[2,3,0,1] row_mask:0xf bank_mask:0xf
v_cndmask_b32_e64 v147, v147, v13, s[58:59]
v_mov_b32_dpp v12, v136 quad_perm:[2,3,0,1] row_mask:0xf bank_mask:0xf
v_cndmask_b32_dpp v136, v152, v136, vcc quad_perm:[2,3,0,1] row_mask:0xf bank_mask:0xf
v_cndmask_b32_e64 v152, v152, v12, s[58:59]
v_mov_b32_dpp v13, v137 quad_perm:[2,3,0,1] row_mask:0xf bank_mask:0xf
v_cndmask_b32_dpp v137, v153, v137, vcc quad_perm:[2,3,0,1] row_mask:0xf bank_mask:0xf
v_cndmask_b32_e64 v153, v153, v13, s[58:59]
v_mov_b32_dpp v12, v138 quad_perm:[2,3,0,1] row_mask:0xf bank_mask:0xf
v_cndmask_b32_dpp v138, v154, v138, vcc quad_perm:[2,3,0,1] row_mask:0xf bank_mask:0xf
v_cndmask_b32_e64 v154, v154, v12, s[58:59]
v_mov_b32_dpp v13, v139 quad_perm:[2,3,0,1] row_mask:0xf bank_mask:0xf
v_cndmask_b32_dpp v139, v155, v139, vcc quad_perm:[2,3,0,1] row_mask:0xf bank_mask:0xf
v_cndmask_b32_e64 v155, v155, v13, s[58:59]
s_mov_b64 vcc, s[56:57]
s_waitcnt vmcnt(12)
v_mov_b32_dpp v12, v132 quad_perm:[1,0,3,2] row_mask:0xf bank_mask:0xf
s_waitcnt vmcnt(11)
v_cndmask_b32_dpp v132, v140, v132, vcc quad_perm:[1,0,3,2] row_mask:0xf bank_mask:0xf
v_cndmask_b32_e64 v140, v140, v12, s[56:57]
v_mov_b32_dpp v13, v133 quad_perm:[1,0,3,2] row_mask:0xf bank_mask:0xf
v_cndmask_b32_dpp v133, v141, v133, vcc quad_perm:[1,0,3,2] row_mask:0xf bank_mask:0xf
v_cndmask_b32_e64 v141, v141, v13, s[56:57]
v_mov_b32_dpp v12, v134 quad_perm:[1,0,3,2] row_mask:0xf bank_mask:0xf
v_cndmask_b32_dpp v134, v142, v134, vcc quad_perm:[1,0,3,2] row_mask:0xf bank_mask:0xf
v_cndmask_b32_e64 v142, v142, v12, s[56:57]
v_mov_b32_dpp v13, v135 quad_perm:[1,0,3,2] row_mask:0xf bank_mask:0xf
v_cndmask_b32_dpp v135, v143, v135, vcc quad_perm:[1,0,3,2] row_mask:0xf bank_mask:0xf
v_cndmask_b32_e64 v143, v143, v13, s[56:57]
s_waitcnt vmcnt(10)
v_mov_b32_dpp v12, v148 quad_perm:[1,0,3,2] row_mask:0xf bank_mask:0xf
s_waitcnt vmcnt(9)
v_cndmask_b32_dpp v148, v156, v148, vcc quad_perm:[1,0,3,2] row_mask:0xf bank_mask:0xf
v_cndmask_b32_e64 v156, v156, v12, s[56:57]
v_mov_b32_dpp v13, v149 quad_perm:[1,0,3,2] row_mask:0xf bank_mask:0xf
v_cndmask_b32_dpp v149, v157, v149, vcc quad_perm:[1,0,3,2] row_mask:0xf bank_mask:0xf
v_cndmask_b32_e64 v157, v157, v13, s[56:57]
v_mov_b32_dpp v12, v150 quad_perm:[1,0,3,2] row_mask:0xf bank_mask:0xf
v_cndmask_b32_dpp v150, v158, v150, vcc quad_perm:[1,0,3,2] row_mask:0xf bank_mask:0xf
v_cndmask_b32_e64 v158, v158, v12, s[56:57]
v_mov_b32_dpp v13, v151 quad_perm:[1,0,3,2] row_mask:0xf bank_mask:0xf
v_cndmask_b32_dpp v151, v159, v151, vcc quad_perm:[1,0,3,2] row_mask:0xf bank_mask:0xf
v_cndmask_b32_e64 v159, v159, v13, s[56:57]
s_mov_b64 vcc, s[58:59]
v_mov_b32_dpp v12, v132 quad_perm:[2,3,0,1] row_mask:0xf bank_mask:0xf
v_cndmask_b32_dpp v132, v148, v132, vcc quad_perm:[2,3,0,1] row_mask:0xf bank_mask:0xf
v_cndmask_b32_e64 v148, v148, v12, s[58:59]
v_mov_b32_dpp v13, v133 quad_perm:[2,3,0,1] row_mask:0xf bank_mask:0xf
v_cndmask_b32_dpp v133, v149, v133, vcc quad_perm:[2,3,0,1] row_mask:0xf bank_mask:0xf
v_cndmask_b32_e64 v149, v149, v13, s[58:59]
v_mov_b32_dpp v12, v134 quad_perm:[2,3,0,1] row_mask:0xf bank_mask:0xf
v_cndmask_b32_dpp v134, v150, v134, vcc quad_perm:[2,3,0,1] row_mask:0xf bank_mask:0xf
v_cndmask_b32_e64 v150, v150, v12, s[58:59]
v_mov_b32_dpp v13, v135 quad_perm:[2,3,0,1] row_mask:0xf bank_mask:0xf
v_cndmask_b32_dpp v135, v151, v135, vcc quad_perm:[2,3,0,1] row_mask:0xf bank_mask:0xf
v_cndmask_b32_e64 v151, v151, v13, s[58:59]
v_mov_b32_dpp v12, v140 quad_perm:[2,3,0,1] row_mask:0xf bank_mask:0xf
v_cndmask_b32_dpp v140, v156, v140, vcc quad_perm:[2,3,0,1] row_mask:0xf bank_mask:0xf
v_cndmask_b32_e64 v156, v156, v12, s[58:59]
v_mov_b32_dpp v13, v141 quad_perm:[2,3,0,1] row_mask:0xf bank_mask:0xf
v_cndmask_b32_dpp v141, v157, v141, vcc quad_perm:[2,3,0,1] row_mask:0xf bank_mask:0xf
v_cndmask_b32_e64 v157, v157, v13, s[58:59]
v_mov_b32_dpp v12, v142 quad_perm:[2,3,0,1] row_mask:0xf bank_mask:0xf
v_cndmask_b32_dpp v142, v158, v142, vcc quad_perm:[2,3,0,1] row_mask:0xf bank_mask:0xf
v_cndmask_b32_e64 v158, v158, v12, s[58:59]
v_mov_b32_dpp v13, v143 quad_perm:[2,3,0,1] row_mask:0xf bank_mask:0xf
v_cndmask_b32_dpp v143, v159, v143, vcc quad_perm:[2,3,0,1] row_mask:0xf bank_mask:0xf
v_cndmask_b32_e64 v159, v159, v13, s[58:59]
s_lshr_b32 s96, s68, 1
s_lshl_b32 s96, s96, 11
s_and_b32 s97, s62, 4
s_add_u32 s97, s97, 2
s_xor_b32 s97, s97, s80
s_lshl_b32 s97, s97, 8
s_add_u32 s96, s96, s97
s_lshr_b32 s97, s62, 2
s_lshl_b32 s97, s97, 16
s_add_u32 s81, s96, s97
buffer_load_dwordx4 v[192:195], v6, s[28:31], s81 offen
s_add_u32 s81, s81, 0x1000
buffer_load_dwordx4 v[200:203], v6, s[28:31], s81 offen
s_add_u32 s81, s81, 0x1000
buffer_load_dwordx4 v[208:211], v6, s[28:31], s81 offen
s_add_u32 s81, s81, 0x1000
buffer_load_dwordx4 v[216:219], v6, s[28:31], s81 offen
s_lshr_b32 s96, s68, 1
s_lshl_b32 s96, s96, 11
s_and_b32 s97, s62, 4
s_add_u32 s97, s97, 2
s_xor_b32 s97, s97, s80
s_lshl_b32 s97, s97, 8
s_add_u32 s96, s96, s97
s_lshr_b32 s97, s62, 2
s_xor_b32 s97, s97, 1
s_lshl_b32 s97, s97, 16
s_add_u32 s81, s96, s97
buffer_load_dwordx4 v[196:199], v6, s[28:31], s81 offen
s_add_u32 s81, s81, 0x1000
buffer_load_dwordx4 v[204:207], v6, s[28:31], s81 offen
s_add_u32 s81, s81, 0x1000
buffer_load_dwordx4 v[212:215], v6, s[28:31], s81 offen
s_add_u32 s81, s81, 0x1000
buffer_load_dwordx4 v[220:223], v6, s[28:31], s81 offen
s_mov_b64 vcc, s[56:57]
s_waitcnt vmcnt(15)
v_mov_b32_dpp v12, v160 quad_perm:[1,0,3,2] row_mask:0xf bank_mask:0xf
s_waitcnt vmcnt(14)
v_cndmask_b32_dpp v160, v168, v160, vcc quad_perm:[1,0,3,2] row_mask:0xf bank_mask:0xf
v_cndmask_b32_e64 v168, v168, v12, s[56:57]
v_mov_b32_dpp v13, v161 quad_perm:[1,0,3,2] row_mask:0xf bank_mask:0xf
v_cndmask_b32_dpp v161, v169, v161, vcc quad_perm:[1,0,3,2] row_mask:0xf bank_mask:0xf
v_cndmask_b32_e64 v169, v169, v13, s[56:57]
v_mov_b32_dpp v12, v162 quad_perm:[1,0,3,2] row_mask:0xf bank_mask:0xf
v_cndmask_b32_dpp v162, v170, v162, vcc quad_perm:[1,0,3,2] row_mask:0xf bank_mask:0xf
v_cndmask_b32_e64 v170, v170, v12, s[56:57]
v_mov_b32_dpp v13, v163 quad_perm:[1,0,3,2] row_mask:0xf bank_mask:0xf
v_cndmask_b32_dpp v163, v171, v163, vcc quad_perm:[1,0,3,2] row_mask:0xf bank_mask:0xf
v_cndmask_b32_e64 v171, v171, v13, s[56:57]
s_waitcnt vmcnt(13)
v_mov_b32_dpp v12, v176 quad_perm:[1,0,3,2] row_mask:0xf bank_mask:0xf
s_waitcnt vmcnt(12)
v_cndmask_b32_dpp v176, v184, v176, vcc quad_perm:[1,0,3,2] row_mask:0xf bank_mask:0xf
v_cndmask_b32_e64 v184, v184, v12, s[56:57]
v_mov_b32_dpp v13, v177 quad_perm:[1,0,3,2] row_mask:0xf bank_mask:0xf
v_cndmask_b32_dpp v177, v185, v177, vcc quad_perm:[1,0,3,2] row_mask:0xf bank_mask:0xf
v_cndmask_b32_e64 v185, v185, v13, s[56:57]
v_mov_b32_dpp v12, v178 quad_perm:[1,0,3,2] row_mask:0xf bank_mask:0xf
v_cndmask_b32_dpp v178, v186, v178, vcc quad_perm:[1,0,3,2] row_mask:0xf bank_mask:0xf
v_cndmask_b32_e64 v186, v186, v12, s[56:57]
v_mov_b32_dpp v13, v179 quad_perm:[1,0,3,2] row_mask:0xf bank_mask:0xf
v_cndmask_b32_dpp v179, v187, v179, vcc quad_perm:[1,0,3,2] row_mask:0xf bank_mask:0xf
v_cndmask_b32_e64 v187, v187, v13, s[56:57]
s_mov_b64 vcc, s[58:59]
v_mov_b32_dpp v12, v160 quad_perm:[2,3,0,1] row_mask:0xf bank_mask:0xf
v_cndmask_b32_dpp v160, v176, v160, vcc quad_perm:[2,3,0,1] row_mask:0xf bank_mask:0xf
v_cndmask_b32_e64 v176, v176, v12, s[58:59]
v_mov_b32_dpp v13, v161 quad_perm:[2,3,0,1] row_mask:0xf bank_mask:0xf
v_cndmask_b32_dpp v161, v177, v161, vcc quad_perm:[2,3,0,1] row_mask:0xf bank_mask:0xf
v_cndmask_b32_e64 v177, v177, v13, s[58:59]
v_mov_b32_dpp v12, v162 quad_perm:[2,3,0,1] row_mask:0xf bank_mask:0xf
v_cndmask_b32_dpp v162, v178, v162, vcc quad_perm:[2,3,0,1] row_mask:0xf bank_mask:0xf
v_cndmask_b32_e64 v178, v178, v12, s[58:59]
v_mov_b32_dpp v13, v163 quad_perm:[2,3,0,1] row_mask:0xf bank_mask:0xf
v_cndmask_b32_dpp v163, v179, v163, vcc quad_perm:[2,3,0,1] row_mask:0xf bank_mask:0xf
v_cndmask_b32_e64 v179, v179, v13, s[58:59]
v_mov_b32_dpp v12, v168 quad_perm:[2,3,0,1] row_mask:0xf bank_mask:0xf
v_cndmask_b32_dpp v168, v184, v168, vcc quad_perm:[2,3,0,1] row_mask:0xf bank_mask:0xf
v_cndmask_b32_e64 v184, v184, v12, s[58:59]
v_mov_b32_dpp v13, v169 quad_perm:[2,3,0,1] row_mask:0xf bank_mask:0xf
v_cndmask_b32_dpp v169, v185, v169, vcc quad_perm:[2,3,0,1] row_mask:0xf bank_mask:0xf
v_cndmask_b32_e64 v185, v185, v13, s[58:59]
v_mov_b32_dpp v12, v170 quad_perm:[2,3,0,1] row_mask:0xf bank_mask:0xf
v_cndmask_b32_dpp v170, v186, v170, vcc quad_perm:[2,3,0,1] row_mask:0xf bank_mask:0xf
v_cndmask_b32_e64 v186, v186, v12, s[58:59]
v_mov_b32_dpp v13, v171 quad_perm:[2,3,0,1] row_mask:0xf bank_mask:0xf
v_cndmask_b32_dpp v171, v187, v171, vcc quad_perm:[2,3,0,1] row_mask:0xf bank_mask:0xf
v_cndmask_b32_e64 v187, v187, v13, s[58:59]
s_mov_b64 vcc, s[56:57]
s_waitcnt vmcnt(11)
v_mov_b32_dpp v12, v164 quad_perm:[1,0,3,2] row_mask:0xf bank_mask:0xf
s_waitcnt vmcnt(10)
v_cndmask_b32_dpp v164, v172, v164, vcc quad_perm:[1,0,3,2] row_mask:0xf bank_mask:0xf
v_cndmask_b32_e64 v172, v172, v12, s[56:57]
v_mov_b32_dpp v13, v165 quad_perm:[1,0,3,2] row_mask:0xf bank_mask:0xf
v_cndmask_b32_dpp v165, v173, v165, vcc quad_perm:[1,0,3,2] row_mask:0xf bank_mask:0xf
v_cndmask_b32_e64 v173, v173, v13, s[56:57]
v_mov_b32_dpp v12, v166 quad_perm:[1,0,3,2] row_mask:0xf bank_mask:0xf
v_cndmask_b32_dpp v166, v174, v166, vcc quad_perm:[1,0,3,2] row_mask:0xf bank_mask:0xf
v_cndmask_b32_e64 v174, v174, v12, s[56:57]
v_mov_b32_dpp v13, v167 quad_perm:[1,0,3,2] row_mask:0xf bank_mask:0xf
v_cndmask_b32_dpp v167, v175, v167, vcc quad_perm:[1,0,3,2] row_mask:0xf bank_mask:0xf
v_cndmask_b32_e64 v175, v175, v13, s[56:57]
s_waitcnt vmcnt(9)
v_mov_b32_dpp v12, v180 quad_perm:[1,0,3,2] row_mask:0xf bank_mask:0xf
s_waitcnt vmcnt(8)
v_cndmask_b32_dpp v180, v188, v180, vcc quad_perm:[1,0,3,2] row_mask:0xf bank_mask:0xf
v_cndmask_b32_e64 v188, v188, v12, s[56:57]
v_mov_b32_dpp v13, v181 quad_perm:[1,0,3,2] row_mask:0xf bank_mask:0xf
v_cndmask_b32_dpp v181, v189, v181, vcc quad_perm:[1,0,3,2] row_mask:0xf bank_mask:0xf
v_cndmask_b32_e64 v189, v189, v13, s[56:57]
v_mov_b32_dpp v12, v182 quad_perm:[1,0,3,2] row_mask:0xf bank_mask:0xf
v_cndmask_b32_dpp v182, v190, v182, vcc quad_perm:[1,0,3,2] row_mask:0xf bank_mask:0xf
v_cndmask_b32_e64 v190, v190, v12, s[56:57]
v_mov_b32_dpp v13, v183 quad_perm:[1,0,3,2] row_mask:0xf bank_mask:0xf
v_cndmask_b32_dpp v183, v191, v183, vcc quad_perm:[1,0,3,2] row_mask:0xf bank_mask:0xf
v_cndmask_b32_e64 v191, v191, v13, s[56:57]
s_mov_b64 vcc, s[58:59]
v_mov_b32_dpp v12, v164 quad_perm:[2,3,0,1] row_mask:0xf bank_mask:0xf
v_cndmask_b32_dpp v164, v180, v164, vcc quad_perm:[2,3,0,1] row_mask:0xf bank_mask:0xf
v_cndmask_b32_e64 v180, v180, v12, s[58:59]
v_mov_b32_dpp v13, v165 quad_perm:[2,3,0,1] row_mask:0xf bank_mask:0xf
v_cndmask_b32_dpp v165, v181, v165, vcc quad_perm:[2,3,0,1] row_mask:0xf bank_mask:0xf
v_cndmask_b32_e64 v181, v181, v13, s[58:59]
v_mov_b32_dpp v12, v166 quad_perm:[2,3,0,1] row_mask:0xf bank_mask:0xf
v_cndmask_b32_dpp v166, v182, v166, vcc quad_perm:[2,3,0,1] row_mask:0xf bank_mask:0xf
v_cndmask_b32_e64 v182, v182, v12, s[58:59]
v_mov_b32_dpp v13, v167 quad_perm:[2,3,0,1] row_mask:0xf bank_mask:0xf
v_cndmask_b32_dpp v167, v183, v167, vcc quad_perm:[2,3,0,1] row_mask:0xf bank_mask:0xf
v_cndmask_b32_e64 v183, v183, v13, s[58:59]
v_mov_b32_dpp v12, v172 quad_perm:[2,3,0,1] row_mask:0xf bank_mask:0xf
v_cndmask_b32_dpp v172, v188, v172, vcc quad_perm:[2,3,0,1] row_mask:0xf bank_mask:0xf
v_cndmask_b32_e64 v188, v188, v12, s[58:59]
v_mov_b32_dpp v13, v173 quad_perm:[2,3,0,1] row_mask:0xf bank_mask:0xf
v_cndmask_b32_dpp v173, v189, v173, vcc quad_perm:[2,3,0,1] row_mask:0xf bank_mask:0xf
v_cndmask_b32_e64 v189, v189, v13, s[58:59]
v_mov_b32_dpp v12, v174 quad_perm:[2,3,0,1] row_mask:0xf bank_mask:0xf
v_cndmask_b32_dpp v174, v190, v174, vcc quad_perm:[2,3,0,1] row_mask:0xf bank_mask:0xf
v_cndmask_b32_e64 v190, v190, v12, s[58:59]
v_mov_b32_dpp v13, v175 quad_perm:[2,3,0,1] row_mask:0xf bank_mask:0xf
v_cndmask_b32_dpp v175, v191, v175, vcc quad_perm:[2,3,0,1] row_mask:0xf bank_mask:0xf
v_cndmask_b32_e64 v191, v191, v13, s[58:59]
s_lshr_b32 s96, s68, 1
s_lshl_b32 s96, s96, 11
s_and_b32 s97, s62, 4
s_add_u32 s97, s97, 3
s_xor_b32 s97, s97, s80
s_lshl_b32 s97, s97, 8
s_add_u32 s96, s96, s97
s_lshr_b32 s97, s62, 2
s_lshl_b32 s97, s97, 16
s_add_u32 s81, s96, s97
buffer_load_dwordx4 v[224:227], v6, s[28:31], s81 offen
s_add_u32 s81, s81, 0x1000
buffer_load_dwordx4 v[232:235], v6, s[28:31], s81 offen
s_add_u32 s81, s81, 0x1000
buffer_load_dwordx4 v[240:243], v6, s[28:31], s81 offen
s_add_u32 s81, s81, 0x1000
buffer_load_dwordx4 v[248:251], v6, s[28:31], s81 offen
s_lshr_b32 s96, s68, 1
s_lshl_b32 s96, s96, 11
s_and_b32 s97, s62, 4
s_add_u32 s97, s97, 3
s_xor_b32 s97, s97, s80
s_lshl_b32 s97, s97, 8
s_add_u32 s96, s96, s97
s_lshr_b32 s97, s62, 2
s_xor_b32 s97, s97, 1
s_lshl_b32 s97, s97, 16
s_add_u32 s81, s96, s97
buffer_load_dwordx4 v[228:231], v6, s[28:31], s81 offen
s_add_u32 s81, s81, 0x1000
buffer_load_dwordx4 v[236:239], v6, s[28:31], s81 offen
s_add_u32 s81, s81, 0x1000
buffer_load_dwordx4 v[244:247], v6, s[28:31], s81 offen
s_add_u32 s81, s81, 0x1000
buffer_load_dwordx4 v[252:255], v6, s[28:31], s81 offen
s_mov_b64 vcc, s[56:57]
s_waitcnt vmcnt(15)
v_mov_b32_dpp v12, v192 quad_perm:[1,0,3,2] row_mask:0xf bank_mask:0xf
s_waitcnt vmcnt(14)
v_cndmask_b32_dpp v192, v200, v192, vcc quad_perm:[1,0,3,2] row_mask:0xf bank_mask:0xf
v_cndmask_b32_e64 v200, v200, v12, s[56:57]
v_mov_b32_dpp v13, v193 quad_perm:[1,0,3,2] row_mask:0xf bank_mask:0xf
v_cndmask_b32_dpp v193, v201, v193, vcc quad_perm:[1,0,3,2] row_mask:0xf bank_mask:0xf
v_cndmask_b32_e64 v201, v201, v13, s[56:57]
v_mov_b32_dpp v12, v194 quad_perm:[1,0,3,2] row_mask:0xf bank_mask:0xf
v_cndmask_b32_dpp v194, v202, v194, vcc quad_perm:[1,0,3,2] row_mask:0xf bank_mask:0xf
v_cndmask_b32_e64 v202, v202, v12, s[56:57]
v_mov_b32_dpp v13, v195 quad_perm:[1,0,3,2] row_mask:0xf bank_mask:0xf
v_cndmask_b32_dpp v195, v203, v195, vcc quad_perm:[1,0,3,2] row_mask:0xf bank_mask:0xf
v_cndmask_b32_e64 v203, v203, v13, s[56:57]
s_waitcnt vmcnt(13)
v_mov_b32_dpp v12, v208 quad_perm:[1,0,3,2] row_mask:0xf bank_mask:0xf
s_waitcnt vmcnt(12)
v_cndmask_b32_dpp v208, v216, v208, vcc quad_perm:[1,0,3,2] row_mask:0xf bank_mask:0xf
v_cndmask_b32_e64 v216, v216, v12, s[56:57]
v_mov_b32_dpp v13, v209 quad_perm:[1,0,3,2] row_mask:0xf bank_mask:0xf
v_cndmask_b32_dpp v209, v217, v209, vcc quad_perm:[1,0,3,2] row_mask:0xf bank_mask:0xf
v_cndmask_b32_e64 v217, v217, v13, s[56:57]
v_mov_b32_dpp v12, v210 quad_perm:[1,0,3,2] row_mask:0xf bank_mask:0xf
v_cndmask_b32_dpp v210, v218, v210, vcc quad_perm:[1,0,3,2] row_mask:0xf bank_mask:0xf
v_cndmask_b32_e64 v218, v218, v12, s[56:57]
v_mov_b32_dpp v13, v211 quad_perm:[1,0,3,2] row_mask:0xf bank_mask:0xf
v_cndmask_b32_dpp v211, v219, v211, vcc quad_perm:[1,0,3,2] row_mask:0xf bank_mask:0xf
v_cndmask_b32_e64 v219, v219, v13, s[56:57]
s_mov_b64 vcc, s[58:59]
v_mov_b32_dpp v12, v192 quad_perm:[2,3,0,1] row_mask:0xf bank_mask:0xf
v_cndmask_b32_dpp v192, v208, v192, vcc quad_perm:[2,3,0,1] row_mask:0xf bank_mask:0xf
v_cndmask_b32_e64 v208, v208, v12, s[58:59]
v_mov_b32_dpp v13, v193 quad_perm:[2,3,0,1] row_mask:0xf bank_mask:0xf
v_cndmask_b32_dpp v193, v209, v193, vcc quad_perm:[2,3,0,1] row_mask:0xf bank_mask:0xf
v_cndmask_b32_e64 v209, v209, v13, s[58:59]
v_mov_b32_dpp v12, v194 quad_perm:[2,3,0,1] row_mask:0xf bank_mask:0xf
v_cndmask_b32_dpp v194, v210, v194, vcc quad_perm:[2,3,0,1] row_mask:0xf bank_mask:0xf
v_cndmask_b32_e64 v210, v210, v12, s[58:59]
v_mov_b32_dpp v13, v195 quad_perm:[2,3,0,1] row_mask:0xf bank_mask:0xf
v_cndmask_b32_dpp v195, v211, v195, vcc quad_perm:[2,3,0,1] row_mask:0xf bank_mask:0xf
v_cndmask_b32_e64 v211, v211, v13, s[58:59]
v_mov_b32_dpp v12, v200 quad_perm:[2,3,0,1] row_mask:0xf bank_mask:0xf
v_cndmask_b32_dpp v200, v216, v200, vcc quad_perm:[2,3,0,1] row_mask:0xf bank_mask:0xf
v_cndmask_b32_e64 v216, v216, v12, s[58:59]
v_mov_b32_dpp v13, v201 quad_perm:[2,3,0,1] row_mask:0xf bank_mask:0xf
v_cndmask_b32_dpp v201, v217, v201, vcc quad_perm:[2,3,0,1] row_mask:0xf bank_mask:0xf
v_cndmask_b32_e64 v217, v217, v13, s[58:59]
v_mov_b32_dpp v12, v202 quad_perm:[2,3,0,1] row_mask:0xf bank_mask:0xf
v_cndmask_b32_dpp v202, v218, v202, vcc quad_perm:[2,3,0,1] row_mask:0xf bank_mask:0xf
v_cndmask_b32_e64 v218, v218, v12, s[58:59]
v_mov_b32_dpp v13, v203 quad_perm:[2,3,0,1] row_mask:0xf bank_mask:0xf
v_cndmask_b32_dpp v203, v219, v203, vcc quad_perm:[2,3,0,1] row_mask:0xf bank_mask:0xf
v_cndmask_b32_e64 v219, v219, v13, s[58:59]
s_mov_b64 vcc, s[56:57]
s_waitcnt vmcnt(11)
v_mov_b32_dpp v12, v196 quad_perm:[1,0,3,2] row_mask:0xf bank_mask:0xf
s_waitcnt vmcnt(10)
v_cndmask_b32_dpp v196, v204, v196, vcc quad_perm:[1,0,3,2] row_mask:0xf bank_mask:0xf
v_cndmask_b32_e64 v204, v204, v12, s[56:57]
v_mov_b32_dpp v13, v197 quad_perm:[1,0,3,2] row_mask:0xf bank_mask:0xf
v_cndmask_b32_dpp v197, v205, v197, vcc quad_perm:[1,0,3,2] row_mask:0xf bank_mask:0xf
v_cndmask_b32_e64 v205, v205, v13, s[56:57]
v_mov_b32_dpp v12, v198 quad_perm:[1,0,3,2] row_mask:0xf bank_mask:0xf
v_cndmask_b32_dpp v198, v206, v198, vcc quad_perm:[1,0,3,2] row_mask:0xf bank_mask:0xf
v_cndmask_b32_e64 v206, v206, v12, s[56:57]
v_mov_b32_dpp v13, v199 quad_perm:[1,0,3,2] row_mask:0xf bank_mask:0xf
v_cndmask_b32_dpp v199, v207, v199, vcc quad_perm:[1,0,3,2] row_mask:0xf bank_mask:0xf
v_cndmask_b32_e64 v207, v207, v13, s[56:57]
s_waitcnt vmcnt(9)
v_mov_b32_dpp v12, v212 quad_perm:[1,0,3,2] row_mask:0xf bank_mask:0xf
s_waitcnt vmcnt(8)
v_cndmask_b32_dpp v212, v220, v212, vcc quad_perm:[1,0,3,2] row_mask:0xf bank_mask:0xf
v_cndmask_b32_e64 v220, v220, v12, s[56:57]
v_mov_b32_dpp v13, v213 quad_perm:[1,0,3,2] row_mask:0xf bank_mask:0xf
v_cndmask_b32_dpp v213, v221, v213, vcc quad_perm:[1,0,3,2] row_mask:0xf bank_mask:0xf
v_cndmask_b32_e64 v221, v221, v13, s[56:57]
v_mov_b32_dpp v12, v214 quad_perm:[1,0,3,2] row_mask:0xf bank_mask:0xf
v_cndmask_b32_dpp v214, v222, v214, vcc quad_perm:[1,0,3,2] row_mask:0xf bank_mask:0xf
v_cndmask_b32_e64 v222, v222, v12, s[56:57]
v_mov_b32_dpp v13, v215 quad_perm:[1,0,3,2] row_mask:0xf bank_mask:0xf
v_cndmask_b32_dpp v215, v223, v215, vcc quad_perm:[1,0,3,2] row_mask:0xf bank_mask:0xf
v_cndmask_b32_e64 v223, v223, v13, s[56:57]
s_mov_b64 vcc, s[58:59]
v_mov_b32_dpp v12, v196 quad_perm:[2,3,0,1] row_mask:0xf bank_mask:0xf
v_cndmask_b32_dpp v196, v212, v196, vcc quad_perm:[2,3,0,1] row_mask:0xf bank_mask:0xf
v_cndmask_b32_e64 v212, v212, v12, s[58:59]
v_mov_b32_dpp v13, v197 quad_perm:[2,3,0,1] row_mask:0xf bank_mask:0xf
v_cndmask_b32_dpp v197, v213, v197, vcc quad_perm:[2,3,0,1] row_mask:0xf bank_mask:0xf
v_cndmask_b32_e64 v213, v213, v13, s[58:59]
v_mov_b32_dpp v12, v198 quad_perm:[2,3,0,1] row_mask:0xf bank_mask:0xf
v_cndmask_b32_dpp v198, v214, v198, vcc quad_perm:[2,3,0,1] row_mask:0xf bank_mask:0xf
v_cndmask_b32_e64 v214, v214, v12, s[58:59]
v_mov_b32_dpp v13, v199 quad_perm:[2,3,0,1] row_mask:0xf bank_mask:0xf
v_cndmask_b32_dpp v199, v215, v199, vcc quad_perm:[2,3,0,1] row_mask:0xf bank_mask:0xf
v_cndmask_b32_e64 v215, v215, v13, s[58:59]
v_mov_b32_dpp v12, v204 quad_perm:[2,3,0,1] row_mask:0xf bank_mask:0xf
v_cndmask_b32_dpp v204, v220, v204, vcc quad_perm:[2,3,0,1] row_mask:0xf bank_mask:0xf
v_cndmask_b32_e64 v220, v220, v12, s[58:59]
v_mov_b32_dpp v13, v205 quad_perm:[2,3,0,1] row_mask:0xf bank_mask:0xf
v_cndmask_b32_dpp v205, v221, v205, vcc quad_perm:[2,3,0,1] row_mask:0xf bank_mask:0xf
v_cndmask_b32_e64 v221, v221, v13, s[58:59]
v_mov_b32_dpp v12, v206 quad_perm:[2,3,0,1] row_mask:0xf bank_mask:0xf
v_cndmask_b32_dpp v206, v222, v206, vcc quad_perm:[2,3,0,1] row_mask:0xf bank_mask:0xf
v_cndmask_b32_e64 v222, v222, v12, s[58:59]
v_mov_b32_dpp v13, v207 quad_perm:[2,3,0,1] row_mask:0xf bank_mask:0xf
v_cndmask_b32_dpp v207, v223, v207, vcc quad_perm:[2,3,0,1] row_mask:0xf bank_mask:0xf
v_cndmask_b32_e64 v223, v223, v13, s[58:59]
s_mov_b64 vcc, s[56:57]
s_waitcnt vmcnt(7)
v_mov_b32_dpp v12, v224 quad_perm:[1,0,3,2] row_mask:0xf bank_mask:0xf
s_waitcnt vmcnt(6)
v_cndmask_b32_dpp v224, v232, v224, vcc quad_perm:[1,0,3,2] row_mask:0xf bank_mask:0xf
v_cndmask_b32_e64 v232, v232, v12, s[56:57]
v_mov_b32_dpp v13, v225 quad_perm:[1,0,3,2] row_mask:0xf bank_mask:0xf
v_cndmask_b32_dpp v225, v233, v225, vcc quad_perm:[1,0,3,2] row_mask:0xf bank_mask:0xf
v_cndmask_b32_e64 v233, v233, v13, s[56:57]
v_mov_b32_dpp v12, v226 quad_perm:[1,0,3,2] row_mask:0xf bank_mask:0xf
v_cndmask_b32_dpp v226, v234, v226, vcc quad_perm:[1,0,3,2] row_mask:0xf bank_mask:0xf
v_cndmask_b32_e64 v234, v234, v12, s[56:57]
v_mov_b32_dpp v13, v227 quad_perm:[1,0,3,2] row_mask:0xf bank_mask:0xf
v_cndmask_b32_dpp v227, v235, v227, vcc quad_perm:[1,0,3,2] row_mask:0xf bank_mask:0xf
v_cndmask_b32_e64 v235, v235, v13, s[56:57]
s_waitcnt vmcnt(5)
v_mov_b32_dpp v12, v240 quad_perm:[1,0,3,2] row_mask:0xf bank_mask:0xf
s_waitcnt vmcnt(4)
v_cndmask_b32_dpp v240, v248, v240, vcc quad_perm:[1,0,3,2] row_mask:0xf bank_mask:0xf
v_cndmask_b32_e64 v248, v248, v12, s[56:57]
v_mov_b32_dpp v13, v241 quad_perm:[1,0,3,2] row_mask:0xf bank_mask:0xf
v_cndmask_b32_dpp v241, v249, v241, vcc quad_perm:[1,0,3,2] row_mask:0xf bank_mask:0xf
v_cndmask_b32_e64 v249, v249, v13, s[56:57]
v_mov_b32_dpp v12, v242 quad_perm:[1,0,3,2] row_mask:0xf bank_mask:0xf
v_cndmask_b32_dpp v242, v250, v242, vcc quad_perm:[1,0,3,2] row_mask:0xf bank_mask:0xf
v_cndmask_b32_e64 v250, v250, v12, s[56:57]
v_mov_b32_dpp v13, v243 quad_perm:[1,0,3,2] row_mask:0xf bank_mask:0xf
v_cndmask_b32_dpp v243, v251, v243, vcc quad_perm:[1,0,3,2] row_mask:0xf bank_mask:0xf
v_cndmask_b32_e64 v251, v251, v13, s[56:57]
s_mov_b64 vcc, s[58:59]
v_mov_b32_dpp v12, v224 quad_perm:[2,3,0,1] row_mask:0xf bank_mask:0xf
v_cndmask_b32_dpp v224, v240, v224, vcc quad_perm:[2,3,0,1] row_mask:0xf bank_mask:0xf
v_cndmask_b32_e64 v240, v240, v12, s[58:59]
v_mov_b32_dpp v13, v225 quad_perm:[2,3,0,1] row_mask:0xf bank_mask:0xf
v_cndmask_b32_dpp v225, v241, v225, vcc quad_perm:[2,3,0,1] row_mask:0xf bank_mask:0xf
v_cndmask_b32_e64 v241, v241, v13, s[58:59]
v_mov_b32_dpp v12, v226 quad_perm:[2,3,0,1] row_mask:0xf bank_mask:0xf
v_cndmask_b32_dpp v226, v242, v226, vcc quad_perm:[2,3,0,1] row_mask:0xf bank_mask:0xf
v_cndmask_b32_e64 v242, v242, v12, s[58:59]
v_mov_b32_dpp v13, v227 quad_perm:[2,3,0,1] row_mask:0xf bank_mask:0xf
v_cndmask_b32_dpp v227, v243, v227, vcc quad_perm:[2,3,0,1] row_mask:0xf bank_mask:0xf
v_cndmask_b32_e64 v243, v243, v13, s[58:59]
v_mov_b32_dpp v12, v232 quad_perm:[2,3,0,1] row_mask:0xf bank_mask:0xf
v_cndmask_b32_dpp v232, v248, v232, vcc quad_perm:[2,3,0,1] row_mask:0xf bank_mask:0xf
v_cndmask_b32_e64 v248, v248, v12, s[58:59]
v_mov_b32_dpp v13, v233 quad_perm:[2,3,0,1] row_mask:0xf bank_mask:0xf
v_cndmask_b32_dpp v233, v249, v233, vcc quad_perm:[2,3,0,1] row_mask:0xf bank_mask:0xf
v_cndmask_b32_e64 v249, v249, v13, s[58:59]
v_mov_b32_dpp v12, v234 quad_perm:[2,3,0,1] row_mask:0xf bank_mask:0xf
v_cndmask_b32_dpp v234, v250, v234, vcc quad_perm:[2,3,0,1] row_mask:0xf bank_mask:0xf
v_cndmask_b32_e64 v250, v250, v12, s[58:59]
v_mov_b32_dpp v13, v235 quad_perm:[2,3,0,1] row_mask:0xf bank_mask:0xf
v_cndmask_b32_dpp v235, v251, v235, vcc quad_perm:[2,3,0,1] row_mask:0xf bank_mask:0xf
v_cndmask_b32_e64 v251, v251, v13, s[58:59]
s_mov_b64 vcc, s[56:57]
s_waitcnt vmcnt(3)
v_mov_b32_dpp v12, v228 quad_perm:[1,0,3,2] row_mask:0xf bank_mask:0xf
s_waitcnt vmcnt(2)
v_cndmask_b32_dpp v228, v236, v228, vcc quad_perm:[1,0,3,2] row_mask:0xf bank_mask:0xf
v_cndmask_b32_e64 v236, v236, v12, s[56:57]
v_mov_b32_dpp v13, v229 quad_perm:[1,0,3,2] row_mask:0xf bank_mask:0xf
v_cndmask_b32_dpp v229, v237, v229, vcc quad_perm:[1,0,3,2] row_mask:0xf bank_mask:0xf
v_cndmask_b32_e64 v237, v237, v13, s[56:57]
v_mov_b32_dpp v12, v230 quad_perm:[1,0,3,2] row_mask:0xf bank_mask:0xf
v_cndmask_b32_dpp v230, v238, v230, vcc quad_perm:[1,0,3,2] row_mask:0xf bank_mask:0xf
v_cndmask_b32_e64 v238, v238, v12, s[56:57]
v_mov_b32_dpp v13, v231 quad_perm:[1,0,3,2] row_mask:0xf bank_mask:0xf
v_cndmask_b32_dpp v231, v239, v231, vcc quad_perm:[1,0,3,2] row_mask:0xf bank_mask:0xf
v_cndmask_b32_e64 v239, v239, v13, s[56:57]
s_waitcnt vmcnt(1)
v_mov_b32_dpp v12, v244 quad_perm:[1,0,3,2] row_mask:0xf bank_mask:0xf
s_waitcnt vmcnt(0)
v_cndmask_b32_dpp v244, v252, v244, vcc quad_perm:[1,0,3,2] row_mask:0xf bank_mask:0xf
v_cndmask_b32_e64 v252, v252, v12, s[56:57]
v_mov_b32_dpp v13, v245 quad_perm:[1,0,3,2] row_mask:0xf bank_mask:0xf
v_cndmask_b32_dpp v245, v253, v245, vcc quad_perm:[1,0,3,2] row_mask:0xf bank_mask:0xf
v_cndmask_b32_e64 v253, v253, v13, s[56:57]
v_mov_b32_dpp v12, v246 quad_perm:[1,0,3,2] row_mask:0xf bank_mask:0xf
v_cndmask_b32_dpp v246, v254, v246, vcc quad_perm:[1,0,3,2] row_mask:0xf bank_mask:0xf
v_cndmask_b32_e64 v254, v254, v12, s[56:57]
v_mov_b32_dpp v13, v247 quad_perm:[1,0,3,2] row_mask:0xf bank_mask:0xf
v_cndmask_b32_dpp v247, v255, v247, vcc quad_perm:[1,0,3,2] row_mask:0xf bank_mask:0xf
v_cndmask_b32_e64 v255, v255, v13, s[56:57]
s_mov_b64 vcc, s[58:59]
v_mov_b32_dpp v12, v228 quad_perm:[2,3,0,1] row_mask:0xf bank_mask:0xf
v_cndmask_b32_dpp v228, v244, v228, vcc quad_perm:[2,3,0,1] row_mask:0xf bank_mask:0xf
v_cndmask_b32_e64 v244, v244, v12, s[58:59]
v_mov_b32_dpp v13, v229 quad_perm:[2,3,0,1] row_mask:0xf bank_mask:0xf
v_cndmask_b32_dpp v229, v245, v229, vcc quad_perm:[2,3,0,1] row_mask:0xf bank_mask:0xf
v_cndmask_b32_e64 v245, v245, v13, s[58:59]
v_mov_b32_dpp v12, v230 quad_perm:[2,3,0,1] row_mask:0xf bank_mask:0xf
v_cndmask_b32_dpp v230, v246, v230, vcc quad_perm:[2,3,0,1] row_mask:0xf bank_mask:0xf
v_cndmask_b32_e64 v246, v246, v12, s[58:59]
v_mov_b32_dpp v13, v231 quad_perm:[2,3,0,1] row_mask:0xf bank_mask:0xf
v_cndmask_b32_dpp v231, v247, v231, vcc quad_perm:[2,3,0,1] row_mask:0xf bank_mask:0xf
v_cndmask_b32_e64 v247, v247, v13, s[58:59]
v_mov_b32_dpp v12, v236 quad_perm:[2,3,0,1] row_mask:0xf bank_mask:0xf
v_cndmask_b32_dpp v236, v252, v236, vcc quad_perm:[2,3,0,1] row_mask:0xf bank_mask:0xf
v_cndmask_b32_e64 v252, v252, v12, s[58:59]
v_mov_b32_dpp v13, v237 quad_perm:[2,3,0,1] row_mask:0xf bank_mask:0xf
v_cndmask_b32_dpp v237, v253, v237, vcc quad_perm:[2,3,0,1] row_mask:0xf bank_mask:0xf
v_cndmask_b32_e64 v253, v253, v13, s[58:59]
v_mov_b32_dpp v12, v238 quad_perm:[2,3,0,1] row_mask:0xf bank_mask:0xf
v_cndmask_b32_dpp v238, v254, v238, vcc quad_perm:[2,3,0,1] row_mask:0xf bank_mask:0xf
v_cndmask_b32_e64 v254, v254, v12, s[58:59]
v_mov_b32_dpp v13, v239 quad_perm:[2,3,0,1] row_mask:0xf bank_mask:0xf
v_cndmask_b32_dpp v239, v255, v239, vcc quad_perm:[2,3,0,1] row_mask:0xf bank_mask:0xf
v_cndmask_b32_e64 v255, v255, v13, s[58:59]
s_setprio 0
s_mov_b32 s70, s80
s_mov_b32 s80, 0xffff0000
s_add_u32 s74, s74, 0x8000
s_barrier
ds_read_b128 v[72:75], v21
ds_read_b128 v[76:79], v21 offset:1024
ds_read_b128 v[80:83], v21 offset:2048
ds_read_b128 v[84:87], v21 offset:3072
ds_read_b128 v[88:91], v21 offset:4096
s_waitcnt lgkmcnt(4)
v_mfma_f32_16x16x32_bf16 v[56:59], v[72:75], v[128:131], 0
v_mfma_f32_16x16x32_bf16 v[60:63], v[72:75], v[132:135], 0
s_lshl_b32 s96, s83, 5
s_lshl_b32 s97, s62, 1
s_add_u32 s96, s96, s97
s_lshl_b32 s96, s96, 12
s_add_u32 s29, 0x500000, s96
s_add_u32 m0, s75, 0x10000
s_add_u32 s74, s74, 0x8000
buffer_load_dwordx4 v1, s[36:39], s74 offen sc1 lds
ds_read_b128 v[92:95], v21 offset:5120
s_waitcnt lgkmcnt(4)
v_mfma_f32_16x16x32_bf16 v[56:59], v[76:79], v[136:139], v[56:59]
s_lshl_b32 s96, s82, 18
s_add_u32 s96, 0x1a00000, s96
s_lshl_b32 s97, s62, 10
s_add_u32 s30, s96, s97
s_lshl_b32 s96, s62, 12
v_add_u32 v5, s96, v1
v_mfma_f32_16x16x32_bf16 v[60:63], v[76:79], v[140:143], v[60:63]
v_add_u32 v6, 0x20000, v5
s_add_u32 m0, s75, 0x10000
s_nop 0
buffer_load_dwordx4 v1, s[36:39], s74 offen offset:1024 sc1 lds
v_and_b32 v12, 1, v0
v_lshlrev_b32 v52, 4, v12
s_movk_i32 s78, 0x3f80
v_lshlrev_b32_e64 v52, v52, s78
ds_read_b128 v[72:75], v21 offset:6144
s_waitcnt lgkmcnt(4)
v_mfma_f32_16x16x32_bf16 v[56:59], v[80:83], v[144:147], v[56:59]
s_mov_b32 vcc_lo, 0x30003
s_mov_b32 vcc_hi, 0x30003
v_cndmask_b32 v48, 0, v52, vcc
v_mfma_f32_16x16x32_bf16 v[60:63], v[80:83], v[148:151], v[60:63]
s_mov_b32 vcc_lo, 0x300030
s_mov_b32 vcc_hi, 0x300030
v_cndmask_b32 v49, 0, v52, vcc
s_add_u32 m0, s75, 0x10000
s_nop 0
buffer_load_dwordx4 v1, s[36:39], s74 offen offset:2048 sc1 lds
s_mov_b32 vcc_lo, 0x3000300
s_mov_b32 vcc_hi, 0x3000300
v_cndmask_b32 v50, 0, v52, vcc
ds_read_b128 v[76:79], v21 offset:7168
s_waitcnt lgkmcnt(4)
v_mfma_f32_16x16x32_bf16 v[56:59], v[84:87], v[152:155], v[56:59]
s_mov_b32 vcc_lo, 0x30003000
s_mov_b32 vcc_hi, 0x30003000
v_cndmask_b32 v51, 0, v52, vcc
v_mfma_f32_16x16x32_bf16 v[60:63], v[84:87], v[156:159], v[60:63]
s_lshl_b32 s96, s62, 10
v_add_u32 v12, s96, v2
v_add_u32 v100, 0x22000, v12
s_add_u32 m0, s75, 0x10000
s_nop 0
buffer_load_dwordx4 v1, s[36:39], s74 offen offset:3072 sc1 lds
ds_read_b64_tr_b16 v[112:113], v100
ds_read_b128 v[80:83], v21 offset:8192
s_waitcnt lgkmcnt(5)
v_mfma_f32_16x16x32_bf16 v[64:67], v[88:91], v[128:131], 0
ds_read_b64_tr_b16 v[114:115], v100 offset:512
ds_read_b64_tr_b16 v[116:117], v100 offset:8192
v_mfma_f32_16x16x32_bf16 v[68:71], v[88:91], v[132:135], 0
ds_read_b64_tr_b16 v[118:119], v100 offset:8704
v_mov_b32 v124, 0x3f803f80
ds_read_b128 v[84:87], v21 offset:9216
s_waitcnt lgkmcnt(8)
v_mfma_f32_16x16x32_bf16 v[64:67], v[92:95], v[136:139], v[64:67]
v_mov_b32 v125, 0x3f803f80
v_mov_b32 v126, 0x3f803f80
v_mfma_f32_16x16x32_bf16 v[68:71], v[92:95], v[140:143], v[68:71]
v_mov_b32 v127, 0x3f803f80
ds_read_b128 v[88:91], v21 offset:10240
s_waitcnt lgkmcnt(8)
v_mfma_f32_16x16x32_bf16 v[64:67], v[72:75], v[144:147], v[64:67]
v_mfma_f32_16x16x32_bf16 v[68:71], v[72:75], v[148:151], v[68:71]
s_waitcnt lgkmcnt(4)
v_mfma_f32_16x16x32_bf16 v[100:103], v[112:115], v[124:127], 0
ds_read_b128 v[92:95], v21 offset:11264
v_mfma_f32_16x16x32_bf16 v[64:67], v[76:79], v[152:155], v[64:67]
s_waitcnt lgkmcnt(3)
v_mfma_f32_16x16x32_bf16 v[104:107], v[116:119], v[124:127], 0
v_lshrrev_b32 v12, 4, v0
v_lshlrev_b32 v12, 5, v12
v_and_b32 v13, 1, v0
v_lshlrev_b32 v13, 4, v13
v_add_u32 v53, v12, v13
v_mfma_f32_16x16x32_bf16 v[68:71], v[76:79], v[156:159], v[68:71]
ds_read_b128 v[72:75], v21 offset:12288
v_mfma_f32_16x16x32_bf16 v[56:59], v[80:83], v[160:163], v[56:59]
v_cndmask_b32_e64 v100, v104, v100, s[56:57]
v_mfma_f32_16x16x32_bf16 v[60:63], v[80:83], v[164:167], v[60:63]
v_cndmask_b32_e64 v101, v105, v101, s[56:57]
v_cndmask_b32_e64 v102, v106, v102, s[56:57]
ds_read_b128 v[76:79], v21 offset:13312
s_waitcnt lgkmcnt(4)
v_mfma_f32_16x16x32_bf16 v[56:59], v[84:87], v[168:171], v[56:59]
v_cndmask_b32_e64 v103, v107, v103, s[56:57]
s_lshl_b32 s96, s82, 3
s_add_u32 s96, s96, s62
s_lshl_b32 s96, s96, 4
s_add_u32 s96, s96, s83
s_lshl_b32 s96, s96, 7
s_add_u32 s78, 0x80000, s96
s_mov_b32 exec_lo, 0x30003
s_mov_b32 exec_hi, 0x30003
buffer_store_dwordx4 v[100:103], v53, s[36:39], s78 offen sc1
s_mov_b64 exec, -1
v_mfma_f32_16x16x32_bf16 v[60:63], v[84:87], v[172:175], v[60:63]
ds_read_b128 v[80:83], v21 offset:14336
s_waitcnt lgkmcnt(4)
v_mfma_f32_16x16x32_bf16 v[56:59], v[88:91], v[176:179], v[56:59]
v_mfma_f32_16x16x32_bf16 v[60:63], v[88:91], v[180:183], v[60:63]
ds_read_b128 v[84:87], v21 offset:15360
s_waitcnt lgkmcnt(4)
v_mfma_f32_16x16x32_bf16 v[56:59], v[92:95], v[184:187], v[56:59]
s_add_u32 m0, s75, 0x18000
s_add_u32 s74, s74, 0x8000
buffer_load_dwordx4 v1, s[36:39], s74 offen sc1 lds
s_lshl_b32 s96, s83, 14
s_add_u32 s96, s30, s96
s_add_u32 s78, s96, 0
buffer_store_dwordx4 v[112:115], v20, s[36:39], s78 offen sc1
s_barrier
v_mfma_f32_16x16x32_bf16 v[60:63], v[92:95], v[188:191], v[60:63]
ds_read_b128 v[88:91], v21 offset:32768
s_waitcnt lgkmcnt(4)
v_mfma_f32_16x16x32_bf16 v[64:67], v[72:75], v[160:163], v[64:67]
v_mfma_f32_16x16x32_bf16 v[68:71], v[72:75], v[164:167], v[68:71]
s_add_u32 m0, s75, 0x18000
s_nop 0
buffer_load_dwordx4 v1, s[36:39], s74 offen offset:1024 sc1 lds
ds_read_b128 v[92:95], v21 offset:33792
s_waitcnt lgkmcnt(4)
v_mfma_f32_16x16x32_bf16 v[64:67], v[76:79], v[168:171], v[64:67]
v_mfma_f32_16x16x32_bf16 v[68:71], v[76:79], v[172:175], v[68:71]
ds_read_b128 v[72:75], v21 offset:34816
s_waitcnt lgkmcnt(4)
v_mfma_f32_16x16x32_bf16 v[64:67], v[80:83], v[176:179], v[64:67]
v_mfma_f32_16x16x32_bf16 v[68:71], v[80:83], v[180:183], v[68:71]
s_add_u32 m0, s75, 0x18000
s_nop 0
buffer_load_dwordx4 v1, s[36:39], s74 offen offset:2048 sc1 lds
v_mov_b32 v108, -1
ds_read_b128 v[76:79], v21 offset:35840
s_waitcnt lgkmcnt(4)
v_mfma_f32_16x16x32_bf16 v[64:67], v[84:87], v[184:187], v[64:67]
v_mov_b32 v109, -1
v_mov_b32 v110, -1
v_mfma_f32_16x16x32_bf16 v[68:71], v[84:87], v[188:191], v[68:71]
v_mov_b32 v111, -1
ds_read_b128 v[80:83], v21 offset:36864
s_waitcnt lgkmcnt(4)
v_mfma_f32_16x16x32_bf16 v[56:59], v[88:91], v[192:195], v[56:59]
s_add_u32 m0, s75, 0x18000
s_nop 0
buffer_load_dwordx4 v1, s[36:39], s74 offen offset:3072 sc1 lds
s_lshl_b32 s96, s83, 14
s_add_u32 s96, s30, s96
s_add_u32 s78, s96, 0x2000
buffer_store_dwordx4 v[116:119], v20, s[36:39], s78 offen sc1
v_mfma_f32_16x16x32_bf16 v[60:63], v[88:91], v[196:199], v[60:63]
ds_read_b128 v[84:87], v21 offset:37888
s_waitcnt lgkmcnt(4)
v_mfma_f32_16x16x32_bf16 v[56:59], v[92:95], v[200:203], v[56:59]
v_mfma_f32_16x16x32_bf16 v[60:63], v[92:95], v[204:207], v[60:63]
ds_read_b128 v[88:91], v21 offset:38912
s_waitcnt lgkmcnt(4)
v_mfma_f32_16x16x32_bf16 v[56:59], v[72:75], v[208:211], v[56:59]
v_mfma_f32_16x16x32_bf16 v[60:63], v[72:75], v[212:215], v[60:63]
ds_read_b128 v[92:95], v21 offset:39936
s_waitcnt lgkmcnt(4)
v_mfma_f32_16x16x32_bf16 v[56:59], v[76:79], v[216:219], v[56:59]
s_waitcnt vmcnt(7)
s_barrier
v_mfma_f32_16x16x32_bf16 v[60:63], v[76:79], v[220:223], v[60:63]
s_add_u32 m0, s75, 0x0
s_add_u32 s74, s74, 0x8000
buffer_load_dwordx4 v1, s[36:39], s74 offen sc1 lds
ds_read_b128 v[72:75], v21 offset:40960
s_waitcnt lgkmcnt(4)
v_mfma_f32_16x16x32_bf16 v[64:67], v[80:83], v[192:195], v[64:67]
v_mfma_f32_16x16x32_bf16 v[68:71], v[80:83], v[196:199], v[68:71]
v_lshrrev_b32 v12, 4, v0
v_lshlrev_b32 v12, 15, v12
v_and_b32 v13, 15, v0
v_lshlrev_b32 v13, 4, v13
v_add_u32 v12, v12, v13
buffer_store_dwordx4 v[108:111], v12, s[36:39], s77 offen sc1
ds_read_b128 v[76:79], v21 offset:41984
s_waitcnt lgkmcnt(4)
v_mfma_f32_16x16x32_bf16 v[64:67], v[84:87], v[200:203], v[64:67]
v_mfma_f32_16x16x32_bf16 v[68:71], v[84:87], v[204:207], v[68:71]
ds_read_b128 v[80:83], v21 offset:43008
s_waitcnt lgkmcnt(4)
v_mfma_f32_16x16x32_bf16 v[64:67], v[88:91], v[208:211], v[64:67]
v_mfma_f32_16x16x32_bf16 v[68:71], v[88:91], v[212:215], v[68:71]
s_add_u32 m0, s75, 0x0
s_nop 0
buffer_load_dwordx4 v1, s[36:39], s74 offen offset:1024 sc1 lds
ds_read_b128 v[84:87], v21 offset:44032
s_waitcnt lgkmcnt(4)
v_mfma_f32_16x16x32_bf16 v[64:67], v[92:95], v[216:219], v[64:67]
v_mfma_f32_16x16x32_bf16 v[68:71], v[92:95], v[220:223], v[68:71]
ds_read_b128 v[88:91], v21 offset:45056
s_waitcnt lgkmcnt(4)
v_mfma_f32_16x16x32_bf16 v[56:59], v[72:75], v[224:227], v[56:59]
v_mfma_f32_16x16x32_bf16 v[60:63], v[72:75], v[228:231], v[60:63]
ds_read_b128 v[92:95], v21 offset:46080
s_waitcnt lgkmcnt(4)
v_mfma_f32_16x16x32_bf16 v[56:59], v[76:79], v[232:235], v[56:59]
v_mfma_f32_16x16x32_bf16 v[60:63], v[76:79], v[236:239], v[60:63]
s_add_u32 m0, s75, 0x0
s_nop 0
buffer_load_dwordx4 v1, s[36:39], s74 offen offset:2048 sc1 lds
ds_read_b128 v[72:75], v21 offset:47104
s_waitcnt lgkmcnt(4)
v_mfma_f32_16x16x32_bf16 v[56:59], v[80:83], v[240:243], v[56:59]
v_mfma_f32_16x16x32_bf16 v[60:63], v[80:83], v[244:247], v[60:63]
ds_read_b128 v[76:79], v21 offset:48128
s_waitcnt lgkmcnt(4)
v_mfma_f32_16x16x32_bf16 v[56:59], v[84:87], v[248:251], v[56:59]
v_mfma_f32_16x16x32_bf16 v[60:63], v[84:87], v[252:255], v[60:63]
ds_read_b128 v[80:83], v22
s_waitcnt lgkmcnt(4)
v_mfma_f32_16x16x32_bf16 v[64:67], v[88:91], v[224:227], v[64:67]
v_mfma_f32_16x16x32_bf16 v[68:71], v[88:91], v[228:231], v[68:71]
s_add_u32 m0, s75, 0x0
s_nop 0
buffer_load_dwordx4 v1, s[36:39], s74 offen offset:3072 sc1 lds
ds_read_b128 v[84:87], v22 offset:1024
s_waitcnt lgkmcnt(4)
v_mfma_f32_16x16x32_bf16 v[64:67], v[92:95], v[232:235], v[64:67]
v_mfma_f32_16x16x32_bf16 v[68:71], v[92:95], v[236:239], v[68:71]
ds_read_b128 v[88:91], v22 offset:2048
s_waitcnt lgkmcnt(4)
v_mfma_f32_16x16x32_bf16 v[64:67], v[72:75], v[240:243], v[64:67]
v_mfma_f32_16x16x32_bf16 v[68:71], v[72:75], v[244:247], v[68:71]
ds_read_b128 v[92:95], v22 offset:3072
s_waitcnt lgkmcnt(4)
v_mfma_f32_16x16x32_bf16 v[64:67], v[76:79], v[248:251], v[64:67]
v_mfma_f32_16x16x32_bf16 v[68:71], v[76:79], v[252:255], v[68:71]
ds_read_b128 v[72:75], v22 offset:4096
s_waitcnt lgkmcnt(4)
v_mfma_f32_16x16x32_bf16 v[40:43], v[80:83], v[128:131], 0
v_mfma_f32_16x16x32_bf16 v[44:47], v[80:83], v[132:135], 0
ds_write_b128 v23, v[60:63] offset:16384
ds_read_b128 v[76:79], v22 offset:5120
s_waitcnt lgkmcnt(5)
v_mfma_f32_16x16x32_bf16 v[40:43], v[84:87], v[136:139], v[40:43]
ds_write_b128 v23, v[68:71] offset:8192
v_mfma_f32_16x16x32_bf16 v[44:47], v[84:87], v[140:143], v[44:47]
ds_read_b128 v[80:83], v22 offset:6144
s_waitcnt lgkmcnt(6)
v_mfma_f32_16x16x32_bf16 v[40:43], v[88:91], v[144:147], v[40:43]
v_mfma_f32_16x16x32_bf16 v[44:47], v[88:91], v[148:151], v[44:47]
ds_read_b128 v[84:87], v22 offset:7168
s_waitcnt lgkmcnt(6)
v_mfma_f32_16x16x32_bf16 v[40:43], v[92:95], v[152:155], v[40:43]
s_waitcnt vmcnt(5) lgkmcnt(2)
s_barrier
v_mfma_f32_16x16x32_bf16 v[44:47], v[92:95], v[156:159], v[44:47]
ds_read_b128 v[96:99], v24 offset:16384
s_add_u32 m0, s75, 0x8000
s_add_u32 s74, s74, 0x8000
buffer_load_dwordx4 v1, s[36:39], s74 offen sc1 lds
ds_read_b128 v[88:91], v22 offset:8192
v_mfma_f32_16x16x32_bf16 v[40:43], v[72:75], v[160:163], v[40:43]
v_mfma_f32_16x16x32_bf16 v[44:47], v[72:75], v[164:167], v[44:47]
ds_read_b128 v[92:95], v22 offset:9216
v_mfma_f32_16x16x32_bf16 v[40:43], v[76:79], v[168:171], v[40:43]
v_mfma_f32_16x16x32_bf16 v[44:47], v[76:79], v[172:175], v[44:47]
s_waitcnt lgkmcnt(2)
v_add_f32 v56, v56, v96
ds_read_b128 v[72:75], v22 offset:10240
v_mfma_f32_16x16x32_bf16 v[40:43], v[80:83], v[176:179], v[40:43]
v_add_f32 v57, v57, v97
v_add_f32 v58, v58, v98
v_mfma_f32_16x16x32_bf16 v[44:47], v[80:83], v[180:183], v[44:47]
v_add_f32 v59, v59, v99
s_add_u32 m0, s75, 0x8000
s_nop 0
buffer_load_dwordx4 v1, s[36:39], s74 offen offset:1024 sc1 lds
s_cmp_eq_u32 s68, 0
s_cbranch_scc0 .Lfk_else110
v_lshlrev_b32 v96, 16, v16
v_and_b32 v97, s80, v16
v_lshlrev_b32 v98, 16, v17
v_and_b32 v99, s80, v17
v_add_f32 v56, v56, v96
v_add_f32 v57, v57, v97
v_add_f32 v58, v58, v98
v_add_f32 v59, v59, v99
.Lfk_else110:
ds_read_b128 v[76:79], v22 offset:11264
v_mfma_f32_16x16x32_bf16 v[40:43], v[84:87], v[184:187], v[40:43]
v_cvt_pk_bf16_f32 v32, v56, v57
v_cvt_pk_bf16_f32 v33, v58, v59
v_mfma_f32_16x16x32_bf16 v[44:47], v[84:87], v[188:191], v[44:47]
ds_read_b128 v[96:99], v24 offset:8192
v_min_u32 v32, -2, v32
v_min_u32 v33, -2, v33
ds_read_b128 v[80:83], v22 offset:12288
s_waitcnt lgkmcnt(5)
v_mfma_f32_16x16x32_bf16 v[40:43], v[88:91], v[192:195], v[40:43]
v_mfma_f32_16x16x32_bf16 v[44:47], v[88:91], v[196:199], v[44:47]
ds_read_b128 v[84:87], v22 offset:13312
s_waitcnt lgkmcnt(5)
v_mfma_f32_16x16x32_bf16 v[40:43], v[92:95], v[200:203], v[40:43]
v_mfma_f32_16x16x32_bf16 v[44:47], v[92:95], v[204:207], v[44:47]
s_add_u32 m0, s75, 0x8000
s_nop 0
buffer_load_dwordx4 v1, s[36:39], s74 offen offset:2048 sc1 lds
s_waitcnt lgkmcnt(2)
v_add_f32 v64, v64, v96
ds_read_b128 v[88:91], v22 offset:14336
v_mfma_f32_16x16x32_bf16 v[40:43], v[72:75], v[208:211], v[40:43]
v_add_f32 v65, v65, v97
v_add_f32 v66, v66, v98
v_mfma_f32_16x16x32_bf16 v[44:47], v[72:75], v[212:215], v[44:47]
v_add_f32 v67, v67, v99
s_cmp_eq_u32 s68, 0
s_cbranch_scc0 .Lfk_else111
v_lshlrev_b32 v96, 16, v18
v_and_b32 v97, s80, v18
v_lshlrev_b32 v98, 16, v19
v_and_b32 v99, s80, v19
v_add_f32 v64, v64, v96
v_add_f32 v65, v65, v97
v_add_f32 v66, v66, v98
v_add_f32 v67, v67, v99
.Lfk_else111:
ds_read_b128 v[92:95], v22 offset:15360
v_mfma_f32_16x16x32_bf16 v[40:43], v[76:79], v[216:219], v[40:43]
v_cvt_pk_bf16_f32 v34, v64, v65
v_cvt_pk_bf16_f32 v35, v66, v67
v_mfma_f32_16x16x32_bf16 v[44:47], v[76:79], v[220:223], v[44:47]
v_min_u32 v34, -2, v34
v_min_u32 v35, -2, v35
ds_read_b128 v[72:75], v22 offset:32768
s_waitcnt lgkmcnt(4)
v_mfma_f32_16x16x32_bf16 v[40:43], v[80:83], v[224:227], v[40:43]
s_add_u32 s78, s76, 0x0
s_mov_b64 exec, s[86:87]
buffer_store_dwordx4 v[32:35], v25, s[36:39], s78 offen sc1
s_mov_b64 exec, -1
v_mfma_f32_16x16x32_bf16 v[44:47], v[80:83], v[228:231], v[44:47]
s_add_u32 m0, s75, 0x8000
s_nop 0
buffer_load_dwordx4 v1, s[36:39], s74 offen offset:3072 sc1 lds
ds_read_b128 v[76:79], v22 offset:33792
s_waitcnt lgkmcnt(4)
v_mfma_f32_16x16x32_bf16 v[40:43], v[84:87], v[232:235], v[40:43]
v_mfma_f32_16x16x32_bf16 v[44:47], v[84:87], v[236:239], v[44:47]
ds_read_b128 v[80:83], v22 offset:34816
s_waitcnt lgkmcnt(4)
v_mfma_f32_16x16x32_bf16 v[40:43], v[88:91], v[240:243], v[40:43]
v_mfma_f32_16x16x32_bf16 v[44:47], v[88:91], v[244:247], v[44:47]
v_mov_b32 v108, 0
ds_read_b128 v[84:87], v22 offset:35840
s_waitcnt lgkmcnt(4)
v_mfma_f32_16x16x32_bf16 v[40:43], v[92:95], v[248:251], v[40:43]
v_mov_b32 v109, 0
v_mov_b32 v110, s60
v_mfma_f32_16x16x32_bf16 v[44:47], v[92:95], v[252:255], v[44:47]
v_mov_b32 v111, s60
ds_read_b128 v[88:91], v22 offset:36864
s_waitcnt lgkmcnt(4)
v_mfma_f32_16x16x32_bf16 v[64:67], v[72:75], v[128:131], 0
v_mfma_f32_16x16x32_bf16 v[68:71], v[72:75], v[132:135], 0
ds_read_b128 v[92:95], v22 offset:37888
s_waitcnt lgkmcnt(4)
v_mfma_f32_16x16x32_bf16 v[64:67], v[76:79], v[136:139], v[64:67]
ds_write_b128 v23, v[44:47]
v_mfma_f32_16x16x32_bf16 v[68:71], v[76:79], v[140:143], v[68:71]
ds_read_b128 v[72:75], v22 offset:38912
s_waitcnt lgkmcnt(5)
v_mfma_f32_16x16x32_bf16 v[64:67], v[80:83], v[144:147], v[64:67]
v_mfma_f32_16x16x32_bf16 v[68:71], v[80:83], v[148:151], v[68:71]
ds_read_b128 v[76:79], v22 offset:39936
s_waitcnt lgkmcnt(5)
v_mfma_f32_16x16x32_bf16 v[64:67], v[84:87], v[152:155], v[64:67]
s_waitcnt vmcnt(5) lgkmcnt(2)
s_barrier
v_mfma_f32_16x16x32_bf16 v[68:71], v[84:87], v[156:159], v[68:71]
ds_read_b128 v[96:99], v24
s_add_u32 m0, s75, 0x10000
s_add_u32 s74, s74, 0x8000
buffer_load_dwordx4 v1, s[36:39], s74 offen sc1 lds
ds_read_b128 v[80:83], v22 offset:40960
v_mfma_f32_16x16x32_bf16 v[64:67], v[88:91], v[160:163], v[64:67]
v_mfma_f32_16x16x32_bf16 v[68:71], v[88:91], v[164:167], v[68:71]
ds_read_b128 v[84:87], v22 offset:41984
v_mfma_f32_16x16x32_bf16 v[64:67], v[92:95], v[168:171], v[64:67]
v_mfma_f32_16x16x32_bf16 v[68:71], v[92:95], v[172:175], v[68:71]
s_waitcnt lgkmcnt(2)
v_add_f32 v40, v40, v96
ds_read_b128 v[88:91], v22 offset:43008
v_mfma_f32_16x16x32_bf16 v[64:67], v[72:75], v[176:179], v[64:67]
v_add_f32 v41, v41, v97
v_add_f32 v42, v42, v98
v_mfma_f32_16x16x32_bf16 v[68:71], v[72:75], v[180:183], v[68:71]
v_add_f32 v43, v43, v99
s_add_u32 m0, s75, 0x10000
s_nop 0
buffer_load_dwordx4 v1, s[36:39], s74 offen offset:1024 sc1 lds
s_cmp_eq_u32 s68, 1
s_cbranch_scc0 .Lfk_else112
v_lshlrev_b32 v96, 16, v16
v_and_b32 v97, s80, v16
v_lshlrev_b32 v98, 16, v17
v_and_b32 v99, s80, v17
v_add_f32 v40, v40, v96
v_add_f32 v41, v41, v97
v_add_f32 v42, v42, v98
v_add_f32 v43, v43, v99
.Lfk_else112:
ds_read_b128 v[92:95], v22 offset:44032
v_mfma_f32_16x16x32_bf16 v[64:67], v[76:79], v[184:187], v[64:67]
v_cvt_pk_bf16_f32 v36, v40, v41
v_cvt_pk_bf16_f32 v37, v42, v43
v_mfma_f32_16x16x32_bf16 v[68:71], v[76:79], v[188:191], v[68:71]
v_lshrrev_b32 v12, 2, v0
v_lshlrev_b32 v12, 8, v12
v_lshrrev_b32 v13, 2, v0
v_xor_b32 v13, s62, v13
v_and_b32 v13, 7, v13
v_lshlrev_b32 v13, 5, v13
v_add_u32 v12, v12, v13
v_and_b32 v13, 3, v0
v_lshlrev_b32 v13, 3, v13
v_add_u32 v12, v12, v13
v_add_u32 v55, 0x26000, v12
v_min_u32 v36, -2, v36
v_min_u32 v37, -2, v37
ds_read_b128 v[72:75], v22 offset:45056
s_waitcnt lgkmcnt(4)
v_mfma_f32_16x16x32_bf16 v[64:67], v[80:83], v[192:195], v[64:67]
ds_read_b64_tr_b16 v[120:121], v55
ds_read_b64_tr_b16 v[122:123], v55 offset:4096
v_mfma_f32_16x16x32_bf16 v[68:71], v[80:83], v[196:199], v[68:71]
ds_read_b128 v[76:79], v22 offset:46080
s_waitcnt lgkmcnt(6)
v_mfma_f32_16x16x32_bf16 v[64:67], v[84:87], v[200:203], v[64:67]
v_mfma_f32_16x16x32_bf16 v[68:71], v[84:87], v[204:207], v[68:71]
s_add_u32 m0, s75, 0x10000
s_nop 0
buffer_load_dwordx4 v1, s[36:39], s74 offen offset:2048 sc1 lds
ds_read_b128 v[80:83], v22 offset:47104
s_waitcnt lgkmcnt(6)
v_mfma_f32_16x16x32_bf16 v[64:67], v[88:91], v[208:211], v[64:67]
v_mfma_f32_16x16x32_bf16 v[68:71], v[88:91], v[212:215], v[68:71]
s_and_b32 s96, s2, 3
s_lshl_b32 s96, s96, 1
s_lshr_b32 s97, s62, 2
s_add_u32 s96, s96, s97
s_lshl_b32 s96, s96, 3
s_lshr_b32 s97, s2, 2
s_and_b32 s97, s97, 7
s_add_u32 s96, s96, s97
s_mul_i32 s96, s96, 7
s_lshr_b32 s97, s2, 5
s_add_u32 s96, s96, s97
s_lshl_b32 s96, s96, 12
s_add_u32 s96, 0x1800000, s96
s_and_b32 s97, s62, 3
s_lshl_b32 s97, s97, 10
s_add_u32 s78, s96, s97
s_waitcnt lgkmcnt(2)
s_cmp_ge_u32 s2, 224
s_cbranch_scc1 .Lfk_else113
buffer_store_dwordx4 v[120:123], v1, s[36:39], s78 offen sc1
.Lfk_else113:
ds_read_b128 v[84:87], v22 offset:48128
v_mfma_f32_16x16x32_bf16 v[64:67], v[92:95], v[216:219], v[64:67]
v_mfma_f32_16x16x32_bf16 v[68:71], v[92:95], v[220:223], v[68:71]
ds_read_b128 v[88:91], v21
v_mfma_f32_16x16x32_bf16 v[64:67], v[72:75], v[224:227], v[64:67]
v_mov_b32 v40, v32
v_mov_b32 v41, v33
v_mov_b32 v42, v34
v_mfma_f32_16x16x32_bf16 v[68:71], v[72:75], v[228:231], v[68:71]
v_mov_b32 v43, v35
s_add_u32 m0, s75, 0x10000
s_nop 0
buffer_load_dwordx4 v1, s[36:39], s74 offen offset:3072 sc1 lds
s_add_u32 s78, s76, 0x0
s_mov_b64 exec, s[86:87]
buffer_load_dwordx4 v[40:43], v27, s[36:39], s78 offen sc1 nt
s_mov_b64 exec, -1
ds_read_b128 v[92:95], v21 offset:1024
s_waitcnt lgkmcnt(4)
v_mfma_f32_16x16x32_bf16 v[64:67], v[76:79], v[232:235], v[64:67]
v_mfma_f32_16x16x32_bf16 v[68:71], v[76:79], v[236:239], v[68:71]
ds_read_b128 v[72:75], v21 offset:2048
s_waitcnt lgkmcnt(4)
v_mfma_f32_16x16x32_bf16 v[64:67], v[80:83], v[240:243], v[64:67]
v_mfma_f32_16x16x32_bf16 v[68:71], v[80:83], v[244:247], v[68:71]
ds_read_b128 v[76:79], v21 offset:3072
s_waitcnt lgkmcnt(4)
v_mfma_f32_16x16x32_bf16 v[64:67], v[84:87], v[248:251], v[64:67]
v_mfma_f32_16x16x32_bf16 v[68:71], v[84:87], v[252:255], v[68:71]
ds_read_b128 v[80:83], v21 offset:4096
s_waitcnt lgkmcnt(4)
v_mfma_f32_16x16x32_bf16 v[56:59], v[88:91], v[128:131], 0
v_mfma_f32_16x16x32_bf16 v[60:63], v[88:91], v[132:135], 0
ds_read_b128 v[84:87], v21 offset:5120
s_waitcnt lgkmcnt(4)
v_mfma_f32_16x16x32_bf16 v[56:59], v[92:95], v[136:139], v[56:59]
s_nop 0
ds_write_b128 v23, v[68:71] offset:8192
v_mfma_f32_16x16x32_bf16 v[60:63], v[92:95], v[140:143], v[60:63]
ds_read_b128 v[88:91], v21 offset:6144
s_waitcnt lgkmcnt(5)
v_mfma_f32_16x16x32_bf16 v[56:59], v[72:75], v[144:147], v[56:59]
v_mfma_f32_16x16x32_bf16 v[60:63], v[72:75], v[148:151], v[60:63]
ds_read_b128 v[92:95], v21 offset:7168
s_waitcnt lgkmcnt(5)
v_mfma_f32_16x16x32_bf16 v[56:59], v[76:79], v[152:155], v[56:59]
s_waitcnt vmcnt(5) lgkmcnt(2)
s_barrier
v_mfma_f32_16x16x32_bf16 v[60:63], v[76:79], v[156:159], v[60:63]
ds_read_b128 v[96:99], v24 offset:8192
s_add_u32 m0, s75, 0x18000
s_add_u32 s74, s74, 0x8000
buffer_load_dwordx4 v1, s[36:39], s74 offen sc1 lds
ds_read_b128 v[72:75], v21 offset:8192
v_mfma_f32_16x16x32_bf16 v[56:59], v[80:83], v[160:163], v[56:59]
v_mfma_f32_16x16x32_bf16 v[60:63], v[80:83], v[164:167], v[60:63]
ds_read_b128 v[76:79], v21 offset:9216
v_mfma_f32_16x16x32_bf16 v[56:59], v[84:87], v[168:171], v[56:59]
v_mfma_f32_16x16x32_bf16 v[60:63], v[84:87], v[172:175], v[60:63]
s_waitcnt lgkmcnt(2)
v_add_f32 v64, v64, v96
ds_read_b128 v[80:83], v21 offset:10240
v_mfma_f32_16x16x32_bf16 v[56:59], v[88:91], v[176:179], v[56:59]
v_add_f32 v65, v65, v97
v_add_f32 v66, v66, v98
v_mfma_f32_16x16x32_bf16 v[60:63], v[88:91], v[180:183], v[60:63]
v_add_f32 v67, v67, v99
s_add_u32 m0, s75, 0x18000
s_nop 0
buffer_load_dwordx4 v1, s[36:39], s74 offen offset:1024 sc1 lds
s_cmp_eq_u32 s68, 1
s_cbranch_scc0 .Lfk_else114
v_lshlrev_b32 v96, 16, v18
v_and_b32 v97, s80, v18
v_lshlrev_b32 v98, 16, v19
v_and_b32 v99, s80, v19
v_add_f32 v64, v64, v96
v_add_f32 v65, v65, v97
v_add_f32 v66, v66, v98
v_add_f32 v67, v67, v99
.Lfk_else114:
ds_read_b128 v[84:87], v21 offset:11264
v_mfma_f32_16x16x32_bf16 v[56:59], v[92:95], v[184:187], v[56:59]
v_cvt_pk_bf16_f32 v38, v64, v65
v_cvt_pk_bf16_f32 v39, v66, v67
v_mfma_f32_16x16x32_bf16 v[60:63], v[92:95], v[188:191], v[60:63]
v_min_u32 v38, -2, v38
v_min_u32 v39, -2, v39
ds_read_b128 v[88:91], v21 offset:12288
s_waitcnt lgkmcnt(4)
v_mfma_f32_16x16x32_bf16 v[56:59], v[72:75], v[192:195], v[56:59]
v_mfma_f32_16x16x32_bf16 v[60:63], v[72:75], v[196:199], v[60:63]
ds_read_b128 v[92:95], v21 offset:13312
s_waitcnt lgkmcnt(4)
v_mfma_f32_16x16x32_bf16 v[56:59], v[76:79], v[200:203], v[56:59]
v_mfma_f32_16x16x32_bf16 v[60:63], v[76:79], v[204:207], v[60:63]
s_add_u32 m0, s75, 0x18000
s_nop 0
buffer_load_dwordx4 v1, s[36:39], s74 offen offset:2048 sc1 lds
ds_read_b128 v[72:75], v21 offset:14336
s_waitcnt lgkmcnt(4)
v_mfma_f32_16x16x32_bf16 v[56:59], v[80:83], v[208:211], v[56:59]
s_waitcnt vmcnt(3)
v_max3_u32 v104, v40, v41, v42
v_max_u32 v104, v104, v43
v_cmp_eq_u32 vcc, -1, v104
s_cmp_lg_u64 vcc, 0
s_cbranch_scc1 .Lfk_rare116
.Lfk_join115:
s_add_u32 s78, s76, 0x8000
s_mov_b64 exec, s[86:87]
buffer_store_dwordx4 v[36:39], v25, s[36:39], s78 offen sc1
s_mov_b64 exec, -1
v_mfma_f32_16x16x32_bf16 v[100:103], v[48:51], v[40:43], 0
v_mfma_f32_16x16x32_bf16 v[60:63], v[80:83], v[212:215], v[60:63]
ds_read_b128 v[76:79], v21 offset:15360
s_waitcnt lgkmcnt(4)
v_mfma_f32_16x16x32_bf16 v[56:59], v[84:87], v[216:219], v[56:59]
s_nop 3
v_cvt_pk_bf16_f32 v104, v100, v101
v_min_u32 v105, -2, v104
v_mfma_f32_16x16x32_bf16 v[60:63], v[84:87], v[220:223], v[60:63]
v_min_u32 v104, -2, v104
v_add_f32 v108, v108, v100
v_fmac_f32 v109, v100, v100
ds_read_b128 v[80:83], v21 offset:32768
s_waitcnt lgkmcnt(4)
v_mfma_f32_16x16x32_bf16 v[56:59], v[88:91], v[224:227], v[56:59]
v_permlane16_swap_b32 v104, v105
v_mov_b32 v106, v104
v_mfma_f32_16x16x32_bf16 v[60:63], v[88:91], v[228:231], v[60:63]
v_mov_b32 v107, v105
v_add_f32 v108, v108, v101
s_add_u32 m0, s75, 0x18000
s_nop 0
buffer_load_dwordx4 v1, s[36:39], s74 offen offset:3072 sc1 lds
v_fmac_f32 v109, v101, v101
ds_read_b128 v[84:87], v21 offset:33792
s_waitcnt lgkmcnt(4)
v_mfma_f32_16x16x32_bf16 v[56:59], v[92:95], v[232:235], v[56:59]
v_permlane32_swap_b32 v104, v106
v_permlane32_swap_b32 v105, v107
s_add_u32 s78, s77, 0x0
s_mov_b64 exec, 0xffff
buffer_store_dwordx4 v[104:107], v1, s[36:39], s78 offen sc1
s_mov_b64 exec, -1
v_mfma_f32_16x16x32_bf16 v[60:63], v[92:95], v[236:239], v[60:63]
ds_read_b128 v[88:91], v21 offset:34816
s_waitcnt lgkmcnt(4)
v_mfma_f32_16x16x32_bf16 v[56:59], v[72:75], v[240:243], v[56:59]
v_mfma_f32_16x16x32_bf16 v[60:63], v[72:75], v[244:247], v[60:63]
ds_read_b128 v[92:95], v21 offset:35840
s_waitcnt lgkmcnt(4)
v_mfma_f32_16x16x32_bf16 v[56:59], v[76:79], v[248:251], v[56:59]
v_mfma_f32_16x16x32_bf16 v[60:63], v[76:79], v[252:255], v[60:63]
ds_read_b128 v[72:75], v21 offset:36864
s_waitcnt lgkmcnt(4)
v_mfma_f32_16x16x32_bf16 v[64:67], v[80:83], v[128:131], 0
v_mfma_f32_16x16x32_bf16 v[68:71], v[80:83], v[132:135], 0
ds_read_b128 v[76:79], v21 offset:37888
s_waitcnt lgkmcnt(4)
v_mfma_f32_16x16x32_bf16 v[64:67], v[84:87], v[136:139], v[64:67]
s_nop 0
ds_write_b128 v23, v[60:63]
v_mfma_f32_16x16x32_bf16 v[68:71], v[84:87], v[140:143], v[68:71]
ds_read_b128 v[80:83], v21 offset:38912
s_waitcnt lgkmcnt(5)
v_mfma_f32_16x16x32_bf16 v[64:67], v[88:91], v[144:147], v[64:67]
v_mfma_f32_16x16x32_bf16 v[68:71], v[88:91], v[148:151], v[68:71]
s_and_b32 s96, s2, 2
s_lshl_b32 s96, s96, 1
s_lshr_b32 s97, s64, 3
s_add_u32 s96, s96, s97
s_lshl_b32 s96, s96, 3
s_add_u32 s96, s96, s62
s_mul_i32 s96, s96, 0x7000
s_add_u32 s31, 0x1800000, s96
ds_read_b128 v[84:87], v21 offset:39936
s_waitcnt lgkmcnt(5)
v_mfma_f32_16x16x32_bf16 v[64:67], v[92:95], v[152:155], v[64:67]
s_waitcnt lgkmcnt(2)
s_barrier
v_mfma_f32_16x16x32_bf16 v[68:71], v[92:95], v[156:159], v[68:71]
ds_read_b128 v[96:99], v24
s_add_u32 s78, s30, 0x0
s_add_u32 m0, s89, 0x0
s_nop 0
buffer_load_dwordx4 v20, s[36:39], s78 offen sc1 lds
ds_read_b128 v[88:91], v21 offset:40960
v_mfma_f32_16x16x32_bf16 v[64:67], v[72:75], v[160:163], v[64:67]
v_mfma_f32_16x16x32_bf16 v[68:71], v[72:75], v[164:167], v[68:71]
ds_read_b128 v[92:95], v21 offset:41984
v_mfma_f32_16x16x32_bf16 v[64:67], v[76:79], v[168:171], v[64:67]
s_lshl_b32 s96, s82, 3
s_add_u32 s96, s96, s62
s_lshl_b32 s96, s96, 11
s_add_u32 s78, 0x80000, s96
buffer_load_dwordx4 v[120:123], v1, s[36:39], s78 offen sc1
buffer_load_dwordx4 v[124:127], v1, s[36:39], s78 offen offset:1024 sc1
v_mfma_f32_16x16x32_bf16 v[68:71], v[76:79], v[172:175], v[68:71]
s_waitcnt lgkmcnt(2)
v_add_f32 v56, v56, v96
ds_read_b128 v[72:75], v21 offset:43008
v_mfma_f32_16x16x32_bf16 v[64:67], v[80:83], v[176:179], v[64:67]
v_add_f32 v57, v57, v97
v_add_f32 v58, v58, v98
v_mfma_f32_16x16x32_bf16 v[68:71], v[80:83], v[180:183], v[68:71]
v_add_f32 v59, v59, v99
s_cmp_eq_u32 s68, 2
s_cbranch_scc0 .Lfk_else119
v_lshlrev_b32 v96, 16, v16
v_and_b32 v97, s80, v16
v_lshlrev_b32 v98, 16, v17
v_and_b32 v99, s80, v17
v_add_f32 v56, v56, v96
v_add_f32 v57, v57, v97
v_add_f32 v58, v58, v98
v_add_f32 v59, v59, v99
.Lfk_else119:
ds_read_b128 v[76:79], v21 offset:44032
v_mfma_f32_16x16x32_bf16 v[64:67], v[84:87], v[184:187], v[64:67]
v_cvt_pk_bf16_f32 v56, v56, v57
v_cvt_pk_bf16_f32 v58, v58, v59
v_mfma_f32_16x16x32_bf16 v[68:71], v[84:87], v[188:191], v[68:71]
s_add_u32 s78, s30, 0x2000
s_add_u32 m0, s89, 0x2000
s_nop 0
buffer_load_dwordx4 v20, s[36:39], s78 offen sc1 lds
v_cmp_eq_u32 vcc, v56, v32
s_nop 1
v_addc_co_u32 v32, vcc, 0, v56, vcc
v_cmp_eq_u32 vcc, v58, v33
s_nop 1
v_addc_co_u32 v33, vcc, 0, v58, vcc
ds_read_b128 v[80:83], v21 offset:45056
s_waitcnt lgkmcnt(4)
v_mfma_f32_16x16x32_bf16 v[64:67], v[88:91], v[192:195], v[64:67]
v_mfma_f32_16x16x32_bf16 v[68:71], v[88:91], v[196:199], v[68:71]
ds_read_b128 v[84:87], v21 offset:46080
s_waitcnt lgkmcnt(4)
v_mfma_f32_16x16x32_bf16 v[64:67], v[92:95], v[200:203], v[64:67]
v_mov_b32 v44, v36
v_mov_b32 v45, v37
v_mov_b32 v46, v38
v_mfma_f32_16x16x32_bf16 v[68:71], v[92:95], v[204:207], v[68:71]
v_mov_b32 v47, v39
s_add_u32 s78, s76, 0x8000
s_mov_b64 exec, s[86:87]
buffer_load_dwordx4 v[44:47], v27, s[36:39], s78 offen sc1 nt
s_mov_b64 exec, -1
ds_read_b128 v[88:91], v21 offset:47104
s_waitcnt lgkmcnt(4)
v_mfma_f32_16x16x32_bf16 v[64:67], v[72:75], v[208:211], v[64:67]
v_mfma_f32_16x16x32_bf16 v[68:71], v[72:75], v[212:215], v[68:71]
ds_read_b128 v[92:95], v21 offset:48128
s_waitcnt lgkmcnt(4)
v_mfma_f32_16x16x32_bf16 v[64:67], v[76:79], v[216:219], v[64:67]
s_add_u32 s78, s30, 0x10000
s_add_u32 m0, s89, 0x4000
s_nop 0
buffer_load_dwordx4 v20, s[36:39], s78 offen sc1 lds
v_mfma_f32_16x16x32_bf16 v[68:71], v[76:79], v[220:223], v[68:71]
ds_read_b128 v[72:75], v22
s_waitcnt lgkmcnt(4)
v_mfma_f32_16x16x32_bf16 v[64:67], v[80:83], v[224:227], v[64:67]
v_mfma_f32_16x16x32_bf16 v[68:71], v[80:83], v[228:231], v[68:71]
ds_read_b128 v[76:79], v22 offset:1024
s_waitcnt lgkmcnt(4)
v_mfma_f32_16x16x32_bf16 v[64:67], v[84:87], v[232:235], v[64:67]
v_mfma_f32_16x16x32_bf16 v[68:71], v[84:87], v[236:239], v[68:71]
ds_read_b128 v[80:83], v22 offset:2048
s_waitcnt lgkmcnt(4)
v_mfma_f32_16x16x32_bf16 v[64:67], v[88:91], v[240:243], v[64:67]
v_mfma_f32_16x16x32_bf16 v[68:71], v[88:91], v[244:247], v[68:71]
ds_read_b128 v[84:87], v22 offset:3072
s_waitcnt lgkmcnt(4)
v_mfma_f32_16x16x32_bf16 v[64:67], v[92:95], v[248:251], v[64:67]
s_add_u32 s78, s30, 0x12000
s_add_u32 m0, s89, 0x6000
s_nop 0
buffer_load_dwordx4 v20, s[36:39], s78 offen sc1 lds
v_mfma_f32_16x16x32_bf16 v[68:71], v[92:95], v[252:255], v[68:71]
ds_read_b128 v[88:91], v22 offset:4096
s_waitcnt lgkmcnt(4)
v_mfma_f32_16x16x32_bf16 v[56:59], v[72:75], v[128:131], 0
v_mfma_f32_16x16x32_bf16 v[60:63], v[72:75], v[132:135], 0
ds_read_b128 v[92:95], v22 offset:5120
s_waitcnt lgkmcnt(4)
v_mfma_f32_16x16x32_bf16 v[56:59], v[76:79], v[136:139], v[56:59]
s_nop 0
ds_write_b128 v23, v[68:71] offset:8192
v_mfma_f32_16x16x32_bf16 v[60:63], v[76:79], v[140:143], v[60:63]
ds_read_b128 v[72:75], v22 offset:6144
s_waitcnt lgkmcnt(5)
v_mfma_f32_16x16x32_bf16 v[56:59], v[80:83], v[144:147], v[56:59]
v_mfma_f32_16x16x32_bf16 v[60:63], v[80:83], v[148:151], v[60:63]
ds_read_b128 v[76:79], v22 offset:7168
s_waitcnt lgkmcnt(5)
v_mfma_f32_16x16x32_bf16 v[56:59], v[84:87], v[152:155], v[56:59]
s_waitcnt vmcnt(8) lgkmcnt(2)
s_barrier
v_mfma_f32_16x16x32_bf16 v[60:63], v[84:87], v[156:159], v[60:63]
ds_read_b128 v[96:99], v24 offset:8192
ds_read_b128 v[80:83], v22 offset:8192
v_mfma_f32_16x16x32_bf16 v[56:59], v[88:91], v[160:163], v[56:59]
v_mfma_f32_16x16x32_bf16 v[60:63], v[88:91], v[164:167], v[60:63]
ds_read_b128 v[84:87], v22 offset:9216
v_mfma_f32_16x16x32_bf16 v[56:59], v[92:95], v[168:171], v[56:59]
v_mfma_f32_16x16x32_bf16 v[60:63], v[92:95], v[172:175], v[60:63]
s_waitcnt lgkmcnt(2)
v_add_f32 v64, v64, v96
ds_read_b128 v[88:91], v22 offset:10240
v_mfma_f32_16x16x32_bf16 v[56:59], v[72:75], v[176:179], v[56:59]
v_add_f32 v65, v65, v97
v_add_f32 v66, v66, v98
v_mfma_f32_16x16x32_bf16 v[60:63], v[72:75], v[180:183], v[60:63]
v_add_f32 v67, v67, v99
s_cmp_eq_u32 s68, 2
s_cbranch_scc0 .Lfk_else120
v_lshlrev_b32 v96, 16, v18
v_and_b32 v97, s80, v18
v_lshlrev_b32 v98, 16, v19
v_and_b32 v99, s80, v19
v_add_f32 v64, v64, v96
v_add_f32 v65, v65, v97
v_add_f32 v66, v66, v98
v_add_f32 v67, v67, v99
.Lfk_else120:
ds_read_b128 v[92:95], v22 offset:11264
v_mfma_f32_16x16x32_bf16 v[56:59], v[76:79], v[184:187], v[56:59]
v_cvt_pk_bf16_f32 v64, v64, v65
v_cvt_pk_bf16_f32 v66, v66, v67
v_mfma_f32_16x16x32_bf16 v[60:63], v[76:79], v[188:191], v[60:63]
v_cmp_eq_u32 vcc, v64, v34
s_nop 1
v_addc_co_u32 v34, vcc, 0, v64, vcc
s_waitcnt vmcnt(2)
v_max3_u32 v104, v44, v45, v46
v_max_u32 v104, v104, v47
v_cmp_eq_u32 vcc, -1, v104
s_cmp_lg_u64 vcc, 0
s_cbranch_scc1 .Lfk_rare122
.Lfk_join121:
v_cmp_eq_u32 vcc, v66, v35
s_nop 1
v_addc_co_u32 v35, vcc, 0, v66, vcc
ds_read_b128 v[72:75], v22 offset:12288
s_waitcnt lgkmcnt(4)
v_mfma_f32_16x16x32_bf16 v[56:59], v[80:83], v[192:195], v[56:59]
v_mfma_f32_16x16x32_bf16 v[100:103], v[48:51], v[44:47], 0
s_add_u32 s78, s76, 0x0
s_mov_b64 exec, s[86:87]
buffer_store_dwordx4 v[32:35], v25, s[36:39], s78 offen sc1
s_mov_b64 exec, -1
v_mfma_f32_16x16x32_bf16 v[60:63], v[80:83], v[196:199], v[60:63]
ds_read_b128 v[76:79], v22 offset:13312
s_waitcnt lgkmcnt(4)
v_mfma_f32_16x16x32_bf16 v[56:59], v[84:87], v[200:203], v[56:59]
v_cvt_pk_bf16_f32 v104, v100, v101
v_min_u32 v105, -2, v104
v_min_u32 v104, -2, v104
v_mfma_f32_16x16x32_bf16 v[60:63], v[84:87], v[204:207], v[60:63]
v_add_f32 v108, v108, v100
v_fmac_f32 v109, v100, v100
v_permlane16_swap_b32 v104, v105
ds_read_b128 v[80:83], v22 offset:14336
s_waitcnt lgkmcnt(4)
v_mfma_f32_16x16x32_bf16 v[56:59], v[88:91], v[208:211], v[56:59]
v_mov_b32 v106, v104
v_mov_b32 v107, v105
v_mfma_f32_16x16x32_bf16 v[60:63], v[88:91], v[212:215], v[60:63]
v_add_f32 v108, v108, v101
v_fmac_f32 v109, v101, v101
v_permlane32_swap_b32 v104, v106
ds_read_b128 v[84:87], v22 offset:15360
s_waitcnt lgkmcnt(4)
v_mfma_f32_16x16x32_bf16 v[56:59], v[92:95], v[216:219], v[56:59]
v_permlane32_swap_b32 v105, v107
s_add_u32 s78, s77, 0x8000
s_mov_b64 exec, 0xffff
buffer_store_dwordx4 v[104:107], v1, s[36:39], s78 offen sc1
s_mov_b64 exec, -1
v_mfma_f32_16x16x32_bf16 v[60:63], v[92:95], v[220:223], v[60:63]
v_add_f32 v120, v120, v124
v_add_f32 v121, v121, v125
ds_read_b128 v[88:91], v22 offset:32768
s_waitcnt lgkmcnt(4)
v_mfma_f32_16x16x32_bf16 v[56:59], v[72:75], v[224:227], v[56:59]
v_add_f32 v122, v122, v126
v_add_f32 v123, v123, v127
v_mfma_f32_16x16x32_bf16 v[60:63], v[72:75], v[228:231], v[60:63]
v_and_b32 v12, 7, v0
v_lshlrev_b32 v12, 4, v12
v_lshrrev_b32 v13, 4, v0
v_and_b32 v13, 1, v13
v_lshlrev_b32 v13, 3, v13
v_add_u32 v12, v12, v13
v_lshrrev_b32 v13, 5, v0
v_lshlrev_b32 v13, 2, v13
v_add_u32 v12, v12, v13
s_lshl_b32 s96, s62, 7
s_add_u32 s96, s96, 0x27400
v_add_u32 v124, s96, v12
v_permlane32_swap_b32 v120, v121
ds_read_b128 v[92:95], v22 offset:33792
s_waitcnt lgkmcnt(4)
v_mfma_f32_16x16x32_bf16 v[56:59], v[76:79], v[232:235], v[56:59]
v_permlane32_swap_b32 v122, v123
v_add_f32 v120, v120, v121
v_mfma_f32_16x16x32_bf16 v[60:63], v[76:79], v[236:239], v[60:63]
v_add_f32 v122, v122, v123
ds_read_b128 v[72:75], v22 offset:34816
s_waitcnt lgkmcnt(4)
v_mfma_f32_16x16x32_bf16 v[56:59], v[80:83], v[240:243], v[56:59]
v_permlane16_swap_b32 v120, v122
v_add_f32 v120, v120, v122
v_mfma_f32_16x16x32_bf16 v[60:63], v[80:83], v[244:247], v[60:63]
s_nop 0
v_add_f32_dpp v120, v120, v120 row_ror:8 row_mask:0xf bank_mask:0xf
ds_read_b128 v[76:79], v22 offset:35840
s_waitcnt lgkmcnt(4)
v_mfma_f32_16x16x32_bf16 v[56:59], v[84:87], v[248:251], v[56:59]
ds_write_b32 v124, v120
v_mfma_f32_16x16x32_bf16 v[60:63], v[84:87], v[252:255], v[60:63]
ds_read_b128 v[80:83], v22 offset:36864
s_waitcnt lgkmcnt(5)
v_mfma_f32_16x16x32_bf16 v[64:67], v[88:91], v[128:131], 0
v_mfma_f32_16x16x32_bf16 v[68:71], v[88:91], v[132:135], 0
ds_read_b128 v[84:87], v22 offset:37888
s_waitcnt lgkmcnt(5)
v_mfma_f32_16x16x32_bf16 v[64:67], v[92:95], v[136:139], v[64:67]
s_nop 0
ds_write_b128 v23, v[60:63]
v_mfma_f32_16x16x32_bf16 v[68:71], v[92:95], v[140:143], v[68:71]
ds_read_b128 v[88:91], v22 offset:38912
s_waitcnt lgkmcnt(6)
v_mfma_f32_16x16x32_bf16 v[64:67], v[72:75], v[144:147], v[64:67]
v_mfma_f32_16x16x32_bf16 v[68:71], v[72:75], v[148:151], v[68:71]
ds_read_b128 v[92:95], v22 offset:39936
s_waitcnt lgkmcnt(6)
v_mfma_f32_16x16x32_bf16 v[64:67], v[76:79], v[152:155], v[64:67]
s_waitcnt lgkmcnt(2)
s_barrier
v_mfma_f32_16x16x32_bf16 v[68:71], v[76:79], v[156:159], v[68:71]
ds_read_b128 v[96:99], v24
s_add_u32 s78, s30, 0x20000
s_add_u32 m0, s89, 0x10000
s_nop 0
buffer_load_dwordx4 v20, s[36:39], s78 offen sc1 lds
ds_read_b128 v[72:75], v22 offset:40960
v_mfma_f32_16x16x32_bf16 v[64:67], v[80:83], v[160:163], v[64:67]
v_mfma_f32_16x16x32_bf16 v[68:71], v[80:83], v[164:167], v[68:71]
s_add_u32 s78, s30, 0x22000
s_add_u32 m0, s89, 0x12000
s_nop 0
buffer_load_dwordx4 v20, s[36:39], s78 offen sc1 lds
ds_read_b128 v[76:79], v22 offset:41984
v_mfma_f32_16x16x32_bf16 v[64:67], v[84:87], v[168:171], v[64:67]
v_mfma_f32_16x16x32_bf16 v[68:71], v[84:87], v[172:175], v[68:71]
s_add_u32 s78, s30, 0x30000
s_add_u32 m0, s89, 0x14000
s_nop 0
buffer_load_dwordx4 v20, s[36:39], s78 offen sc1 lds
s_waitcnt lgkmcnt(2)
v_add_f32 v56, v56, v96
ds_read_b128 v[80:83], v22 offset:43008
v_mfma_f32_16x16x32_bf16 v[64:67], v[88:91], v[176:179], v[64:67]
v_add_f32 v57, v57, v97
v_add_f32 v58, v58, v98
v_mfma_f32_16x16x32_bf16 v[68:71], v[88:91], v[180:183], v[68:71]
v_add_f32 v59, v59, v99
s_add_u32 s78, s30, 0x32000
s_add_u32 m0, s89, 0x16000
s_nop 0
buffer_load_dwordx4 v20, s[36:39], s78 offen sc1 lds
s_cmp_eq_u32 s68, 3
s_cbranch_scc0 .Lfk_else125
v_lshlrev_b32 v96, 16, v16
v_and_b32 v97, s80, v16
v_lshlrev_b32 v98, 16, v17
v_and_b32 v99, s80, v17
v_add_f32 v56, v56, v96
v_add_f32 v57, v57, v97
v_add_f32 v58, v58, v98
v_add_f32 v59, v59, v99
.Lfk_else125:
ds_read_b128 v[84:87], v22 offset:44032
v_mfma_f32_16x16x32_bf16 v[64:67], v[92:95], v[184:187], v[64:67]
v_cvt_pk_bf16_f32 v56, v56, v57
v_cvt_pk_bf16_f32 v58, v58, v59
v_mfma_f32_16x16x32_bf16 v[68:71], v[92:95], v[188:191], v[68:71]
v_cmp_eq_u32 vcc, v56, v36
s_nop 1
v_addc_co_u32 v36, vcc, 0, v56, vcc
v_cmp_eq_u32 vcc, v58, v37
s_nop 1
v_addc_co_u32 v37, vcc, 0, v58, vcc
ds_read_b128 v[88:91], v22 offset:45056
s_waitcnt lgkmcnt(4)
v_mfma_f32_16x16x32_bf16 v[64:67], v[72:75], v[192:195], v[64:67]
v_mfma_f32_16x16x32_bf16 v[68:71], v[72:75], v[196:199], v[68:71]
ds_read_b128 v[92:95], v22 offset:46080
s_waitcnt lgkmcnt(4)
v_mfma_f32_16x16x32_bf16 v[64:67], v[76:79], v[200:203], v[64:67]
v_mfma_f32_16x16x32_bf16 v[68:71], v[76:79], v[204:207], v[68:71]
ds_read_b128 v[72:75], v22 offset:47104
s_waitcnt lgkmcnt(4)
v_mfma_f32_16x16x32_bf16 v[64:67], v[80:83], v[208:211], v[64:67]
s_add_u32 s78, s76, 0x0
s_mov_b64 exec, s[86:87]
buffer_load_dwordx4 v[32:35], v27, s[36:39], s78 offen sc1
s_mov_b64 exec, -1
v_mfma_f32_16x16x32_bf16 v[68:71], v[80:83], v[212:215], v[68:71]
ds_read_b128 v[76:79], v22 offset:48128
s_waitcnt lgkmcnt(4)
v_mfma_f32_16x16x32_bf16 v[64:67], v[84:87], v[216:219], v[64:67]
v_add_u32 v29, 0x1000, v1
s_and_b32 s96, s62, 3
s_lshl_b32 s96, s96, 12
v_add_u32 v31, s96, v1
v_add_u32 v30, 0x10000, v29
v_add_u32 v4, 0x20000, v31
s_waitcnt vmcnt(7)
s_barrier
v_mfma_f32_16x16x32_bf16 v[68:71], v[84:87], v[220:223], v[68:71]
ds_read_b128 v[152:155], v31 offset:32768
ds_read_b128 v[156:159], v31 offset:34816
s_waitcnt lgkmcnt(5)
v_mfma_f32_16x16x32_bf16 v[64:67], v[88:91], v[224:227], v[64:67]
ds_read_b128 v[128:131], v29
v_mfma_f32_16x16x32_bf16 v[68:71], v[88:91], v[228:231], v[68:71]
ds_read_b128 v[132:135], v29 offset:8192
s_waitcnt lgkmcnt(6)
v_mfma_f32_16x16x32_bf16 v[64:67], v[92:95], v[232:235], v[64:67]
ds_read_b128 v[136:139], v29 offset:1024
v_mfma_f32_16x16x32_bf16 v[68:71], v[92:95], v[236:239], v[68:71]
ds_read_b128 v[140:143], v29 offset:9216
s_waitcnt lgkmcnt(7)
v_mfma_f32_16x16x32_bf16 v[64:67], v[72:75], v[240:243], v[64:67]
ds_read_b128 v[144:147], v29 offset:2048
v_mfma_f32_16x16x32_bf16 v[68:71], v[72:75], v[244:247], v[68:71]
ds_read_b128 v[148:151], v29 offset:10240
s_waitcnt lgkmcnt(8)
v_mfma_f32_16x16x32_bf16 v[64:67], v[76:79], v[248:251], v[64:67]
v_mfma_f32_16x16x32_bf16 v[68:71], v[76:79], v[252:255], v[68:71]
s_waitcnt lgkmcnt(5)
v_mfma_f32_16x16x32_bf16 v[192:195], v[128:131], v[152:155], 0
ds_read_b128 v[184:187], v31 offset:33792
v_mfma_f32_16x16x32_bf16 v[216:219], v[128:131], v[156:159], 0
ds_read_b128 v[188:191], v31 offset:35840
s_waitcnt lgkmcnt(6)
v_mfma_f32_16x16x32_bf16 v[196:199], v[132:135], v[152:155], 0
s_nop 0
ds_write_b128 v23, v[68:71] offset:16384
ds_read_b128 v[160:163], v29 offset:16384
v_mfma_f32_16x16x32_bf16 v[220:223], v[132:135], v[156:159], 0
ds_read_b128 v[164:167], v29 offset:24576
s_waitcnt lgkmcnt(8)
v_mfma_f32_16x16x32_bf16 v[200:203], v[136:139], v[152:155], 0
ds_read_b128 v[168:171], v29 offset:17408
v_mfma_f32_16x16x32_bf16 v[224:227], v[136:139], v[156:159], 0
ds_read_b128 v[172:175], v29 offset:25600
s_waitcnt lgkmcnt(9)
v_mfma_f32_16x16x32_bf16 v[204:207], v[140:143], v[152:155], 0
s_waitcnt lgkmcnt(4)
s_barrier
ds_read_b128 v[96:99], v24 offset:16384
s_add_u32 s78, s31, 0x0
buffer_load_dwordx4 v[56:59], v1, s[36:39], s78 offen offset:2048 sc1
s_add_u32 s78, s31, 0x0
buffer_load_dwordx4 v[60:63], v1, s[36:39], s78 offen offset:3072 sc1
s_add_u32 s78, s30, 0x4000
s_add_u32 m0, s89, 0x18000
s_nop 0
buffer_load_dwordx4 v20, s[36:39], s78 offen sc1 lds
ds_read_b128 v[176:179], v29 offset:18432
v_mfma_f32_16x16x32_bf16 v[228:231], v[140:143], v[156:159], 0
ds_read_b128 v[180:183], v29 offset:26624
v_mfma_f32_16x16x32_bf16 v[208:211], v[144:147], v[152:155], 0
s_waitcnt lgkmcnt(2)
v_add_f32 v64, v64, v96
v_add_f32 v65, v65, v97
s_add_u32 s78, s30, 0x6000
s_add_u32 m0, s89, 0x1a000
s_nop 0
buffer_load_dwordx4 v20, s[36:39], s78 offen sc1 lds
v_add_f32 v66, v66, v98
v_add_f32 v67, v67, v99
s_cmp_eq_u32 s68, 3
s_cbranch_scc0 .Lfk_else126
v_lshlrev_b32 v96, 16, v18
v_and_b32 v97, s80, v18
v_lshlrev_b32 v98, 16, v19
v_and_b32 v99, s80, v19
v_add_f32 v64, v64, v96
v_add_f32 v65, v65, v97
v_add_f32 v66, v66, v98
v_add_f32 v67, v67, v99
.Lfk_else126:
v_mfma_f32_16x16x32_bf16 v[232:235], v[144:147], v[156:159], 0
v_cvt_pk_bf16_f32 v64, v64, v65
v_cvt_pk_bf16_f32 v66, v66, v67
v_cmp_eq_u32 vcc, v64, v38
s_nop 1
v_addc_co_u32 v38, vcc, 0, v64, vcc
v_cmp_eq_u32 vcc, v66, v39
s_nop 1
v_addc_co_u32 v39, vcc, 0, v66, vcc
v_mfma_f32_16x16x32_bf16 v[212:215], v[148:151], v[152:155], 0
s_add_u32 s78, s30, 0x14000
s_add_u32 m0, s89, 0x1c000
s_nop 0
buffer_load_dwordx4 v20, s[36:39], s78 offen sc1 lds
v_mfma_f32_16x16x32_bf16 v[236:239], v[148:151], v[156:159], 0
s_add_u32 s78, s30, 0x16000
s_add_u32 m0, s89, 0x1e000
s_nop 0
buffer_load_dwordx4 v20, s[36:39], s78 offen sc1 lds
v_mfma_f32_16x16x32_bf16 v[192:195], v[160:163], v[184:187], v[192:195]
s_lshl_b32 s96, s82, 18
s_add_u32 s96, 0x1a00000, s96
s_lshr_b32 s97, s62, 1
s_and_b32 s97, s97, 1
s_lshl_b32 s97, s97, 16
s_add_u32 s96, s96, s97
s_and_b32 s97, s62, 1
s_lshl_b32 s97, s97, 13
s_add_u32 s30, s96, s97
s_and_b32 s96, s62, 3
s_lshl_b32 s89, s96, 13
v_mfma_f32_16x16x32_bf16 v[216:219], v[160:163], v[188:191], v[216:219]
v_mfma_f32_16x16x32_bf16 v[196:199], v[164:167], v[184:187], v[196:199]
s_waitcnt vmcnt(6)
v_cmp_eq_u32 vcc, v32, v40
v_cmp_eq_u32 s[100:101], v33, v41
s_or_b64 vcc, vcc, s[100:101]
v_cmp_eq_u32 s[100:101], v34, v42
s_or_b64 vcc, vcc, s[100:101]
v_cmp_eq_u32 s[100:101], v35, v43
s_or_b64 vcc, vcc, s[100:101]
s_cbranch_scc1 .Lfk_rare128
.Lfk_join127:
s_add_u32 s78, s76, 0x8000
s_mov_b64 exec, s[86:87]
buffer_store_dwordx4 v[36:39], v25, s[36:39], s78 offen sc1
s_mov_b64 exec, -1
v_mfma_f32_16x16x32_bf16 v[100:103], v[48:51], v[32:35], 0
v_mfma_f32_16x16x32_bf16 v[220:223], v[164:167], v[188:191], v[220:223]
s_nop 6
v_cvt_pk_bf16_f32 v104, v100, v101
v_min_u32 v105, -2, v104
v_min_u32 v104, -2, v104
v_mfma_f32_16x16x32_bf16 v[200:203], v[168:171], v[184:187], v[200:203]
v_add_f32 v108, v108, v100
v_fmac_f32 v109, v100, v100
v_permlane16_swap_b32 v104, v105
v_mov_b32 v106, v104
v_mov_b32 v107, v105
v_mfma_f32_16x16x32_bf16 v[224:227], v[168:171], v[188:191], v[224:227]
v_add_f32 v108, v108, v101
v_fmac_f32 v109, v101, v101
v_permlane32_swap_b32 v104, v106
v_permlane32_swap_b32 v105, v107
v_mfma_f32_16x16x32_bf16 v[204:207], v[172:175], v[184:187], v[204:207]
s_add_u32 s78, s77, 0x10000
s_mov_b64 exec, 0xffff
buffer_store_dwordx4 v[104:107], v1, s[36:39], s78 offen sc1
s_mov_b64 exec, -1
v_mfma_f32_16x16x32_bf16 v[228:231], v[172:175], v[188:191], v[228:231]
s_waitcnt lgkmcnt(1)
v_mfma_f32_16x16x32_bf16 v[208:211], v[176:179], v[184:187], v[208:211]
v_mfma_f32_16x16x32_bf16 v[232:235], v[176:179], v[188:191], v[232:235]
s_waitcnt lgkmcnt(0)
v_mfma_f32_16x16x32_bf16 v[212:215], v[180:183], v[184:187], v[212:215]
v_mfma_f32_16x16x32_bf16 v[236:239], v[180:183], v[188:191], v[236:239]
s_setprio 3
s_barrier
s_add_u32 s78, s30, 0x24000
s_add_u32 m0, s89, 0x0
s_nop 0
buffer_load_dwordx4 v1, s[36:39], s78 offen sc1 lds
ds_read_b128 v[152:155], v31 offset:49152
ds_read_b128 v[156:159], v31 offset:51200
buffer_load_dwordx4 v1, s[36:39], s78 offen offset:1024 sc1 lds
ds_read_b128 v[128:131], v30
ds_read_b128 v[132:135], v30 offset:8192
buffer_load_dwordx4 v1, s[36:39], s78 offen offset:2048 sc1 lds
ds_read_b128 v[136:139], v30 offset:1024
ds_read_b128 v[140:143], v30 offset:9216
buffer_load_dwordx4 v1, s[36:39], s78 offen offset:3072 sc1 lds
ds_read_b128 v[144:147], v30 offset:2048
ds_read_b128 v[148:151], v30 offset:10240
s_add_u32 s78, s30, 0x25000
s_add_u32 m0, s89, 0x1000
s_nop 0
buffer_load_dwordx4 v1, s[36:39], s78 offen sc1 lds
ds_read_b128 v[184:187], v31 offset:50176
ds_read_b128 v[188:191], v31 offset:52224
buffer_load_dwordx4 v1, s[36:39], s78 offen offset:1024 sc1 lds
ds_read_b128 v[160:163], v30 offset:16384
ds_read_b128 v[164:167], v30 offset:24576
buffer_load_dwordx4 v1, s[36:39], s78 offen offset:2048 sc1 lds
ds_read_b128 v[168:171], v30 offset:17408
ds_read_b128 v[172:175], v30 offset:25600
s_add_u32 s78, s31, 0x0
buffer_load_dwordx4 v[32:35], v1, s[36:39], s78 offen sc1
ds_read_b128 v[176:179], v30 offset:18432
ds_read_b128 v[180:183], v30 offset:26624
s_add_u32 s78, s31, 0x1000
buffer_load_dwordx4 v[72:75], v1, s[36:39], s78 offen sc1
s_setprio 0
s_waitcnt lgkmcnt(13)
v_mfma_f32_16x16x32_bf16 v[192:195], v[128:131], v[152:155], v[192:195]
v_mfma_f32_16x16x32_bf16 v[216:219], v[128:131], v[156:159], v[216:219]
s_waitcnt lgkmcnt(12)
v_mfma_f32_16x16x32_bf16 v[196:199], v[132:135], v[152:155], v[196:199]
v_mfma_f32_16x16x32_bf16 v[220:223], v[132:135], v[156:159], v[220:223]
s_waitcnt lgkmcnt(11)
v_mfma_f32_16x16x32_bf16 v[200:203], v[136:139], v[152:155], v[200:203]
v_mfma_f32_16x16x32_bf16 v[224:227], v[136:139], v[156:159], v[224:227]
s_waitcnt lgkmcnt(10)
v_mfma_f32_16x16x32_bf16 v[204:207], v[140:143], v[152:155], v[204:207]
v_mfma_f32_16x16x32_bf16 v[228:231], v[140:143], v[156:159], v[228:231]
s_waitcnt lgkmcnt(9)
v_mfma_f32_16x16x32_bf16 v[208:211], v[144:147], v[152:155], v[208:211]
v_mfma_f32_16x16x32_bf16 v[232:235], v[144:147], v[156:159], v[232:235]
s_waitcnt lgkmcnt(8)
v_mfma_f32_16x16x32_bf16 v[212:215], v[148:151], v[152:155], v[212:215]
v_mfma_f32_16x16x32_bf16 v[236:239], v[148:151], v[156:159], v[236:239]
s_waitcnt lgkmcnt(5)
v_mfma_f32_16x16x32_bf16 v[192:195], v[160:163], v[184:187], v[192:195]
v_mfma_f32_16x16x32_bf16 v[216:219], v[160:163], v[188:191], v[216:219]
s_waitcnt lgkmcnt(4)
v_mfma_f32_16x16x32_bf16 v[196:199], v[164:167], v[184:187], v[196:199]
v_mfma_f32_16x16x32_bf16 v[220:223], v[164:167], v[188:191], v[220:223]
s_waitcnt lgkmcnt(3)
v_mfma_f32_16x16x32_bf16 v[200:203], v[168:171], v[184:187], v[200:203]
v_mfma_f32_16x16x32_bf16 v[224:227], v[168:171], v[188:191], v[224:227]
s_waitcnt lgkmcnt(2)
v_mfma_f32_16x16x32_bf16 v[204:207], v[172:175], v[184:187], v[204:207]
v_mfma_f32_16x16x32_bf16 v[228:231], v[172:175], v[188:191], v[228:231]
s_waitcnt lgkmcnt(1)
v_mfma_f32_16x16x32_bf16 v[208:211], v[176:179], v[184:187], v[208:211]
v_mfma_f32_16x16x32_bf16 v[232:235], v[176:179], v[188:191], v[232:235]
s_waitcnt lgkmcnt(0)
v_mfma_f32_16x16x32_bf16 v[212:215], v[180:183], v[184:187], v[212:215]
v_mfma_f32_16x16x32_bf16 v[236:239], v[180:183], v[188:191], v[236:239]
s_setprio 3
s_add_u32 s78, s76, 0x8000
s_mov_b64 exec, s[86:87]
buffer_load_dwordx4 v[36:39], v27, s[36:39], s78 offen sc1
s_mov_b64 exec, -1
s_waitcnt vmcnt(12)
s_barrier
s_add_u32 s78, s30, 0x8000
s_add_u32 m0, s89, 0x10000
s_nop 0
buffer_load_dwordx4 v1, s[36:39], s78 offen sc1 lds
ds_read_b128 v[152:155], v4
ds_read_b128 v[156:159], v4 offset:2048
buffer_load_dwordx4 v1, s[36:39], s78 offen offset:1024 sc1 lds
ds_read_b128 v[128:131], v30 offset:32768
ds_read_b128 v[132:135], v30 offset:40960
buffer_load_dwordx4 v1, s[36:39], s78 offen offset:2048 sc1 lds
ds_read_b128 v[136:139], v30 offset:33792
ds_read_b128 v[140:143], v30 offset:41984
buffer_load_dwordx4 v1, s[36:39], s78 offen offset:3072 sc1 lds
ds_read_b128 v[144:147], v30 offset:34816
ds_read_b128 v[148:151], v30 offset:43008
s_add_u32 s78, s30, 0x9000
s_add_u32 m0, s89, 0x11000
s_nop 0
buffer_load_dwordx4 v1, s[36:39], s78 offen sc1 lds
ds_read_b128 v[184:187], v4 offset:1024
ds_read_b128 v[188:191], v4 offset:3072
buffer_load_dwordx4 v1, s[36:39], s78 offen offset:1024 sc1 lds
ds_read_b128 v[160:163], v30 offset:49152
ds_read_b128 v[164:167], v30 offset:57344
buffer_load_dwordx4 v1, s[36:39], s78 offen offset:2048 sc1 lds
ds_read_b128 v[168:171], v30 offset:50176
ds_read_b128 v[172:175], v30 offset:58368
s_add_u32 s78, s31, 0x1000
buffer_load_dwordx4 v[76:79], v1, s[36:39], s78 offen offset:1024 sc1
ds_read_b128 v[176:179], v30 offset:51200
ds_read_b128 v[180:183], v30 offset:59392
s_add_u32 s78, s31, 0x1000
buffer_load_dwordx4 v[80:83], v1, s[36:39], s78 offen offset:2048 sc1
s_setprio 0
s_waitcnt lgkmcnt(13)
v_mfma_f32_16x16x32_bf16 v[192:195], v[128:131], v[152:155], v[192:195]
v_mfma_f32_16x16x32_bf16 v[216:219], v[128:131], v[156:159], v[216:219]
s_waitcnt lgkmcnt(12)
v_mfma_f32_16x16x32_bf16 v[196:199], v[132:135], v[152:155], v[196:199]
v_mfma_f32_16x16x32_bf16 v[220:223], v[132:135], v[156:159], v[220:223]
s_waitcnt lgkmcnt(11)
v_mfma_f32_16x16x32_bf16 v[200:203], v[136:139], v[152:155], v[200:203]
v_mfma_f32_16x16x32_bf16 v[224:227], v[136:139], v[156:159], v[224:227]
s_waitcnt lgkmcnt(10)
v_mfma_f32_16x16x32_bf16 v[204:207], v[140:143], v[152:155], v[204:207]
v_mfma_f32_16x16x32_bf16 v[228:231], v[140:143], v[156:159], v[228:231]
s_waitcnt lgkmcnt(9)
v_mfma_f32_16x16x32_bf16 v[208:211], v[144:147], v[152:155], v[208:211]
v_mfma_f32_16x16x32_bf16 v[232:235], v[144:147], v[156:159], v[232:235]
s_waitcnt vmcnt(9)
v_cmp_eq_u32 vcc, v36, v44
v_cmp_eq_u32 s[100:101], v37, v45
s_or_b64 vcc, vcc, s[100:101]
v_cmp_eq_u32 s[100:101], v38, v46
s_or_b64 vcc, vcc, s[100:101]
v_cmp_eq_u32 s[100:101], v39, v47
s_or_b64 vcc, vcc, s[100:101]
s_cbranch_scc1 .Lfk_rare132
.Lfk_join131:
s_waitcnt lgkmcnt(8)
v_mfma_f32_16x16x32_bf16 v[212:215], v[148:151], v[152:155], v[212:215]
v_mfma_f32_16x16x32_bf16 v[100:103], v[48:51], v[36:39], 0
v_mfma_f32_16x16x32_bf16 v[236:239], v[148:151], v[156:159], v[236:239]
s_waitcnt lgkmcnt(5)
v_mfma_f32_16x16x32_bf16 v[192:195], v[160:163], v[184:187], v[192:195]
s_nop 4
v_cvt_pk_bf16_f32 v104, v100, v101
v_mfma_f32_16x16x32_bf16 v[216:219], v[160:163], v[188:191], v[216:219]
v_min_u32 v105, -2, v104
s_waitcnt lgkmcnt(4)
v_mfma_f32_16x16x32_bf16 v[196:199], v[164:167], v[184:187], v[196:199]
v_min_u32 v104, -2, v104
v_add_f32 v108, v108, v100
v_mfma_f32_16x16x32_bf16 v[220:223], v[164:167], v[188:191], v[220:223]
v_fmac_f32 v109, v100, v100
v_permlane16_swap_b32 v104, v105
s_waitcnt lgkmcnt(3)
v_mfma_f32_16x16x32_bf16 v[200:203], v[168:171], v[184:187], v[200:203]
v_mov_b32 v106, v104
v_mfma_f32_16x16x32_bf16 v[224:227], v[168:171], v[188:191], v[224:227]
v_mov_b32 v107, v105
v_add_f32 v108, v108, v101
s_waitcnt lgkmcnt(2)
v_mfma_f32_16x16x32_bf16 v[204:207], v[172:175], v[184:187], v[204:207]
v_fmac_f32 v109, v101, v101
v_permlane32_swap_b32 v104, v106
v_mfma_f32_16x16x32_bf16 v[228:231], v[172:175], v[188:191], v[228:231]
v_permlane32_swap_b32 v105, v107
s_waitcnt lgkmcnt(1)
v_mfma_f32_16x16x32_bf16 v[208:211], v[176:179], v[184:187], v[208:211]
v_permlane32_swap_b32 v108, v109
v_add_f32 v108, v108, v109
v_mfma_f32_16x16x32_bf16 v[232:235], v[176:179], v[188:191], v[232:235]
v_mov_b32 v109, v108
s_nop 1
v_permlane16_swap_b32 v108, v109
s_waitcnt lgkmcnt(0)
v_mfma_f32_16x16x32_bf16 v[212:215], v[180:183], v[184:187], v[212:215]
v_add_f32 v108, v108, v109
v_mfma_f32_16x16x32_bf16 v[236:239], v[180:183], v[188:191], v[236:239]
v_mov_b32 v109, v108
s_nop 1
v_permlane32_swap_b32 v108, v109
s_setprio 3
s_add_u32 s78, s77, 0x18000
s_mov_b64 exec, 0xffff
buffer_store_dwordx4 v[104:107], v1, s[36:39], s78 offen sc1
s_mov_b64 exec, -1
s_lshl_b32 s96, s69, 3
s_add_u32 s96, s96, s88
s_lshl_b32 s96, s96, 4
s_lshl_b32 s97, s67, 2
s_add_u32 s96, s96, s97
s_add_u32 s96, s96, s68
s_lshl_b32 s96, s96, 8
s_add_u32 s78, 0xe00000, s96
s_mov_b64 exec, 0xffff
buffer_store_dwordx4 v[108:111], v1, s[36:39], s78 offen sc1
s_mov_b64 exec, -1
s_barrier
s_add_u32 s78, s30, 0x28000
s_add_u32 m0, s89, 0x18000
s_nop 0
buffer_load_dwordx4 v1, s[36:39], s78 offen sc1 lds
ds_read_b128 v[152:155], v31 offset:32768
ds_read_b128 v[156:159], v31 offset:34816
buffer_load_dwordx4 v1, s[36:39], s78 offen offset:1024 sc1 lds
ds_read_b128 v[128:131], v29
ds_read_b128 v[132:135], v29 offset:8192
buffer_load_dwordx4 v1, s[36:39], s78 offen offset:2048 sc1 lds
ds_read_b128 v[136:139], v29 offset:1024
ds_read_b128 v[140:143], v29 offset:9216
buffer_load_dwordx4 v1, s[36:39], s78 offen offset:3072 sc1 lds
ds_read_b128 v[144:147], v29 offset:2048
ds_read_b128 v[148:151], v29 offset:10240
s_add_u32 s78, s30, 0x29000
s_add_u32 m0, s89, 0x19000
s_nop 0
buffer_load_dwordx4 v1, s[36:39], s78 offen sc1 lds
ds_read_b128 v[184:187], v31 offset:33792
ds_read_b128 v[188:191], v31 offset:35840
buffer_load_dwordx4 v1, s[36:39], s78 offen offset:1024 sc1 lds
ds_read_b128 v[160:163], v29 offset:16384
ds_read_b128 v[164:167], v29 offset:24576
buffer_load_dwordx4 v1, s[36:39], s78 offen offset:2048 sc1 lds
ds_read_b128 v[168:171], v29 offset:17408
ds_read_b128 v[172:175], v29 offset:25600
s_add_u32 s78, s31, 0x0
buffer_load_dwordx4 v[36:39], v1, s[36:39], s78 offen offset:1024 sc1
ds_read_b128 v[176:179], v29 offset:18432
ds_read_b128 v[180:183], v29 offset:26624
s_add_u32 s78, s31, 0x1000
buffer_load_dwordx4 v[84:87], v1, s[36:39], s78 offen offset:3072 sc1
s_setprio 0
s_waitcnt lgkmcnt(13)
v_mfma_f32_16x16x32_bf16 v[192:195], v[128:131], v[152:155], v[192:195]
v_mfma_f32_16x16x32_bf16 v[216:219], v[128:131], v[156:159], v[216:219]
s_waitcnt lgkmcnt(12)
v_mfma_f32_16x16x32_bf16 v[196:199], v[132:135], v[152:155], v[196:199]
v_mfma_f32_16x16x32_bf16 v[220:223], v[132:135], v[156:159], v[220:223]
s_waitcnt lgkmcnt(11)
v_mfma_f32_16x16x32_bf16 v[200:203], v[136:139], v[152:155], v[200:203]
v_mfma_f32_16x16x32_bf16 v[224:227], v[136:139], v[156:159], v[224:227]
s_waitcnt lgkmcnt(10)
v_mfma_f32_16x16x32_bf16 v[204:207], v[140:143], v[152:155], v[204:207]
v_mfma_f32_16x16x32_bf16 v[228:231], v[140:143], v[156:159], v[228:231]
s_waitcnt lgkmcnt(9)
v_mfma_f32_16x16x32_bf16 v[208:211], v[144:147], v[152:155], v[208:211]
v_mfma_f32_16x16x32_bf16 v[232:235], v[144:147], v[156:159], v[232:235]
s_waitcnt lgkmcnt(8)
v_mfma_f32_16x16x32_bf16 v[212:215], v[148:151], v[152:155], v[212:215]
v_mfma_f32_16x16x32_bf16 v[236:239], v[148:151], v[156:159], v[236:239]
s_waitcnt lgkmcnt(5)
v_mfma_f32_16x16x32_bf16 v[192:195], v[160:163], v[184:187], v[192:195]
v_mfma_f32_16x16x32_bf16 v[216:219], v[160:163], v[188:191], v[216:219]
s_waitcnt lgkmcnt(4)
v_mfma_f32_16x16x32_bf16 v[196:199], v[164:167], v[184:187], v[196:199]
v_mfma_f32_16x16x32_bf16 v[220:223], v[164:167], v[188:191], v[220:223]
s_waitcnt lgkmcnt(3)
v_mfma_f32_16x16x32_bf16 v[200:203], v[168:171], v[184:187], v[200:203]
v_mfma_f32_16x16x32_bf16 v[224:227], v[168:171], v[188:191], v[224:227]
s_waitcnt lgkmcnt(2)
v_mfma_f32_16x16x32_bf16 v[204:207], v[172:175], v[184:187], v[204:207]
v_mfma_f32_16x16x32_bf16 v[228:231], v[172:175], v[188:191], v[228:231]
s_waitcnt lgkmcnt(1)
v_mfma_f32_16x16x32_bf16 v[208:211], v[176:179], v[184:187], v[208:211]
v_mfma_f32_16x16x32_bf16 v[232:235], v[176:179], v[188:191], v[232:235]
s_waitcnt lgkmcnt(0)
v_mfma_f32_16x16x32_bf16 v[212:215], v[180:183], v[184:187], v[212:215]
v_mfma_f32_16x16x32_bf16 v[236:239], v[180:183], v[188:191], v[236:239]
s_setprio 3
s_waitcnt vmcnt(13)
s_barrier
s_add_u32 s78, s30, 0xc000
s_add_u32 m0, s89, 0x0
s_nop 0
buffer_load_dwordx4 v1, s[36:39], s78 offen sc1 lds
ds_read_b128 v[152:155], v31 offset:49152
ds_read_b128 v[156:159], v31 offset:51200
buffer_load_dwordx4 v1, s[36:39], s78 offen offset:1024 sc1 lds
ds_read_b128 v[128:131], v30
ds_read_b128 v[132:135], v30 offset:8192
buffer_load_dwordx4 v1, s[36:39], s78 offen offset:2048 sc1 lds
ds_read_b128 v[136:139], v30 offset:1024
ds_read_b128 v[140:143], v30 offset:9216
buffer_load_dwordx4 v1, s[36:39], s78 offen offset:3072 sc1 lds
ds_read_b128 v[144:147], v30 offset:2048
ds_read_b128 v[148:151], v30 offset:10240
s_add_u32 s78, s30, 0xd000
s_add_u32 m0, s89, 0x1000
s_nop 0
buffer_load_dwordx4 v1, s[36:39], s78 offen sc1 lds
ds_read_b128 v[184:187], v31 offset:50176
ds_read_b128 v[188:191], v31 offset:52224
buffer_load_dwordx4 v1, s[36:39], s78 offen offset:1024 sc1 lds
ds_read_b128 v[160:163], v30 offset:16384
ds_read_b128 v[164:167], v30 offset:24576
buffer_load_dwordx4 v1, s[36:39], s78 offen offset:2048 sc1 lds
ds_read_b128 v[168:171], v30 offset:17408
ds_read_b128 v[172:175], v30 offset:25600
s_add_u32 s78, s31, 0x2000
buffer_load_dwordx4 v[52:55], v1, s[36:39], s78 offen sc1
ds_read_b128 v[176:179], v30 offset:18432
ds_read_b128 v[180:183], v30 offset:26624
s_add_u32 s78, s31, 0x2000
buffer_load_dwordx4 v[64:67], v1, s[36:39], s78 offen offset:1024 sc1
s_setprio 0
s_waitcnt lgkmcnt(13)
v_mfma_f32_16x16x32_bf16 v[192:195], v[128:131], v[152:155], v[192:195]
v_mfma_f32_16x16x32_bf16 v[216:219], v[128:131], v[156:159], v[216:219]
s_waitcnt lgkmcnt(12)
v_mfma_f32_16x16x32_bf16 v[196:199], v[132:135], v[152:155], v[196:199]
s_lshl_b32 s96, s83, 3
s_and_b32 s97, s62, 3
s_lshl_b32 s97, s97, 1
s_add_u32 s96, s96, s97
s_lshl_b32 s96, s96, 3
s_lshr_b32 s97, s82, 1
s_add_u32 s96, s96, s97
s_mul_i32 s96, s96, 0x1c00
s_add_u32 s96, 0x700000, s96
s_and_b32 s97, s82, 1
s_mul_i32 s97, s97, 3
s_add_u32 s97, s97, 2
s_lshl_b32 s97, s97, 10
s_add_u32 s32, s96, s97
v_mfma_f32_16x16x32_bf16 v[220:223], v[132:135], v[156:159], v[220:223]
s_waitcnt lgkmcnt(11)
v_mfma_f32_16x16x32_bf16 v[200:203], v[136:139], v[152:155], v[200:203]
s_lshl_b32 s96, s83, 3
s_and_b32 s97, s62, 3
s_lshl_b32 s97, s97, 1
s_add_u32 s96, s96, s97
s_and_b32 s97, s82, 1
s_add_u32 s96, s96, s97
s_lshl_b32 s96, s96, 3
s_lshr_b32 s97, s82, 1
s_add_u32 s96, s96, s97
s_mul_i32 s96, s96, 0x1c00
s_add_u32 s96, 0x700000, s96
s_add_u32 s79, s96, 0x1800
v_mfma_f32_16x16x32_bf16 v[224:227], v[136:139], v[156:159], v[224:227]
s_waitcnt lgkmcnt(10)
v_mfma_f32_16x16x32_bf16 v[204:207], v[140:143], v[152:155], v[204:207]
v_lshrrev_b32 v12, 4, v0
v_lshlrev_b32 v12, 5, v12
v_add_u32 v20, 0x27600, v12
v_mfma_f32_16x16x32_bf16 v[228:231], v[140:143], v[156:159], v[228:231]
s_waitcnt lgkmcnt(9)
v_mfma_f32_16x16x32_bf16 v[208:211], v[144:147], v[152:155], v[208:211]
v_mfma_f32_16x16x32_bf16 v[232:235], v[144:147], v[156:159], v[232:235]
v_lshlrev_b32 v12, 1, v3
s_and_b32 s96, s62, 3
s_lshl_b32 s96, s96, 9
v_add_u32 v12, s96, v12
v_add_u32 v21, 0x27800, v12
s_waitcnt lgkmcnt(8)
v_mfma_f32_16x16x32_bf16 v[212:215], v[148:151], v[152:155], v[212:215]
v_mfma_f32_16x16x32_bf16 v[236:239], v[148:151], v[156:159], v[236:239]
s_waitcnt lgkmcnt(5)
v_mfma_f32_16x16x32_bf16 v[192:195], v[160:163], v[184:187], v[192:195]
v_mfma_f32_16x16x32_bf16 v[216:219], v[160:163], v[188:191], v[216:219]
s_waitcnt lgkmcnt(4)
v_mfma_f32_16x16x32_bf16 v[196:199], v[164:167], v[184:187], v[196:199]
v_mfma_f32_16x16x32_bf16 v[220:223], v[164:167], v[188:191], v[220:223]
s_waitcnt lgkmcnt(3)
v_mfma_f32_16x16x32_bf16 v[200:203], v[168:171], v[184:187], v[200:203]
v_mfma_f32_16x16x32_bf16 v[224:227], v[168:171], v[188:191], v[224:227]
s_waitcnt lgkmcnt(2)
v_mfma_f32_16x16x32_bf16 v[204:207], v[172:175], v[184:187], v[204:207]
v_mfma_f32_16x16x32_bf16 v[228:231], v[172:175], v[188:191], v[228:231]
s_waitcnt lgkmcnt(1)
v_mfma_f32_16x16x32_bf16 v[208:211], v[176:179], v[184:187], v[208:211]
v_mfma_f32_16x16x32_bf16 v[232:235], v[176:179], v[188:191], v[232:235]
s_waitcnt lgkmcnt(0)
v_mfma_f32_16x16x32_bf16 v[212:215], v[180:183], v[184:187], v[212:215]
v_mfma_f32_16x16x32_bf16 v[236:239], v[180:183], v[188:191], v[236:239]
s_setprio 3
s_waitcnt vmcnt(11)
s_barrier
s_add_u32 s78, s30, 0x2c000
s_add_u32 m0, s89, 0x10000
s_nop 0
buffer_load_dwordx4 v1, s[36:39], s78 offen sc1 lds
ds_read_b128 v[152:155], v4
ds_read_b128 v[156:159], v4 offset:2048
buffer_load_dwordx4 v1, s[36:39], s78 offen offset:1024 sc1 lds
ds_read_b128 v[128:131], v30 offset:32768
ds_read_b128 v[132:135], v30 offset:40960
buffer_load_dwordx4 v1, s[36:39], s78 offen offset:2048 sc1 lds
ds_read_b128 v[136:139], v30 offset:33792
ds_read_b128 v[140:143], v30 offset:41984
buffer_load_dwordx4 v1, s[36:39], s78 offen offset:3072 sc1 lds
ds_read_b128 v[144:147], v30 offset:34816
ds_read_b128 v[148:151], v30 offset:43008
s_add_u32 s78, s30, 0x2d000
s_add_u32 m0, s89, 0x11000
s_nop 0
buffer_load_dwordx4 v1, s[36:39], s78 offen sc1 lds
ds_read_b128 v[184:187], v4 offset:1024
ds_read_b128 v[188:191], v4 offset:3072
buffer_load_dwordx4 v1, s[36:39], s78 offen offset:1024 sc1 lds
ds_read_b128 v[160:163], v30 offset:49152
ds_read_b128 v[164:167], v30 offset:57344
buffer_load_dwordx4 v1, s[36:39], s78 offen offset:2048 sc1 lds
ds_read_b128 v[168:171], v30 offset:50176
ds_read_b128 v[172:175], v30 offset:58368
s_lshl_b32 s96, s83, 3
s_add_u32 s96, s96, s62
s_lshl_b32 s96, s96, 12
s_add_u32 s78, 0xe00000, s96
buffer_load_dwordx4 v[96:99], v1, s[36:39], s78 offen sc1
buffer_load_dwordx4 v[100:103], v1, s[36:39], s78 offen offset:1024 sc1
buffer_load_dwordx4 v[104:107], v1, s[36:39], s78 offen offset:2048 sc1
buffer_load_dwordx4 v[108:111], v1, s[36:39], s78 offen offset:3072 sc1
ds_read_b128 v[176:179], v30 offset:51200
ds_read_b128 v[180:183], v30 offset:59392
s_setprio 0
s_waitcnt lgkmcnt(13)
v_mfma_f32_16x16x32_bf16 v[192:195], v[128:131], v[152:155], v[192:195]
v_mfma_f32_16x16x32_bf16 v[216:219], v[128:131], v[156:159], v[216:219]
s_waitcnt lgkmcnt(12)
v_mfma_f32_16x16x32_bf16 v[196:199], v[132:135], v[152:155], v[196:199]
v_mfma_f32_16x16x32_bf16 v[220:223], v[132:135], v[156:159], v[220:223]
s_waitcnt lgkmcnt(11)
v_mfma_f32_16x16x32_bf16 v[200:203], v[136:139], v[152:155], v[200:203]
v_mfma_f32_16x16x32_bf16 v[224:227], v[136:139], v[156:159], v[224:227]
s_waitcnt lgkmcnt(10)
v_mfma_f32_16x16x32_bf16 v[204:207], v[140:143], v[152:155], v[204:207]
v_mfma_f32_16x16x32_bf16 v[228:231], v[140:143], v[156:159], v[228:231]
s_waitcnt lgkmcnt(9)
v_mfma_f32_16x16x32_bf16 v[208:211], v[144:147], v[152:155], v[208:211]
v_mfma_f32_16x16x32_bf16 v[232:235], v[144:147], v[156:159], v[232:235]
s_waitcnt lgkmcnt(8)
v_mfma_f32_16x16x32_bf16 v[212:215], v[148:151], v[152:155], v[212:215]
v_mfma_f32_16x16x32_bf16 v[236:239], v[148:151], v[156:159], v[236:239]
s_waitcnt lgkmcnt(5)
v_mfma_f32_16x16x32_bf16 v[192:195], v[160:163], v[184:187], v[192:195]
v_mfma_f32_16x16x32_bf16 v[216:219], v[160:163], v[188:191], v[216:219]
s_waitcnt lgkmcnt(4)
v_mfma_f32_16x16x32_bf16 v[196:199], v[164:167], v[184:187], v[196:199]
v_mfma_f32_16x16x32_bf16 v[220:223], v[164:167], v[188:191], v[220:223]
s_waitcnt lgkmcnt(3)
v_mfma_f32_16x16x32_bf16 v[200:203], v[168:171], v[184:187], v[200:203]
v_mfma_f32_16x16x32_bf16 v[224:227], v[168:171], v[188:191], v[224:227]
s_waitcnt lgkmcnt(2)
v_mfma_f32_16x16x32_bf16 v[204:207], v[172:175], v[184:187], v[204:207]
v_mfma_f32_16x16x32_bf16 v[228:231], v[172:175], v[188:191], v[228:231]
s_waitcnt lgkmcnt(1)
v_mfma_f32_16x16x32_bf16 v[208:211], v[176:179], v[184:187], v[208:211]
v_mfma_f32_16x16x32_bf16 v[232:235], v[176:179], v[188:191], v[232:235]
s_waitcnt lgkmcnt(0)
v_mfma_f32_16x16x32_bf16 v[212:215], v[180:183], v[184:187], v[212:215]
v_mfma_f32_16x16x32_bf16 v[236:239], v[180:183], v[188:191], v[236:239]
s_setprio 3
s_waitcnt vmcnt(13)
s_barrier
s_add_u32 s78, s31, 0x2000
buffer_load_dwordx4 v[68:71], v1, s[36:39], s78 offen offset:2048 sc1
ds_read_b128 v[152:155], v31 offset:32768
ds_read_b128 v[156:159], v31 offset:34816
s_add_u32 s78, s31, 0x2000
buffer_load_dwordx4 v[40:43], v1, s[36:39], s78 offen offset:3072 sc1
ds_read_b128 v[128:131], v29
ds_read_b128 v[132:135], v29 offset:8192
s_add_u32 s78, s31, 0x3000
buffer_load_dwordx4 v[88:91], v1, s[36:39], s78 offen sc1
ds_read_b128 v[136:139], v29 offset:1024
ds_read_b128 v[140:143], v29 offset:9216
s_add_u32 s78, s31, 0x3000
buffer_load_dwordx4 v[92:95], v1, s[36:39], s78 offen offset:1024 sc1
ds_read_b128 v[144:147], v29 offset:2048
ds_read_b128 v[148:151], v29 offset:10240
s_add_u32 s78, s31, 0x3000
buffer_load_dwordx4 v[44:47], v1, s[36:39], s78 offen offset:2048 sc1
ds_read_b128 v[184:187], v31 offset:33792
ds_read_b128 v[188:191], v31 offset:35840
s_add_u32 s78, s31, 0x3000
buffer_load_dwordx4 v[48:51], v1, s[36:39], s78 offen offset:3072 sc1
ds_read_b128 v[160:163], v29 offset:16384
ds_read_b128 v[164:167], v29 offset:24576
s_add_u32 s78, s31, 0x4000
buffer_load_dwordx4 v[112:115], v1, s[36:39], s78 offen sc1
ds_read_b128 v[168:171], v29 offset:17408
ds_read_b128 v[172:175], v29 offset:25600
ds_read_b128 v[176:179], v29 offset:18432
ds_read_b128 v[180:183], v29 offset:26624
s_setprio 0
s_waitcnt lgkmcnt(13)
v_mfma_f32_16x16x32_bf16 v[192:195], v[128:131], v[152:155], v[192:195]
v_mfma_f32_16x16x32_bf16 v[216:219], v[128:131], v[156:159], v[216:219]
s_waitcnt lgkmcnt(12)
v_mfma_f32_16x16x32_bf16 v[196:199], v[132:135], v[152:155], v[196:199]
v_mfma_f32_16x16x32_bf16 v[220:223], v[132:135], v[156:159], v[220:223]
s_waitcnt lgkmcnt(11)
v_mfma_f32_16x16x32_bf16 v[200:203], v[136:139], v[152:155], v[200:203]
v_mfma_f32_16x16x32_bf16 v[224:227], v[136:139], v[156:159], v[224:227]
s_waitcnt vmcnt(10)
v_cmp_ne_u32 vcc, s60, v98
s_waitcnt vmcnt(9)
v_cmp_ne_u32 s[100:101], s60, v102
s_or_b64 vcc, vcc, s[100:101]
s_waitcnt vmcnt(8)
v_cmp_ne_u32 s[100:101], s60, v106
s_or_b64 vcc, vcc, s[100:101]
s_waitcnt vmcnt(7)
v_cmp_ne_u32 s[100:101], s60, v110
s_or_b64 vcc, vcc, s[100:101]
s_cbranch_scc1 .Lfk_rare136
.Lfk_join135:
s_waitcnt lgkmcnt(10)
v_mfma_f32_16x16x32_bf16 v[204:207], v[140:143], v[152:155], v[204:207]
v_add_f32 v96, v96, v100
v_add_f32 v97, v97, v101
v_mfma_f32_16x16x32_bf16 v[228:231], v[140:143], v[156:159], v[228:231]
v_add_f32 v96, v96, v104
s_waitcnt lgkmcnt(9)
v_mfma_f32_16x16x32_bf16 v[208:211], v[144:147], v[152:155], v[208:211]
v_add_f32 v97, v97, v105
v_add_f32 v96, v96, v108
v_mfma_f32_16x16x32_bf16 v[232:235], v[144:147], v[156:159], v[232:235]
v_add_f32 v97, v97, v109
v_mov_b32 v98, v96
s_waitcnt lgkmcnt(8)
v_mfma_f32_16x16x32_bf16 v[212:215], v[148:151], v[152:155], v[212:215]
v_mov_b32 v99, v97
v_mfma_f32_16x16x32_bf16 v[236:239], v[148:151], v[156:159], v[236:239]
v_permlane32_swap_b32 v96, v98
s_waitcnt lgkmcnt(5)
v_mfma_f32_16x16x32_bf16 v[192:195], v[160:163], v[184:187], v[192:195]
v_permlane32_swap_b32 v97, v99
v_add_f32 v96, v96, v98
v_mfma_f32_16x16x32_bf16 v[216:219], v[160:163], v[188:191], v[216:219]
v_add_f32 v97, v97, v99
v_mov_b32 v98, v96
s_waitcnt lgkmcnt(4)
v_mfma_f32_16x16x32_bf16 v[196:199], v[164:167], v[184:187], v[196:199]
v_mov_b32 v99, v97
v_mfma_f32_16x16x32_bf16 v[220:223], v[164:167], v[188:191], v[220:223]
v_permlane16_swap_b32 v96, v98
s_waitcnt lgkmcnt(3)
v_mfma_f32_16x16x32_bf16 v[200:203], v[168:171], v[184:187], v[200:203]
v_permlane16_swap_b32 v97, v99
v_add_f32 v96, v96, v98
v_mfma_f32_16x16x32_bf16 v[224:227], v[168:171], v[188:191], v[224:227]
v_add_f32 v97, v97, v99
s_waitcnt lgkmcnt(2)
v_mfma_f32_16x16x32_bf16 v[204:207], v[172:175], v[184:187], v[204:207]
v_mul_f32 v96, 0xbb000000, v96
v_mul_f32 v97, 0x3b000000, v97
v_mfma_f32_16x16x32_bf16 v[228:231], v[172:175], v[188:191], v[228:231]
v_fma_f32 v97, -v96, v96, v97
v_max_f32 v97, 0, v97
s_waitcnt lgkmcnt(1)
v_mfma_f32_16x16x32_bf16 v[208:211], v[176:179], v[184:187], v[208:211]
v_add_f32 v97, 0x3727c5ac, v97
v_rsq_f32 v98, v97
v_mfma_f32_16x16x32_bf16 v[232:235], v[176:179], v[188:191], v[232:235]
v_sqrt_f32 v97, v97
s_waitcnt lgkmcnt(0)
v_mfma_f32_16x16x32_bf16 v[212:215], v[180:183], v[184:187], v[212:215]
v_lshlrev_b32 v12, 1, v3
s_lshl_b32 s96, s62, 8
v_add_u32 v12, s96, v12
v_add_u32 v100, 0x27800, v12
v_mul_f32 v98, 0xbfb8aa3b, v98
v_mfma_f32_16x16x32_bf16 v[236:239], v[180:183], v[188:191], v[236:239]
ds_write_b128 v100, v[96:99]
s_setprio 3
s_waitcnt lgkmcnt(0)
s_barrier
s_setprio 2
ds_read_b128 v[152:155], v31 offset:49152
ds_read_b128 v[156:159], v31 offset:51200
ds_read_b128 v[128:131], v30
ds_read_b128 v[132:135], v30 offset:8192
ds_read_b128 v[136:139], v30 offset:1024
ds_read_b128 v[140:143], v30 offset:9216
ds_read_b128 v[144:147], v30 offset:2048
ds_read_b128 v[148:151], v30 offset:10240
ds_read_b128 v[184:187], v31 offset:50176
ds_read_b128 v[188:191], v31 offset:52224
ds_read_b128 v[160:163], v30 offset:16384
ds_read_b128 v[164:167], v30 offset:24576
ds_read_b128 v[168:171], v30 offset:17408
ds_read_b128 v[172:175], v30 offset:25600
ds_read_b128 v[176:179], v30 offset:18432
ds_read_b128 v[180:183], v30 offset:26624
s_add_u32 s78, s31, 0x4000
buffer_load_dwordx4 v[116:119], v1, s[36:39], s78 offen offset:1024 sc1
s_add_u32 s78, s31, 0x4000
buffer_load_dwordx4 v[120:123], v1, s[36:39], s78 offen offset:2048 sc1
s_add_u32 s78, s31, 0x4000
buffer_load_dwordx4 v[124:127], v1, s[36:39], s78 offen offset:3072 sc1
s_waitcnt lgkmcnt(13)
v_mfma_f32_16x16x32_bf16 v[192:195], v[128:131], v[152:155], v[192:195]
v_mfma_f32_16x16x32_bf16 v[216:219], v[128:131], v[156:159], v[216:219]
s_waitcnt lgkmcnt(12)
v_mfma_f32_16x16x32_bf16 v[196:199], v[132:135], v[152:155], v[196:199]
v_mfma_f32_16x16x32_bf16 v[220:223], v[132:135], v[156:159], v[220:223]
s_waitcnt lgkmcnt(11)
v_mfma_f32_16x16x32_bf16 v[200:203], v[136:139], v[152:155], v[200:203]
v_mfma_f32_16x16x32_bf16 v[224:227], v[136:139], v[156:159], v[224:227]
s_waitcnt lgkmcnt(10)
v_mfma_f32_16x16x32_bf16 v[204:207], v[140:143], v[152:155], v[204:207]
v_mfma_f32_16x16x32_bf16 v[228:231], v[140:143], v[156:159], v[228:231]
s_waitcnt lgkmcnt(9)
v_mfma_f32_16x16x32_bf16 v[208:211], v[144:147], v[152:155], v[208:211]
v_mfma_f32_16x16x32_bf16 v[232:235], v[144:147], v[156:159], v[232:235]
s_waitcnt lgkmcnt(8)
v_mfma_f32_16x16x32_bf16 v[212:215], v[148:151], v[152:155], v[212:215]
v_mfma_f32_16x16x32_bf16 v[236:239], v[148:151], v[156:159], v[236:239]
s_waitcnt lgkmcnt(5)
v_mfma_f32_16x16x32_bf16 v[192:195], v[160:163], v[184:187], v[192:195]
ds_read_b128 v[152:155], v21
v_mfma_f32_16x16x32_bf16 v[216:219], v[160:163], v[188:191], v[216:219]
ds_read_b128 v[128:131], v20
s_waitcnt lgkmcnt(6)
v_mfma_f32_16x16x32_bf16 v[196:199], v[164:167], v[184:187], v[196:199]
ds_read_b128 v[132:135], v20 offset:16
v_mfma_f32_16x16x32_bf16 v[220:223], v[164:167], v[188:191], v[220:223]
ds_read_b128 v[136:139], v20 offset:128
s_waitcnt lgkmcnt(7)
v_mfma_f32_16x16x32_bf16 v[200:203], v[168:171], v[184:187], v[200:203]
ds_read_b128 v[140:143], v20 offset:144
v_mfma_f32_16x16x32_bf16 v[224:227], v[168:171], v[188:191], v[224:227]
ds_read_b128 v[144:147], v20 offset:256
s_waitcnt lgkmcnt(8)
v_mfma_f32_16x16x32_bf16 v[204:207], v[172:175], v[184:187], v[204:207]
ds_read_b128 v[148:151], v20 offset:272
v_mfma_f32_16x16x32_bf16 v[228:231], v[172:175], v[188:191], v[228:231]
ds_read_b128 v[156:159], v21 offset:256
s_waitcnt lgkmcnt(9)
v_mfma_f32_16x16x32_bf16 v[208:211], v[176:179], v[184:187], v[208:211]
v_mfma_f32_16x16x32_bf16 v[232:235], v[176:179], v[188:191], v[232:235]
s_waitcnt lgkmcnt(8)
v_mfma_f32_16x16x32_bf16 v[212:215], v[180:183], v[184:187], v[212:215]
v_mfma_f32_16x16x32_bf16 v[236:239], v[180:183], v[188:191], v[236:239]
s_setprio 2
s_waitcnt lgkmcnt(7)
v_mov_b32 v12, v153
s_waitcnt lgkmcnt(6)
v_pk_fma_f32 v[192:193], v[152:153], v[128:129], v[192:193] op_sel_hi:[0,1,1]
v_pk_fma_f32 v[194:195], v[152:153], v[130:131], v[194:195] op_sel_hi:[0,1,1]
v_pk_mul_f32 v[192:193], v[192:193], v[154:155] op_sel_hi:[1,0]
v_pk_mul_f32 v[194:195], v[194:195], v[154:155] op_sel_hi:[1,0]
s_waitcnt lgkmcnt(5)
v_pk_fma_f32 v[196:197], v[152:153], v[132:133], v[196:197] op_sel_hi:[0,1,1]
v_pk_fma_f32 v[198:199], v[152:153], v[134:135], v[198:199] op_sel_hi:[0,1,1]
v_exp_f32 v192, v192
v_exp_f32 v193, v193
v_exp_f32 v194, v194
v_exp_f32 v195, v195
v_pk_fma_f32 v[192:193], v[192:193], v[12:13], v[12:13] op_sel_hi:[1,0,0]
v_pk_fma_f32 v[194:195], v[194:195], v[12:13], v[12:13] op_sel_hi:[1,0,0]
v_rcp_f32 v192, v192
v_rcp_f32 v193, v193
v_rcp_f32 v194, v194
v_rcp_f32 v195, v195
v_pk_mul_f32 v[192:193], v[192:193], v[196:197]
v_pk_mul_f32 v[194:195], v[194:195], v[198:199]
s_add_u32 s78, s31, 0x5000
buffer_load_dwordx4 v[96:99], v1, s[36:39], s78 offen sc1
v_cvt_pk_bf16_f32 v168, v192, v193
v_cvt_pk_bf16_f32 v169, v194, v195
s_waitcnt lgkmcnt(4)
v_pk_fma_f32 v[200:201], v[152:153], v[136:137], v[200:201] op_sel_hi:[0,1,1]
v_pk_fma_f32 v[202:203], v[152:153], v[138:139], v[202:203] op_sel_hi:[0,1,1]
v_pk_mul_f32 v[200:201], v[200:201], v[154:155] op_sel_hi:[1,0]
v_pk_mul_f32 v[202:203], v[202:203], v[154:155] op_sel_hi:[1,0]
s_waitcnt lgkmcnt(3)
v_pk_fma_f32 v[204:205], v[152:153], v[140:141], v[204:205] op_sel_hi:[0,1,1]
v_pk_fma_f32 v[206:207], v[152:153], v[142:143], v[206:207] op_sel_hi:[0,1,1]
v_exp_f32 v200, v200
v_exp_f32 v201, v201
v_exp_f32 v202, v202
v_exp_f32 v203, v203
v_pk_fma_f32 v[200:201], v[200:201], v[12:13], v[12:13] op_sel_hi:[1,0,0]
v_pk_fma_f32 v[202:203], v[202:203], v[12:13], v[12:13] op_sel_hi:[1,0,0]
v_rcp_f32 v200, v200
v_rcp_f32 v201, v201
v_rcp_f32 v202, v202
v_rcp_f32 v203, v203
v_pk_mul_f32 v[200:201], v[200:201], v[204:205]
v_pk_mul_f32 v[202:203], v[202:203], v[206:207]
s_add_u32 s78, s31, 0x5000
buffer_load_dwordx4 v[100:103], v1, s[36:39], s78 offen offset:1024 sc1
v_cvt_pk_bf16_f32 v170, v200, v201
v_cvt_pk_bf16_f32 v171, v202, v203
buffer_store_dwordx4 v[168:171], v1, s[36:39], s32 offen sc1
s_add_u32 s32, s32, 0xe000
s_waitcnt lgkmcnt(2)
v_pk_fma_f32 v[208:209], v[152:153], v[144:145], v[208:209] op_sel_hi:[0,1,1]
v_pk_fma_f32 v[210:211], v[152:153], v[146:147], v[210:211] op_sel_hi:[0,1,1]
v_pk_mul_f32 v[208:209], v[208:209], v[154:155] op_sel_hi:[1,0]
v_pk_mul_f32 v[210:211], v[210:211], v[154:155] op_sel_hi:[1,0]
s_waitcnt lgkmcnt(1)
v_pk_fma_f32 v[212:213], v[152:153], v[148:149], v[212:213] op_sel_hi:[0,1,1]
v_pk_fma_f32 v[214:215], v[152:153], v[150:151], v[214:215] op_sel_hi:[0,1,1]
v_exp_f32 v208, v208
v_exp_f32 v209, v209
v_exp_f32 v210, v210
v_exp_f32 v211, v211
v_pk_fma_f32 v[208:209], v[208:209], v[12:13], v[12:13] op_sel_hi:[1,0,0]
v_pk_fma_f32 v[210:211], v[210:211], v[12:13], v[12:13] op_sel_hi:[1,0,0]
v_rcp_f32 v208, v208
v_rcp_f32 v209, v209
v_rcp_f32 v210, v210
v_rcp_f32 v211, v211
v_pk_mul_f32 v[208:209], v[208:209], v[212:213]
v_pk_mul_f32 v[210:211], v[210:211], v[214:215]
s_add_u32 s78, s31, 0x5000
buffer_load_dwordx4 v[104:107], v1, s[36:39], s78 offen offset:2048 sc1
v_cvt_pk_bf16_f32 v184, v208, v209
v_cvt_pk_bf16_f32 v185, v210, v211
s_waitcnt lgkmcnt(0)
v_mov_b32 v12, v157
v_pk_fma_f32 v[216:217], v[156:157], v[128:129], v[216:217] op_sel_hi:[0,1,1]
v_pk_fma_f32 v[218:219], v[156:157], v[130:131], v[218:219] op_sel_hi:[0,1,1]
v_pk_mul_f32 v[216:217], v[216:217], v[158:159] op_sel_hi:[1,0]
v_pk_mul_f32 v[218:219], v[218:219], v[158:159] op_sel_hi:[1,0]
v_pk_fma_f32 v[220:221], v[156:157], v[132:133], v[220:221] op_sel_hi:[0,1,1]
v_pk_fma_f32 v[222:223], v[156:157], v[134:135], v[222:223] op_sel_hi:[0,1,1]
v_exp_f32 v216, v216
v_exp_f32 v217, v217
v_exp_f32 v218, v218
v_exp_f32 v219, v219
v_pk_fma_f32 v[216:217], v[216:217], v[12:13], v[12:13] op_sel_hi:[1,0,0]
v_pk_fma_f32 v[218:219], v[218:219], v[12:13], v[12:13] op_sel_hi:[1,0,0]
v_rcp_f32 v216, v216
v_rcp_f32 v217, v217
v_rcp_f32 v218, v218
v_rcp_f32 v219, v219
v_pk_mul_f32 v[216:217], v[216:217], v[220:221]
v_pk_mul_f32 v[218:219], v[218:219], v[222:223]
v_cvt_pk_bf16_f32 v174, v216, v217
v_cvt_pk_bf16_f32 v175, v218, v219
v_pk_fma_f32 v[224:225], v[156:157], v[136:137], v[224:225] op_sel_hi:[0,1,1]
v_pk_fma_f32 v[226:227], v[156:157], v[138:139], v[226:227] op_sel_hi:[0,1,1]
v_pk_mul_f32 v[224:225], v[224:225], v[158:159] op_sel_hi:[1,0]
v_pk_mul_f32 v[226:227], v[226:227], v[158:159] op_sel_hi:[1,0]
v_pk_fma_f32 v[228:229], v[156:157], v[140:141], v[228:229] op_sel_hi:[0,1,1]
v_pk_fma_f32 v[230:231], v[156:157], v[142:143], v[230:231] op_sel_hi:[0,1,1]
v_exp_f32 v224, v224
v_exp_f32 v225, v225
v_exp_f32 v226, v226
v_exp_f32 v227, v227
v_pk_fma_f32 v[224:225], v[224:225], v[12:13], v[12:13] op_sel_hi:[1,0,0]
v_pk_fma_f32 v[226:227], v[226:227], v[12:13], v[12:13] op_sel_hi:[1,0,0]
v_rcp_f32 v224, v224
v_rcp_f32 v225, v225
v_rcp_f32 v226, v226
v_rcp_f32 v227, v227
v_pk_mul_f32 v[224:225], v[224:225], v[228:229]
v_pk_mul_f32 v[226:227], v[226:227], v[230:231]
v_cvt_pk_bf16_f32 v176, v224, v225
v_cvt_pk_bf16_f32 v177, v226, v227
buffer_store_dwordx4 v[174:177], v1, s[36:39], s32 offen sc1
v_pk_fma_f32 v[232:233], v[156:157], v[144:145], v[232:233] op_sel_hi:[0,1,1]
v_pk_fma_f32 v[234:235], v[156:157], v[146:147], v[234:235] op_sel_hi:[0,1,1]
v_pk_mul_f32 v[232:233], v[232:233], v[158:159] op_sel_hi:[1,0]
v_pk_mul_f32 v[234:235], v[234:235], v[158:159] op_sel_hi:[1,0]
v_pk_fma_f32 v[236:237], v[156:157], v[148:149], v[236:237] op_sel_hi:[0,1,1]
v_pk_fma_f32 v[238:239], v[156:157], v[150:151], v[238:239] op_sel_hi:[0,1,1]
v_exp_f32 v232, v232
v_exp_f32 v233, v233
v_exp_f32 v234, v234
v_exp_f32 v235, v235
v_pk_fma_f32 v[232:233], v[232:233], v[12:13], v[12:13] op_sel_hi:[1,0,0]
v_pk_fma_f32 v[234:235], v[234:235], v[12:13], v[12:13] op_sel_hi:[1,0,0]
v_rcp_f32 v232, v232
v_rcp_f32 v233, v233
v_rcp_f32 v234, v234
v_rcp_f32 v235, v235
v_pk_mul_f32 v[232:233], v[232:233], v[236:237]
v_pk_mul_f32 v[234:235], v[234:235], v[238:239]
v_cvt_pk_bf16_f32 v186, v232, v233
v_cvt_pk_bf16_f32 v187, v234, v235
buffer_store_dwordx4 v[184:187], v1, s[36:39], s79 offen sc1
s_and_b32 s96, s2, 2
s_lshl_b32 s96, s96, 1
s_lshr_b32 s97, s64, 3
s_add_u32 s81, s96, s97
s_lshl_b32 s96, s83, 3
s_and_b32 s97, s63, 1
s_lshl_b32 s97, s97, 2
s_add_u32 s34, s96, s97
s_lshl_b32 s96, s34, 3
s_add_u32 s96, s96, s62
s_mul_i32 s96, s96, 0x1c00
s_add_u32 s70, 0x700000, s96
s_add_u32 s71, s70, 0xe000
s_add_u32 s72, s70, 0x1c000
s_add_u32 s73, s70, 0x2a000
s_lshl_b32 s96, s34, 14
s_lshl_b32 s97, s81, 7
s_add_u32 s34, s96, s97
v_lshrrev_b32 v12, 3, v0
v_and_b32 v12, 1, v12
v_lshlrev_b32 v12, 15, v12
v_lshrrev_b32 v13, 1, v0
v_and_b32 v13, 3, v13
v_lshlrev_b32 v13, 8, v13
v_add_u32 v12, v12, v13
s_and_b32 s96, s62, 3
s_lshl_b32 s96, s96, 2
v_lshrrev_b32 v13, 4, v0
v_add_u32 v13, s96, v13
v_lshlrev_b32 v13, 4, v13
v_add_u32 v12, v12, v13
v_and_b32 v13, 1, v0
v_lshlrev_b32 v13, 3, v13
v_add_u32 v7, v12, v13
s_lshr_b32 s35, s62, 2
s_lshl_b32 s96, s83, 2
s_and_b32 s97, s81, 1
s_lshl_b32 s97, s97, 1
s_add_u32 s96, s96, s97
s_lshl_b32 s96, s96, 3
s_and_b32 s97, s63, 1
s_lshl_b32 s97, s97, 2
s_add_u32 s96, s96, s97
s_mul_i32 s97, s35, 3
s_add_u32 s96, s96, s97
s_lshl_b32 s96, s96, 12
s_add_u32 s96, 0x500000, s96
s_lshr_b32 s97, s81, 1
s_lshl_b32 s97, s97, 10
s_add_u32 s78, s96, s97
buffer_load_dwordx2 v[24:25], v7, s[36:39], s78 offen sc1
s_and_b32 s96, s62, 3
s_lshl_b32 s96, s96, 2
v_lshrrev_b32 v12, 4, v0
v_add_u32 v12, s96, v12
v_lshlrev_b32 v12, 10, v12
v_and_b32 v13, 15, v0
v_lshlrev_b32 v13, 3, v13
v_add_u32 v7, v12, v13
v_and_b32 v12, 15, v0
v_mul_u32_u24 v12, 0x110, v12
v_lshrrev_b32 v13, 4, v0
v_lshlrev_b32 v13, 4, v13
v_add_u32 v12, v12, v13
s_mul_i32 s96, s62, 0x2200
v_add_u32 v5, s96, v12
s_and_b32 s96, s62, 3
s_lshl_b32 s96, s96, 2
v_lshrrev_b32 v12, 4, v0
v_add_u32 v12, s96, v12
v_mul_u32_u24 v12, 0x110, v12
v_and_b32 v13, 15, v0
v_lshlrev_b32 v13, 4, v13
v_add_u32 v12, v12, v13
s_mul_i32 s96, s35, 0x12100
v_add_u32 v6, s96, v12
s_lshl_b32 s96, s83, 4
s_add_u32 s96, s96, s82
s_lshl_b32 s96, s96, 3
s_add_u32 s96, s96, s62
s_lshl_b32 s96, s96, 7
s_add_u32 s79, 0x40000, s96
s_waitcnt vmcnt(1)
s_mov_b64 exec, 1
buffer_store_dwordx2 v[10:11], off, s[36:39], s79 sc1
s_mov_b64 exec, -1
s_add_u32 s78, s31, 0x5000
buffer_load_dwordx4 v[108:111], v1, s[36:39], s78 offen offset:3072 sc1
s_add_u32 s78, s31, 0x6000
buffer_load_dwordx4 v[240:243], v1, s[36:39], s78 offen sc1
s_add_u32 s78, s31, 0x6000
buffer_load_dwordx4 v[244:247], v1, s[36:39], s78 offen offset:1024 sc1
s_add_u32 s78, s31, 0x6000
buffer_load_dwordx4 v[248:251], v1, s[36:39], s78 offen offset:2048 sc1
s_add_u32 s78, s31, 0x6000
buffer_load_dwordx4 v[252:255], v1, s[36:39], s78 offen offset:3072 sc1
s_barrier
s_sleep 10
.Lfk_end108:
v_lshrrev_b32 v12, 2, v0
v_and_b32 v12, 1, v12
v_lshlrev_b32 v12, 10, v12
v_and_b32 v13, 2, v0
v_lshlrev_b32 v13, 8, v13
v_add_u32 v12, v12, v13
v_and_b32 v13, 1, v0
v_lshlrev_b32 v13, 7, v13
v_add_u32 v4, v12, v13
s_lshl_b32 s96, s83, 4
s_lshl_b32 s97, s62, 1
s_add_u32 s96, s96, s97
s_lshl_b32 s96, s96, 3
s_and_b32 s97, s63, 1
s_lshl_b32 s97, s97, 1
s_add_u32 s96, s96, s97
s_lshl_b32 s96, s96, 7
s_add_u32 s79, 0x40000, s96
s_mov_b32 s84, 0x10000
.Lfk_poll139:
buffer_load_dwordx2 v[12:13], v4, s[36:39], s79 offen sc1
s_waitcnt vmcnt(0)
v_cmp_ne_u64 vcc, s[60:61], v[12:13]
s_cmp_lg_u64 vcc, 0
s_cbranch_scc0 .Lfk_done140
s_sub_u32 s84, s84, 1
s_cbranch_scc1 .Lfk_done140
s_sleep 4
s_branch .Lfk_poll139
.Lfk_done140:
s_barrier
s_cmp_eq_u32 s35, 0
s_cbranch_scc0 .Lfk_else141
s_setprio 3
buffer_load_dwordx4 v[128:131], v1, s[36:39], s70 offen sc1
buffer_load_dwordx4 v[132:135], v1, s[36:39], s70 offen offset:1024 sc1
buffer_load_dwordx4 v[136:139], v1, s[36:39], s70 offen offset:2048 sc1
buffer_load_dwordx4 v[140:143], v1, s[36:39], s70 offen offset:3072 sc1
s_add_u32 s78, s70, 0x1000
buffer_load_dwordx4 v[144:147], v1, s[36:39], s78 offen sc1
buffer_load_dwordx4 v[148:151], v1, s[36:39], s78 offen offset:1024 sc1
buffer_load_dwordx4 v[152:155], v1, s[36:39], s78 offen offset:2048 sc1
s_add_u32 s78, s71, 0x1000
buffer_load_dwordx4 v[180:183], v1, s[36:39], s78 offen offset:2048 sc1
buffer_load_dwordx4 v[156:159], v1, s[36:39], s71 offen sc1
buffer_load_dwordx4 v[160:163], v1, s[36:39], s71 offen offset:1024 sc1
buffer_load_dwordx4 v[164:167], v1, s[36:39], s71 offen offset:2048 sc1
buffer_load_dwordx4 v[168:171], v1, s[36:39], s71 offen offset:3072 sc1
s_add_u32 s78, s71, 0x1000
buffer_load_dwordx4 v[172:175], v1, s[36:39], s78 offen sc1
buffer_load_dwordx4 v[176:179], v1, s[36:39], s78 offen offset:1024 sc1
s_waitcnt vmcnt(13)
v_mfma_f32_16x16x32_bf16 v[8:11], v[32:35], v[128:131], 0
buffer_load_dwordx4 v[184:187], v1, s[36:39], s72 offen sc1
v_mfma_f32_16x16x32_bf16 v[16:19], v[36:39], v[128:131], 0
v_mfma_f32_16x16x32_bf16 v[20:23], v[56:59], v[128:131], 0
v_mfma_f32_16x16x32_bf16 v[28:31], v[60:63], v[128:131], 0
s_waitcnt vmcnt(13)
v_mfma_f32_16x16x32_bf16 v[8:11], v[72:75], v[132:135], v[8:11]
buffer_load_dwordx4 v[188:191], v1, s[36:39], s72 offen offset:1024 sc1
v_mfma_f32_16x16x32_bf16 v[16:19], v[76:79], v[132:135], v[16:19]
v_mfma_f32_16x16x32_bf16 v[20:23], v[80:83], v[132:135], v[20:23]
v_mfma_f32_16x16x32_bf16 v[28:31], v[84:87], v[132:135], v[28:31]
buffer_load_dwordx4 v[192:195], v1, s[36:39], s72 offen offset:2048 sc1
s_waitcnt vmcnt(14)
v_mfma_f32_16x16x32_bf16 v[8:11], v[52:55], v[136:139], v[8:11]
v_mfma_f32_16x16x32_bf16 v[16:19], v[64:67], v[136:139], v[16:19]
v_mfma_f32_16x16x32_bf16 v[20:23], v[68:71], v[136:139], v[20:23]
v_mfma_f32_16x16x32_bf16 v[28:31], v[40:43], v[136:139], v[28:31]
buffer_load_dwordx4 v[196:199], v1, s[36:39], s72 offen offset:3072 sc1
s_waitcnt vmcnt(14)
v_mfma_f32_16x16x32_bf16 v[8:11], v[88:91], v[140:143], v[8:11]
v_mfma_f32_16x16x32_bf16 v[16:19], v[92:95], v[140:143], v[16:19]
v_mfma_f32_16x16x32_bf16 v[20:23], v[44:47], v[140:143], v[20:23]
s_add_u32 s78, s72, 0x1000
buffer_load_dwordx4 v[200:203], v1, s[36:39], s78 offen sc1
v_mfma_f32_16x16x32_bf16 v[28:31], v[48:51], v[140:143], v[28:31]
s_waitcnt vmcnt(14)
v_mfma_f32_16x16x32_bf16 v[8:11], v[112:115], v[144:147], v[8:11]
v_mfma_f32_16x16x32_bf16 v[16:19], v[116:119], v[144:147], v[16:19]
v_mfma_f32_16x16x32_bf16 v[20:23], v[120:123], v[144:147], v[20:23]
buffer_load_dwordx4 v[204:207], v1, s[36:39], s78 offen offset:1024 sc1
v_mfma_f32_16x16x32_bf16 v[28:31], v[124:127], v[144:147], v[28:31]
s_waitcnt vmcnt(14)
v_mfma_f32_16x16x32_bf16 v[8:11], v[96:99], v[148:151], v[8:11]
v_mfma_f32_16x16x32_bf16 v[16:19], v[100:103], v[148:151], v[16:19]
buffer_load_dwordx4 v[208:211], v1, s[36:39], s78 offen offset:2048 sc1
s_waitcnt vmcnt(13)
v_swap_b32 v154, v180
v_swap_b32 v155, v181
v_mfma_f32_16x16x32_bf16 v[20:23], v[104:107], v[148:151], v[20:23]
v_mfma_f32_16x16x32_bf16 v[28:31], v[108:111], v[148:151], v[28:31]
v_mfma_f32_16x16x32_bf16 v[8:11], v[240:243], v[152:155], v[8:11]
v_mfma_f32_16x16x32_bf16 v[16:19], v[244:247], v[152:155], v[16:19]
s_add_u32 s78, s73, 0x1000
buffer_load_dwordx4 v[236:239], v1, s[36:39], s78 offen offset:2048 sc1
v_mfma_f32_16x16x32_bf16 v[20:23], v[248:251], v[152:155], v[20:23]
v_mfma_f32_16x16x32_bf16 v[28:31], v[252:255], v[152:155], v[28:31]
s_waitcnt vmcnt(13)
v_mfma_f32_16x16x32_bf16 v[132:135], v[32:35], v[156:159], 0
buffer_load_dwordx4 v[212:215], v1, s[36:39], s73 offen sc1
v_mfma_f32_16x16x32_bf16 v[136:139], v[36:39], v[156:159], 0
v_mfma_f32_16x16x32_bf16 v[140:143], v[56:59], v[156:159], 0
ds_write_b128 v5, v[8:11]
v_mfma_f32_16x16x32_bf16 v[144:147], v[60:63], v[156:159], 0
ds_write_b128 v5, v[16:19] offset:64
s_waitcnt vmcnt(13)
v_mfma_f32_16x16x32_bf16 v[132:135], v[72:75], v[160:163], v[132:135]
ds_write_b128 v5, v[20:23] offset:128
buffer_load_dwordx4 v[216:219], v1, s[36:39], s73 offen offset:1024 sc1
v_mfma_f32_16x16x32_bf16 v[136:139], v[76:79], v[160:163], v[136:139]
ds_write_b128 v5, v[28:31] offset:192
v_mfma_f32_16x16x32_bf16 v[140:143], v[80:83], v[160:163], v[140:143]
v_mfma_f32_16x16x32_bf16 v[144:147], v[84:87], v[160:163], v[144:147]
buffer_load_dwordx4 v[220:223], v1, s[36:39], s73 offen offset:2048 sc1
s_waitcnt vmcnt(14)
v_mfma_f32_16x16x32_bf16 v[132:135], v[52:55], v[164:167], v[132:135]
v_mfma_f32_16x16x32_bf16 v[136:139], v[64:67], v[164:167], v[136:139]
v_mfma_f32_16x16x32_bf16 v[140:143], v[68:71], v[164:167], v[140:143]
v_mfma_f32_16x16x32_bf16 v[144:147], v[40:43], v[164:167], v[144:147]
buffer_load_dwordx4 v[224:227], v1, s[36:39], s73 offen offset:3072 sc1
s_waitcnt vmcnt(14)
v_mfma_f32_16x16x32_bf16 v[132:135], v[88:91], v[168:171], v[132:135]
v_mfma_f32_16x16x32_bf16 v[136:139], v[92:95], v[168:171], v[136:139]
v_mfma_f32_16x16x32_bf16 v[140:143], v[44:47], v[168:171], v[140:143]
s_add_u32 s78, s73, 0x1000
buffer_load_dwordx4 v[228:231], v1, s[36:39], s78 offen sc1
v_mfma_f32_16x16x32_bf16 v[144:147], v[48:51], v[168:171], v[144:147]
s_waitcnt vmcnt(14)
v_mfma_f32_16x16x32_bf16 v[132:135], v[112:115], v[172:175], v[132:135]
v_mfma_f32_16x16x32_bf16 v[136:139], v[116:119], v[172:175], v[136:139]
v_mfma_f32_16x16x32_bf16 v[140:143], v[120:123], v[172:175], v[140:143]
buffer_load_dwordx4 v[232:235], v1, s[36:39], s78 offen offset:1024 sc1
v_mfma_f32_16x16x32_bf16 v[144:147], v[124:127], v[172:175], v[144:147]
s_waitcnt vmcnt(14)
v_mfma_f32_16x16x32_bf16 v[132:135], v[96:99], v[176:179], v[132:135]
v_mfma_f32_16x16x32_bf16 v[136:139], v[100:103], v[176:179], v[136:139]
v_mfma_f32_16x16x32_bf16 v[140:143], v[104:107], v[176:179], v[140:143]
v_mfma_f32_16x16x32_bf16 v[144:147], v[108:111], v[176:179], v[144:147]
v_mfma_f32_16x16x32_bf16 v[132:135], v[240:243], v[180:183], v[132:135]
v_mfma_f32_16x16x32_bf16 v[136:139], v[244:247], v[180:183], v[136:139]
v_mfma_f32_16x16x32_bf16 v[140:143], v[248:251], v[180:183], v[140:143]
v_mfma_f32_16x16x32_bf16 v[144:147], v[252:255], v[180:183], v[144:147]
s_waitcnt vmcnt(13)
v_mfma_f32_16x16x32_bf16 v[160:163], v[32:35], v[184:187], 0
v_mfma_f32_16x16x32_bf16 v[164:167], v[36:39], v[184:187], 0
v_mfma_f32_16x16x32_bf16 v[168:171], v[56:59], v[184:187], 0
s_nop 0
ds_write_b128 v5, v[132:135] offset:4352
v_mfma_f32_16x16x32_bf16 v[172:175], v[60:63], v[184:187], 0
ds_write_b128 v5, v[136:139] offset:4416
s_waitcnt vmcnt(12)
v_mfma_f32_16x16x32_bf16 v[160:163], v[72:75], v[188:191], v[160:163]
ds_write_b128 v5, v[140:143] offset:4480
v_mfma_f32_16x16x32_bf16 v[164:167], v[76:79], v[188:191], v[164:167]
ds_write_b128 v5, v[144:147] offset:4544
v_mfma_f32_16x16x32_bf16 v[168:171], v[80:83], v[188:191], v[168:171]
v_add_u32 v5, 69632, v5
v_mfma_f32_16x16x32_bf16 v[172:175], v[84:87], v[188:191], v[172:175]
s_waitcnt vmcnt(11)
v_mfma_f32_16x16x32_bf16 v[160:163], v[52:55], v[192:195], v[160:163]
v_mfma_f32_16x16x32_bf16 v[164:167], v[64:67], v[192:195], v[164:167]
v_mfma_f32_16x16x32_bf16 v[168:171], v[68:71], v[192:195], v[168:171]
v_mfma_f32_16x16x32_bf16 v[172:175], v[40:43], v[192:195], v[172:175]
s_waitcnt vmcnt(10)
v_mfma_f32_16x16x32_bf16 v[160:163], v[88:91], v[196:199], v[160:163]
v_mfma_f32_16x16x32_bf16 v[164:167], v[92:95], v[196:199], v[164:167]
v_mfma_f32_16x16x32_bf16 v[168:171], v[44:47], v[196:199], v[168:171]
v_mfma_f32_16x16x32_bf16 v[172:175], v[48:51], v[196:199], v[172:175]
s_waitcnt vmcnt(9)
v_mfma_f32_16x16x32_bf16 v[160:163], v[112:115], v[200:203], v[160:163]
v_mfma_f32_16x16x32_bf16 v[164:167], v[116:119], v[200:203], v[164:167]
v_mfma_f32_16x16x32_bf16 v[168:171], v[120:123], v[200:203], v[168:171]
v_mfma_f32_16x16x32_bf16 v[172:175], v[124:127], v[200:203], v[172:175]
s_waitcnt vmcnt(8)
v_mfma_f32_16x16x32_bf16 v[160:163], v[96:99], v[204:207], v[160:163]
v_mfma_f32_16x16x32_bf16 v[164:167], v[100:103], v[204:207], v[164:167]
s_waitcnt vmcnt(6)
v_swap_b32 v210, v236
v_swap_b32 v211, v237
v_mfma_f32_16x16x32_bf16 v[168:171], v[104:107], v[204:207], v[168:171]
v_mfma_f32_16x16x32_bf16 v[172:175], v[108:111], v[204:207], v[172:175]
v_mfma_f32_16x16x32_bf16 v[160:163], v[240:243], v[208:211], v[160:163]
v_mfma_f32_16x16x32_bf16 v[164:167], v[244:247], v[208:211], v[164:167]
v_mfma_f32_16x16x32_bf16 v[168:171], v[248:251], v[208:211], v[168:171]
v_mfma_f32_16x16x32_bf16 v[172:175], v[252:255], v[208:211], v[172:175]
s_waitcnt vmcnt(5)
v_mfma_f32_16x16x32_bf16 v[188:191], v[32:35], v[212:215], 0
v_mfma_f32_16x16x32_bf16 v[192:195], v[36:39], v[212:215], 0
v_mfma_f32_16x16x32_bf16 v[196:199], v[56:59], v[212:215], 0
s_nop 0
ds_write_b128 v5, v[160:163]
v_mfma_f32_16x16x32_bf16 v[200:203], v[60:63], v[212:215], 0
ds_write_b128 v5, v[164:167] offset:64
s_waitcnt vmcnt(4)
v_mfma_f32_16x16x32_bf16 v[188:191], v[72:75], v[216:219], v[188:191]
ds_write_b128 v5, v[168:171] offset:128
v_mfma_f32_16x16x32_bf16 v[192:195], v[76:79], v[216:219], v[192:195]
ds_write_b128 v5, v[172:175] offset:192
v_mfma_f32_16x16x32_bf16 v[196:199], v[80:83], v[216:219], v[196:199]
v_mfma_f32_16x16x32_bf16 v[200:203], v[84:87], v[216:219], v[200:203]
s_waitcnt vmcnt(3)
v_mfma_f32_16x16x32_bf16 v[188:191], v[52:55], v[220:223], v[188:191]
v_mfma_f32_16x16x32_bf16 v[192:195], v[64:67], v[220:223], v[192:195]
v_mfma_f32_16x16x32_bf16 v[196:199], v[68:71], v[220:223], v[196:199]
v_mfma_f32_16x16x32_bf16 v[200:203], v[40:43], v[220:223], v[200:203]
s_waitcnt vmcnt(2)
v_mfma_f32_16x16x32_bf16 v[188:191], v[88:91], v[224:227], v[188:191]
v_mfma_f32_16x16x32_bf16 v[192:195], v[92:95], v[224:227], v[192:195]
v_mfma_f32_16x16x32_bf16 v[196:199], v[44:47], v[224:227], v[196:199]
v_mfma_f32_16x16x32_bf16 v[200:203], v[48:51], v[224:227], v[200:203]
s_waitcnt vmcnt(1)
v_mfma_f32_16x16x32_bf16 v[188:191], v[112:115], v[228:231], v[188:191]
v_mfma_f32_16x16x32_bf16 v[192:195], v[116:119], v[228:231], v[192:195]
v_mfma_f32_16x16x32_bf16 v[196:199], v[120:123], v[228:231], v[196:199]
v_mfma_f32_16x16x32_bf16 v[200:203], v[124:127], v[228:231], v[200:203]
s_waitcnt vmcnt(0)
v_mfma_f32_16x16x32_bf16 v[188:191], v[96:99], v[232:235], v[188:191]
v_mfma_f32_16x16x32_bf16 v[192:195], v[100:103], v[232:235], v[192:195]
v_mfma_f32_16x16x32_bf16 v[196:199], v[104:107], v[232:235], v[196:199]
v_mfma_f32_16x16x32_bf16 v[200:203], v[108:111], v[232:235], v[200:203]
v_mfma_f32_16x16x32_bf16 v[188:191], v[240:243], v[236:239], v[188:191]
v_mfma_f32_16x16x32_bf16 v[192:195], v[244:247], v[236:239], v[192:195]
v_mfma_f32_16x16x32_bf16 v[196:199], v[248:251], v[236:239], v[196:199]
v_mfma_f32_16x16x32_bf16 v[200:203], v[252:255], v[236:239], v[200:203]
v_lshlrev_b32 v96, 16, v24
v_and_b32 v97, s80, v24
v_lshlrev_b32 v98, 16, v25
v_and_b32 v99, s80, v25
v_lshlrev_b32 v100, 16, v26
v_and_b32 v101, s80, v26
v_lshlrev_b32 v102, 16, v27
v_and_b32 v103, s80, v27
v_lshlrev_b32 v104, 16, v2
v_and_b32 v105, s80, v2
v_lshlrev_b32 v106, 16, v3
v_and_b32 v107, s80, v3
ds_write_b128 v5, v[188:191] offset:4352
ds_write_b128 v5, v[192:195] offset:4416
ds_write_b128 v5, v[196:199] offset:4480
ds_write_b128 v5, v[200:203] offset:4544
s_waitcnt lgkmcnt(8)
s_barrier
ds_read_b128 v[32:35], v6
ds_read_b128 v[36:39], v6 offset:8704
ds_read_b128 v[40:43], v6 offset:17408
ds_read_b128 v[44:47], v6 offset:26112
ds_read_b128 v[48:51], v6 offset:34816
ds_read_b128 v[52:55], v6 offset:43520
ds_read_b128 v[56:59], v6 offset:52224
ds_read_b128 v[60:63], v6 offset:60928
ds_read_b128 v[64:67], v6 offset:4352
ds_read_b128 v[68:71], v6 offset:13056
ds_read_b128 v[72:75], v6 offset:21760
ds_read_b128 v[76:79], v6 offset:30464
ds_read_b128 v[80:83], v6 offset:39168
ds_read_b128 v[84:87], v6 offset:47872
ds_read_b128 v[88:91], v6 offset:56576
ds_read_b128 v[92:95], v6 offset:65280
s_add_u32 s78, s34, 0x0
s_waitcnt lgkmcnt(14)
v_pk_add_f32 v[32:33], v[32:33], v[36:37]
v_pk_add_f32 v[34:35], v[34:35], v[38:39]
s_waitcnt lgkmcnt(12)
v_pk_add_f32 v[40:41], v[40:41], v[44:45]
v_pk_add_f32 v[42:43], v[42:43], v[46:47]
s_waitcnt lgkmcnt(10)
v_pk_add_f32 v[48:49], v[48:49], v[52:53]
v_pk_add_f32 v[50:51], v[50:51], v[54:55]
s_waitcnt lgkmcnt(8)
v_pk_add_f32 v[56:57], v[56:57], v[60:61]
v_pk_add_f32 v[58:59], v[58:59], v[62:63]
v_pk_add_f32 v[32:33], v[32:33], v[40:41]
v_pk_add_f32 v[34:35], v[34:35], v[42:43]
v_pk_add_f32 v[48:49], v[48:49], v[56:57]
v_pk_add_f32 v[50:51], v[50:51], v[58:59]
v_pk_add_f32 v[32:33], v[32:33], v[48:49]
v_pk_add_f32 v[34:35], v[34:35], v[50:51]
v_pk_add_f32 v[32:33], v[32:33], v[96:97]
v_pk_add_f32 v[34:35], v[34:35], v[98:99]
v_cvt_pk_bf16_f32 v32, v32, v33
v_cvt_pk_bf16_f32 v33, v34, v35
buffer_store_dwordx2 v[32:33], v7, s[48:51], s78 offen nt
s_add_u32 s78, s34, 0x4000
s_waitcnt lgkmcnt(6)
v_pk_add_f32 v[64:65], v[64:65], v[68:69]
v_pk_add_f32 v[66:67], v[66:67], v[70:71]
s_waitcnt lgkmcnt(4)
v_pk_add_f32 v[72:73], v[72:73], v[76:77]
v_pk_add_f32 v[74:75], v[74:75], v[78:79]
s_waitcnt lgkmcnt(2)
v_pk_add_f32 v[80:81], v[80:81], v[84:85]
v_pk_add_f32 v[82:83], v[82:83], v[86:87]
s_waitcnt lgkmcnt(0)
v_pk_add_f32 v[88:89], v[88:89], v[92:93]
v_pk_add_f32 v[90:91], v[90:91], v[94:95]
v_pk_add_f32 v[64:65], v[64:65], v[72:73]
v_pk_add_f32 v[66:67], v[66:67], v[74:75]
v_pk_add_f32 v[80:81], v[80:81], v[88:89]
v_pk_add_f32 v[82:83], v[82:83], v[90:91]
v_pk_add_f32 v[64:65], v[64:65], v[80:81]
v_pk_add_f32 v[66:67], v[66:67], v[82:83]
v_pk_add_f32 v[64:65], v[64:65], v[100:101]
v_pk_add_f32 v[66:67], v[66:67], v[102:103]
v_cvt_pk_bf16_f32 v64, v64, v65
v_cvt_pk_bf16_f32 v65, v66, v67
buffer_store_dwordx2 v[64:65], v7, s[48:51], s78 offen nt
v_add_u32 v6, 69632, v6
s_barrier
ds_read_b128 v[128:131], v6
ds_read_b128 v[132:135], v6 offset:8704
ds_read_b128 v[136:139], v6 offset:17408
ds_read_b128 v[140:143], v6 offset:26112
ds_read_b128 v[144:147], v6 offset:34816
ds_read_b128 v[148:151], v6 offset:43520
ds_read_b128 v[152:155], v6 offset:52224
ds_read_b128 v[156:159], v6 offset:60928
s_add_u32 s78, s34, 0x8000
s_waitcnt lgkmcnt(6)
v_pk_add_f32 v[128:129], v[128:129], v[132:133]
v_pk_add_f32 v[130:131], v[130:131], v[134:135]
s_waitcnt lgkmcnt(4)
v_pk_add_f32 v[136:137], v[136:137], v[140:141]
v_pk_add_f32 v[138:139], v[138:139], v[142:143]
s_waitcnt lgkmcnt(2)
v_pk_add_f32 v[144:145], v[144:145], v[148:149]
v_pk_add_f32 v[146:147], v[146:147], v[150:151]
s_waitcnt lgkmcnt(0)
v_pk_add_f32 v[152:153], v[152:153], v[156:157]
v_pk_add_f32 v[154:155], v[154:155], v[158:159]
v_pk_add_f32 v[128:129], v[128:129], v[136:137]
v_pk_add_f32 v[130:131], v[130:131], v[138:139]
v_pk_add_f32 v[144:145], v[144:145], v[152:153]
v_pk_add_f32 v[146:147], v[146:147], v[154:155]
v_pk_add_f32 v[128:129], v[128:129], v[144:145]
v_pk_add_f32 v[130:131], v[130:131], v[146:147]
v_pk_add_f32 v[128:129], v[128:129], v[104:105]
v_pk_add_f32 v[130:131], v[130:131], v[106:107]
v_cvt_pk_bf16_f32 v128, v128, v129
v_cvt_pk_bf16_f32 v129, v130, v131
buffer_store_dwordx2 v[128:129], v7, s[48:51], s78 offen nt
s_branch .Lfk_end142
.Lfk_else141:
s_setprio 0
buffer_load_dwordx4 v[128:131], v1, s[36:39], s70 offen sc1
buffer_load_dwordx4 v[132:135], v1, s[36:39], s70 offen offset:1024 sc1
buffer_load_dwordx4 v[136:139], v1, s[36:39], s70 offen offset:2048 sc1
buffer_load_dwordx4 v[140:143], v1, s[36:39], s70 offen offset:3072 sc1
s_add_u32 s78, s70, 0x1000
buffer_load_dwordx4 v[144:147], v1, s[36:39], s78 offen sc1
buffer_load_dwordx4 v[148:151], v1, s[36:39], s78 offen offset:1024 sc1
buffer_load_dwordx4 v[152:155], v1, s[36:39], s78 offen offset:2048 sc1
s_add_u32 s78, s71, 0x1000
buffer_load_dwordx4 v[180:183], v1, s[36:39], s78 offen offset:2048 sc1
buffer_load_dwordx4 v[156:159], v1, s[36:39], s71 offen sc1
buffer_load_dwordx4 v[160:163], v1, s[36:39], s71 offen offset:1024 sc1
s_waitcnt vmcnt(9)
v_mfma_f32_16x16x32_bf16 v[8:11], v[32:35], v[128:131], 0
buffer_load_dwordx4 v[164:167], v1, s[36:39], s71 offen offset:2048 sc1
v_mfma_f32_16x16x32_bf16 v[16:19], v[36:39], v[128:131], 0
v_mfma_f32_16x16x32_bf16 v[20:23], v[56:59], v[128:131], 0
v_mfma_f32_16x16x32_bf16 v[28:31], v[60:63], v[128:131], 0
buffer_load_dwordx4 v[168:171], v1, s[36:39], s71 offen offset:3072 sc1
s_waitcnt vmcnt(10)
v_mfma_f32_16x16x32_bf16 v[8:11], v[72:75], v[132:135], v[8:11]
v_mfma_f32_16x16x32_bf16 v[16:19], v[76:79], v[132:135], v[16:19]
v_mfma_f32_16x16x32_bf16 v[20:23], v[80:83], v[132:135], v[20:23]
s_add_u32 s78, s71, 0x1000
buffer_load_dwordx4 v[172:175], v1, s[36:39], s78 offen sc1
v_mfma_f32_16x16x32_bf16 v[28:31], v[84:87], v[132:135], v[28:31]
s_waitcnt vmcnt(10)
v_mfma_f32_16x16x32_bf16 v[8:11], v[52:55], v[136:139], v[8:11]
v_mfma_f32_16x16x32_bf16 v[16:19], v[64:67], v[136:139], v[16:19]
buffer_load_dwordx4 v[176:179], v1, s[36:39], s78 offen offset:1024 sc1
v_mfma_f32_16x16x32_bf16 v[20:23], v[68:71], v[136:139], v[20:23]
v_mfma_f32_16x16x32_bf16 v[28:31], v[40:43], v[136:139], v[28:31]
s_waitcnt vmcnt(10)
v_mfma_f32_16x16x32_bf16 v[8:11], v[88:91], v[140:143], v[8:11]
buffer_load_dwordx4 v[184:187], v1, s[36:39], s72 offen sc1
v_mfma_f32_16x16x32_bf16 v[16:19], v[92:95], v[140:143], v[16:19]
v_mfma_f32_16x16x32_bf16 v[20:23], v[44:47], v[140:143], v[20:23]
v_mfma_f32_16x16x32_bf16 v[28:31], v[48:51], v[140:143], v[28:31]
buffer_load_dwordx4 v[188:191], v1, s[36:39], s72 offen offset:1024 sc1
s_waitcnt vmcnt(11)
v_mfma_f32_16x16x32_bf16 v[8:11], v[112:115], v[144:147], v[8:11]
v_mfma_f32_16x16x32_bf16 v[16:19], v[116:119], v[144:147], v[16:19]
v_mfma_f32_16x16x32_bf16 v[20:23], v[120:123], v[144:147], v[20:23]
buffer_load_dwordx4 v[192:195], v1, s[36:39], s72 offen offset:2048 sc1
v_mfma_f32_16x16x32_bf16 v[28:31], v[124:127], v[144:147], v[28:31]
s_waitcnt vmcnt(11)
v_mfma_f32_16x16x32_bf16 v[8:11], v[96:99], v[148:151], v[8:11]
v_mfma_f32_16x16x32_bf16 v[16:19], v[100:103], v[148:151], v[16:19]
buffer_load_dwordx4 v[196:199], v1, s[36:39], s72 offen offset:3072 sc1
s_waitcnt vmcnt(10)
v_swap_b32 v154, v180
v_swap_b32 v155, v181
v_mfma_f32_16x16x32_bf16 v[20:23], v[104:107], v[148:151], v[20:23]
v_mfma_f32_16x16x32_bf16 v[28:31], v[108:111], v[148:151], v[28:31]
v_mfma_f32_16x16x32_bf16 v[8:11], v[240:243], v[152:155], v[8:11]
s_add_u32 s78, s72, 0x1000
buffer_load_dwordx4 v[200:203], v1, s[36:39], s78 offen sc1
v_mfma_f32_16x16x32_bf16 v[16:19], v[244:247], v[152:155], v[16:19]
v_mfma_f32_16x16x32_bf16 v[20:23], v[248:251], v[152:155], v[20:23]
v_mfma_f32_16x16x32_bf16 v[28:31], v[252:255], v[152:155], v[28:31]
buffer_load_dwordx4 v[204:207], v1, s[36:39], s78 offen offset:1024 sc1
s_waitcnt vmcnt(11)
v_mfma_f32_16x16x32_bf16 v[132:135], v[32:35], v[156:159], 0
v_mfma_f32_16x16x32_bf16 v[136:139], v[36:39], v[156:159], 0
v_mfma_f32_16x16x32_bf16 v[140:143], v[56:59], v[156:159], 0
ds_write_b128 v5, v[8:11]
buffer_load_dwordx4 v[208:211], v1, s[36:39], s78 offen offset:2048 sc1
v_mfma_f32_16x16x32_bf16 v[144:147], v[60:63], v[156:159], 0
ds_write_b128 v5, v[16:19] offset:64
s_waitcnt vmcnt(11)
v_mfma_f32_16x16x32_bf16 v[132:135], v[72:75], v[160:163], v[132:135]
ds_write_b128 v5, v[20:23] offset:128
v_mfma_f32_16x16x32_bf16 v[136:139], v[76:79], v[160:163], v[136:139]
ds_write_b128 v5, v[28:31] offset:192
s_add_u32 s78, s73, 0x1000
buffer_load_dwordx4 v[236:239], v1, s[36:39], s78 offen offset:2048 sc1
v_mfma_f32_16x16x32_bf16 v[140:143], v[80:83], v[160:163], v[140:143]
v_mfma_f32_16x16x32_bf16 v[144:147], v[84:87], v[160:163], v[144:147]
s_waitcnt vmcnt(11)
v_mfma_f32_16x16x32_bf16 v[132:135], v[52:55], v[164:167], v[132:135]
buffer_load_dwordx4 v[212:215], v1, s[36:39], s73 offen sc1
v_mfma_f32_16x16x32_bf16 v[136:139], v[64:67], v[164:167], v[136:139]
v_mfma_f32_16x16x32_bf16 v[140:143], v[68:71], v[164:167], v[140:143]
v_mfma_f32_16x16x32_bf16 v[144:147], v[40:43], v[164:167], v[144:147]
buffer_load_dwordx4 v[216:219], v1, s[36:39], s73 offen offset:1024 sc1
s_waitcnt vmcnt(12)
v_mfma_f32_16x16x32_bf16 v[132:135], v[88:91], v[168:171], v[132:135]
v_mfma_f32_16x16x32_bf16 v[136:139], v[92:95], v[168:171], v[136:139]
v_mfma_f32_16x16x32_bf16 v[140:143], v[44:47], v[168:171], v[140:143]
buffer_load_dwordx4 v[220:223], v1, s[36:39], s73 offen offset:2048 sc1
v_mfma_f32_16x16x32_bf16 v[144:147], v[48:51], v[168:171], v[144:147]
s_waitcnt vmcnt(12)
v_mfma_f32_16x16x32_bf16 v[132:135], v[112:115], v[172:175], v[132:135]
v_mfma_f32_16x16x32_bf16 v[136:139], v[116:119], v[172:175], v[136:139]
buffer_load_dwordx4 v[224:227], v1, s[36:39], s73 offen offset:3072 sc1
v_mfma_f32_16x16x32_bf16 v[140:143], v[120:123], v[172:175], v[140:143]
v_mfma_f32_16x16x32_bf16 v[144:147], v[124:127], v[172:175], v[144:147]
s_waitcnt vmcnt(12)
v_mfma_f32_16x16x32_bf16 v[132:135], v[96:99], v[176:179], v[132:135]
s_add_u32 s78, s73, 0x1000
buffer_load_dwordx4 v[228:231], v1, s[36:39], s78 offen sc1
v_mfma_f32_16x16x32_bf16 v[136:139], v[100:103], v[176:179], v[136:139]
v_mfma_f32_16x16x32_bf16 v[140:143], v[104:107], v[176:179], v[140:143]
v_mfma_f32_16x16x32_bf16 v[144:147], v[108:111], v[176:179], v[144:147]
buffer_load_dwordx4 v[232:235], v1, s[36:39], s78 offen offset:1024 sc1
v_mfma_f32_16x16x32_bf16 v[132:135], v[240:243], v[180:183], v[132:135]
v_mfma_f32_16x16x32_bf16 v[136:139], v[244:247], v[180:183], v[136:139]
v_mfma_f32_16x16x32_bf16 v[140:143], v[248:251], v[180:183], v[140:143]
v_mfma_f32_16x16x32_bf16 v[144:147], v[252:255], v[180:183], v[144:147]
s_waitcnt vmcnt(13)
v_mfma_f32_16x16x32_bf16 v[160:163], v[32:35], v[184:187], 0
v_mfma_f32_16x16x32_bf16 v[164:167], v[36:39], v[184:187], 0
v_mfma_f32_16x16x32_bf16 v[168:171], v[56:59], v[184:187], 0
s_nop 0
ds_write_b128 v5, v[132:135] offset:4352
v_mfma_f32_16x16x32_bf16 v[172:175], v[60:63], v[184:187], 0
ds_write_b128 v5, v[136:139] offset:4416
s_waitcnt vmcnt(12)
v_mfma_f32_16x16x32_bf16 v[160:163], v[72:75], v[188:191], v[160:163]
ds_write_b128 v5, v[140:143] offset:4480
v_mfma_f32_16x16x32_bf16 v[164:167], v[76:79], v[188:191], v[164:167]
ds_write_b128 v5, v[144:147] offset:4544
v_mfma_f32_16x16x32_bf16 v[168:171], v[80:83], v[188:191], v[168:171]
v_add_u32 v5, 69632, v5
s_waitcnt lgkmcnt(0)
s_barrier
v_mfma_f32_16x16x32_bf16 v[172:175], v[84:87], v[188:191], v[172:175]
s_waitcnt vmcnt(11)
v_mfma_f32_16x16x32_bf16 v[160:163], v[52:55], v[192:195], v[160:163]
v_mfma_f32_16x16x32_bf16 v[164:167], v[64:67], v[192:195], v[164:167]
v_mfma_f32_16x16x32_bf16 v[168:171], v[68:71], v[192:195], v[168:171]
v_mfma_f32_16x16x32_bf16 v[172:175], v[40:43], v[192:195], v[172:175]
s_waitcnt vmcnt(10)
v_mfma_f32_16x16x32_bf16 v[160:163], v[88:91], v[196:199], v[160:163]
v_mfma_f32_16x16x32_bf16 v[164:167], v[92:95], v[196:199], v[164:167]
v_mfma_f32_16x16x32_bf16 v[168:171], v[44:47], v[196:199], v[168:171]
v_mfma_f32_16x16x32_bf16 v[172:175], v[48:51], v[196:199], v[172:175]
s_waitcnt vmcnt(9)
v_mfma_f32_16x16x32_bf16 v[160:163], v[112:115], v[200:203], v[160:163]
v_mfma_f32_16x16x32_bf16 v[164:167], v[116:119], v[200:203], v[164:167]
v_mfma_f32_16x16x32_bf16 v[168:171], v[120:123], v[200:203], v[168:171]
v_mfma_f32_16x16x32_bf16 v[172:175], v[124:127], v[200:203], v[172:175]
s_waitcnt vmcnt(8)
v_mfma_f32_16x16x32_bf16 v[160:163], v[96:99], v[204:207], v[160:163]
v_mfma_f32_16x16x32_bf16 v[164:167], v[100:103], v[204:207], v[164:167]
s_waitcnt vmcnt(6)
v_swap_b32 v210, v236
v_swap_b32 v211, v237
v_mfma_f32_16x16x32_bf16 v[168:171], v[104:107], v[204:207], v[168:171]
v_mfma_f32_16x16x32_bf16 v[172:175], v[108:111], v[204:207], v[172:175]
v_mfma_f32_16x16x32_bf16 v[160:163], v[240:243], v[208:211], v[160:163]
v_mfma_f32_16x16x32_bf16 v[164:167], v[244:247], v[208:211], v[164:167]
v_mfma_f32_16x16x32_bf16 v[168:171], v[248:251], v[208:211], v[168:171]
v_mfma_f32_16x16x32_bf16 v[172:175], v[252:255], v[208:211], v[172:175]
s_waitcnt vmcnt(5)
v_mfma_f32_16x16x32_bf16 v[188:191], v[32:35], v[212:215], 0
v_mfma_f32_16x16x32_bf16 v[192:195], v[36:39], v[212:215], 0
v_mfma_f32_16x16x32_bf16 v[196:199], v[56:59], v[212:215], 0
s_nop 0
ds_write_b128 v5, v[160:163]
v_mfma_f32_16x16x32_bf16 v[200:203], v[60:63], v[212:215], 0
ds_write_b128 v5, v[164:167] offset:64
s_waitcnt vmcnt(4)
v_mfma_f32_16x16x32_bf16 v[188:191], v[72:75], v[216:219], v[188:191]
ds_write_b128 v5, v[168:171] offset:128
v_mfma_f32_16x16x32_bf16 v[192:195], v[76:79], v[216:219], v[192:195]
ds_write_b128 v5, v[172:175] offset:192
v_mfma_f32_16x16x32_bf16 v[196:199], v[80:83], v[216:219], v[196:199]
v_mfma_f32_16x16x32_bf16 v[200:203], v[84:87], v[216:219], v[200:203]
s_waitcnt vmcnt(3)
v_mfma_f32_16x16x32_bf16 v[188:191], v[52:55], v[220:223], v[188:191]
v_mfma_f32_16x16x32_bf16 v[192:195], v[64:67], v[220:223], v[192:195]
v_mfma_f32_16x16x32_bf16 v[196:199], v[68:71], v[220:223], v[196:199]
v_mfma_f32_16x16x32_bf16 v[200:203], v[40:43], v[220:223], v[200:203]
s_waitcnt vmcnt(2)
v_mfma_f32_16x16x32_bf16 v[188:191], v[88:91], v[224:227], v[188:191]
v_mfma_f32_16x16x32_bf16 v[192:195], v[92:95], v[224:227], v[192:195]
v_mfma_f32_16x16x32_bf16 v[196:199], v[44:47], v[224:227], v[196:199]
v_mfma_f32_16x16x32_bf16 v[200:203], v[48:51], v[224:227], v[200:203]
s_waitcnt vmcnt(1)
v_mfma_f32_16x16x32_bf16 v[188:191], v[112:115], v[228:231], v[188:191]
v_mfma_f32_16x16x32_bf16 v[192:195], v[116:119], v[228:231], v[192:195]
v_mfma_f32_16x16x32_bf16 v[196:199], v[120:123], v[228:231], v[196:199]
v_mfma_f32_16x16x32_bf16 v[200:203], v[124:127], v[228:231], v[200:203]
s_waitcnt vmcnt(0)
v_mfma_f32_16x16x32_bf16 v[188:191], v[96:99], v[232:235], v[188:191]
v_mfma_f32_16x16x32_bf16 v[192:195], v[100:103], v[232:235], v[192:195]
v_mfma_f32_16x16x32_bf16 v[196:199], v[104:107], v[232:235], v[196:199]
v_mfma_f32_16x16x32_bf16 v[200:203], v[108:111], v[232:235], v[200:203]
v_mfma_f32_16x16x32_bf16 v[188:191], v[240:243], v[236:239], v[188:191]
v_mfma_f32_16x16x32_bf16 v[192:195], v[244:247], v[236:239], v[192:195]
v_mfma_f32_16x16x32_bf16 v[196:199], v[248:251], v[236:239], v[196:199]
v_mfma_f32_16x16x32_bf16 v[200:203], v[252:255], v[236:239], v[200:203]
v_lshlrev_b32 v108, 16, v24
v_and_b32 v109, s80, v24
v_lshlrev_b32 v110, 16, v25
v_and_b32 v111, s80, v25
s_nop 0
ds_write_b128 v5, v[188:191] offset:4352
ds_write_b128 v5, v[192:195] offset:4416
ds_write_b128 v5, v[196:199] offset:4480
ds_write_b128 v5, v[200:203] offset:4544
s_waitcnt lgkmcnt(0)
s_barrier
ds_read_b128 v[128:131], v6
ds_read_b128 v[132:135], v6 offset:8704
ds_read_b128 v[136:139], v6 offset:17408
ds_read_b128 v[140:143], v6 offset:26112
ds_read_b128 v[144:147], v6 offset:34816
ds_read_b128 v[148:151], v6 offset:43520
ds_read_b128 v[152:155], v6 offset:52224
ds_read_b128 v[156:159], v6 offset:60928
s_add_u32 s78, s34, 0xc000
s_waitcnt lgkmcnt(6)
v_pk_add_f32 v[128:129], v[128:129], v[132:133]
v_pk_add_f32 v[130:131], v[130:131], v[134:135]
s_waitcnt lgkmcnt(4)
v_pk_add_f32 v[136:137], v[136:137], v[140:141]
v_pk_add_f32 v[138:139], v[138:139], v[142:143]
s_waitcnt lgkmcnt(2)
v_pk_add_f32 v[144:145], v[144:145], v[148:149]
v_pk_add_f32 v[146:147], v[146:147], v[150:151]
s_waitcnt lgkmcnt(0)
v_pk_add_f32 v[152:153], v[152:153], v[156:157]
v_pk_add_f32 v[154:155], v[154:155], v[158:159]
v_pk_add_f32 v[128:129], v[128:129], v[136:137]
v_pk_add_f32 v[130:131], v[130:131], v[138:139]
v_pk_add_f32 v[144:145], v[144:145], v[152:153]
v_pk_add_f32 v[146:147], v[146:147], v[154:155]
v_pk_add_f32 v[128:129], v[128:129], v[144:145]
v_pk_add_f32 v[130:131], v[130:131], v[146:147]
v_pk_add_f32 v[128:129], v[128:129], v[108:109]
v_pk_add_f32 v[130:131], v[130:131], v[110:111]
v_cvt_pk_bf16_f32 v128, v128, v129
v_cvt_pk_bf16_f32 v129, v130, v131
buffer_store_dwordx2 v[128:129], v7, s[48:51], s78 offen nt
.Lfk_end142:
s_branch .Lfk_end
.Lfk_rare6:
s_lshl_b32 s96, s69, 4
s_lshl_b32 s97, s67, 2
s_add_u32 s96, s96, s97
s_add_u32 s96, s96, s68
s_lshl_b32 s96, s96, 7
s_add_u32 s78, 0xd0000, s96
s_mov_b64 exec, 1
buffer_store_dwordx2 v[10:11], off, s[36:39], s78 sc1
s_mov_b64 exec, -1
s_branch .Lfk_join5
.Lfk_rare8:
s_lshl_b32 s96, s82, 12
s_add_u32 s96, 0x10800, s96
s_lshl_b32 s97, s83, 7
s_add_u32 s78, s96, s97
s_mov_b64 exec, 1
buffer_store_dwordx2 v[10:11], off, s[36:39], s78 sc1
s_mov_b64 exec, -1
s_branch .Lfk_join7
.Lfk_rare11:
v_and_b32 v12, 3, v0
v_lshlrev_b32 v12, 7, v12
s_lshl_b32 s96, s69, 4
s_lshl_b32 s97, s67, 2
s_add_u32 s96, s96, s97
s_lshl_b32 s96, s96, 7
s_add_u32 s78, 0xd0000, s96
buffer_load_dwordx2 v[52:53], v12, s[36:39], s78 offen sc1
s_branch .Lfk_join10
.Lfk_rare16:
s_mov_b32 s84, 0x10000
.Lfk_poll17:
v_and_b32 v12, 3, v0
v_lshlrev_b32 v12, 7, v12
s_lshl_b32 s96, s69, 4
s_lshl_b32 s97, s67, 2
s_add_u32 s96, s96, s97
s_lshl_b32 s96, s96, 7
s_add_u32 s78, 0xd0000, s96
buffer_load_dwordx2 v[52:53], v12, s[36:39], s78 offen sc1
s_waitcnt vmcnt(0)
v_cmp_ne_u64 vcc, s[60:61], v[52:53]
s_cmp_lg_u64 vcc, 0
s_cbranch_scc0 .Lfk_done18
s_sub_u32 s84, s84, 1
s_cbranch_scc1 .Lfk_done18
s_sleep 4
s_branch .Lfk_poll17
.Lfk_done18:
s_branch .Lfk_join15
.Lfk_rare14:
s_waitcnt vmcnt(3)
v_cmp_ne_u64 vcc, s[60:61], v[52:53]
s_cmp_lg_u64 vcc, 0
s_cbranch_scc1 .Lfk_rare16
.Lfk_join15:
s_branch .Lfk_join13
.Lfk_rare22:
s_lshl_b32 s96, s69, 12
s_add_u32 s96, 0x10000, s96
s_lshl_b32 s97, s67, 2
s_add_u32 s97, s97, s68
s_lshl_b32 s97, s97, 7
s_add_u32 s78, s96, s97
s_mov_b64 exec, 1
buffer_store_dwordx2 v[10:11], off, s[36:39], s78 sc1
s_mov_b64 exec, -1
s_branch .Lfk_join21
.Lfk_rare24:
v_and_b32 v12, 15, v0
v_lshlrev_b32 v12, 7, v12
s_lshl_b32 s96, s82, 12
s_add_u32 s78, 0x10800, s96
buffer_load_dwordx2 v[52:53], v12, s[36:39], s78 offen sc1
s_branch .Lfk_join23
.Lfk_rare27:
s_mov_b32 s84, 0x10000
.Lfk_poll28:
s_add_u32 s78, s76, 0x0
s_mov_b64 exec, s[86:87]
buffer_load_dwordx4 v[40:43], v27, s[36:39], s78 offen sc1 nt
s_mov_b64 exec, -1
s_waitcnt vmcnt(0)
v_max3_u32 v104, v40, v41, v42
v_max_u32 v104, v104, v43
v_cmp_eq_u32 vcc, -1, v104
s_cmp_lg_u64 vcc, 0
s_cbranch_scc0 .Lfk_done29
s_sub_u32 s84, s84, 1
s_cbranch_scc1 .Lfk_done29
s_sleep 1
s_branch .Lfk_poll28
.Lfk_done29:
s_branch .Lfk_join26
.Lfk_rare33:
s_mov_b32 s84, 0x10000
.Lfk_poll34:
v_and_b32 v12, 15, v0
v_lshlrev_b32 v12, 7, v12
s_lshl_b32 s96, s82, 12
s_add_u32 s78, 0x10800, s96
buffer_load_dwordx2 v[52:53], v12, s[36:39], s78 offen sc1
s_waitcnt vmcnt(0)
v_cmp_ne_u64 vcc, s[60:61], v[52:53]
s_cmp_lg_u64 vcc, 0
s_cbranch_scc0 .Lfk_done35
s_sub_u32 s84, s84, 1
s_cbranch_scc1 .Lfk_done35
s_sleep 4
s_branch .Lfk_poll34
.Lfk_done35:
s_branch .Lfk_join32
.Lfk_rare31:
s_waitcnt vmcnt(5)
v_cmp_ne_u64 vcc, s[60:61], v[52:53]
s_cmp_lg_u64 vcc, 0
s_cbranch_scc1 .Lfk_rare33
.Lfk_join32:
s_branch .Lfk_join30
.Lfk_rare37:
s_and_b32 s96, s2, 3
s_lshl_b32 s96, s96, 6
s_lshr_b32 s97, s2, 2
s_and_b32 s97, s97, 7
s_lshl_b32 s97, s97, 3
s_add_u32 s96, s96, s97
s_lshr_b32 s97, s2, 5
s_add_u32 s96, s96, s97
s_lshl_b32 s96, s96, 7
s_add_u32 s78, 0xc0000, s96
s_mov_b64 exec, 1
buffer_store_dwordx2 v[10:11], off, s[36:39], s78 sc1
s_mov_b64 exec, -1
s_branch .Lfk_join36
.Lfk_rare39:
v_and_b32 v12, 15, v0
v_lshlrev_b32 v12, 7, v12
s_lshl_b32 s96, s83, 12
v_add_u32 v12, s96, v12
v_and_b32 v12, 3, v0
v_lshlrev_b32 v12, 7, v12
s_lshl_b32 s96, s69, 12
s_lshl_b32 s97, s67, 9
s_add_u32 s96, s96, s97
v_add_u32 v13, s96, v12
s_mov_b64 vcc, 0xffff
v_cndmask_b32 v12, v13, v12, vcc
s_mov_b32 s78, 0x10000
s_waitcnt vmcnt(8)
buffer_load_dwordx2 v[52:53], v12, s[36:39], s78 offen sc1
s_branch .Lfk_join38
.Lfk_rare44:
s_mov_b32 s84, 0x10000
.Lfk_poll45:
v_and_b32 v12, 15, v0
v_lshlrev_b32 v12, 7, v12
s_lshl_b32 s96, s83, 12
v_add_u32 v12, s96, v12
v_and_b32 v12, 3, v0
v_lshlrev_b32 v12, 7, v12
s_lshl_b32 s96, s69, 12
s_lshl_b32 s97, s67, 9
s_add_u32 s96, s96, s97
v_add_u32 v13, s96, v12
s_mov_b64 vcc, 0xffff
v_cndmask_b32 v12, v13, v12, vcc
s_mov_b32 s78, 0x10000
buffer_load_dwordx2 v[52:53], v12, s[36:39], s78 offen sc1
s_waitcnt vmcnt(0)
v_cmp_ne_u64 vcc, s[60:61], v[52:53]
s_cmp_lg_u64 vcc, 0
s_cbranch_scc0 .Lfk_done46
s_sub_u32 s84, s84, 1
s_cbranch_scc1 .Lfk_done46
s_sleep 4
s_branch .Lfk_poll45
.Lfk_done46:
s_branch .Lfk_join43
.Lfk_rare42:
s_waitcnt vmcnt(4)
v_cmp_ne_u64 vcc, s[60:61], v[52:53]
s_cmp_lg_u64 vcc, 0
s_cbranch_scc1 .Lfk_rare44
.Lfk_join43:
s_branch .Lfk_join41
.Lfk_rare49:
s_mov_b32 s84, 0x10000
.Lfk_poll50:
s_add_u32 s78, s76, 0x8000
s_mov_b64 exec, s[86:87]
buffer_load_dwordx4 v[44:47], v27, s[36:39], s78 offen sc1 nt
s_mov_b64 exec, -1
s_waitcnt vmcnt(0)
v_max3_u32 v104, v44, v45, v46
v_max_u32 v104, v104, v47
v_cmp_eq_u32 vcc, -1, v104
s_cmp_lg_u64 vcc, 0
s_cbranch_scc0 .Lfk_done51
s_sub_u32 s84, s84, 1
s_cbranch_scc1 .Lfk_done51
s_sleep 1
s_branch .Lfk_poll50
.Lfk_done51:
s_branch .Lfk_join48
.Lfk_rare54:
v_lshlrev_b32 v12, 7, v0
s_and_b32 s96, s2, 2
s_lshl_b32 s96, s96, 1
s_lshr_b32 s97, s64, 3
s_add_u32 s96, s96, s97
s_lshr_b32 s96, s96, 1
s_lshl_b32 s96, s96, 13
s_add_u32 s78, 0xc0000, s96
buffer_load_dwordx2 v[52:53], v12, s[36:39], s78 offen sc1
s_branch .Lfk_join53
.Lfk_rare56:
s_mov_b32 s84, 0x10000
.Lfk_poll57:
s_add_u32 m0, s75, 0x8000
s_add_u32 s78, s29, 0x0
buffer_load_dwordx4 v1, s[36:39], s78 offen sc1 lds
buffer_load_dwordx4 v1, s[36:39], s78 offen offset:1024 sc1 lds
s_add_u32 m0, s75, 0x8800
s_add_u32 s78, s29, 0x1000
buffer_load_dwordx4 v1, s[36:39], s78 offen sc1 lds
buffer_load_dwordx4 v1, s[36:39], s78 offen offset:1024 sc1 lds
s_waitcnt vmcnt(0)
ds_read_b128 v[160:163], v5 offset:32768
ds_read_b128 v[164:167], v5 offset:33792
ds_read_b128 v[168:171], v5 offset:34816
ds_read_b128 v[172:175], v5 offset:35840
s_waitcnt lgkmcnt(3)
v_max3_u32 v8, v160, v161, v162
s_waitcnt lgkmcnt(2)
v_max3_u32 v8, v8, v163, v164
v_max3_u32 v8, v8, v165, v166
s_waitcnt lgkmcnt(1)
v_max3_u32 v8, v8, v167, v168
v_max3_u32 v8, v8, v169, v170
s_waitcnt lgkmcnt(0)
v_max3_u32 v8, v8, v171, v172
v_max3_u32 v8, v8, v173, v174
v_max_u32 v8, v8, v175
v_cmp_eq_u32 vcc, -1, v8
s_cmp_lg_u64 vcc, 0
s_cbranch_scc0 .Lfk_done58
s_sub_u32 s84, s84, 1
s_cbranch_scc1 .Lfk_done58
s_sleep 1
s_branch .Lfk_poll57
.Lfk_done58:
s_branch .Lfk_join55
.Lfk_rare62:
s_mov_b32 s84, 0x10000
.Lfk_poll63:
v_lshlrev_b32 v12, 7, v0
s_and_b32 s96, s2, 2
s_lshl_b32 s96, s96, 1
s_lshr_b32 s97, s64, 3
s_add_u32 s96, s96, s97
s_lshr_b32 s96, s96, 1
s_lshl_b32 s96, s96, 13
s_add_u32 s78, 0xc0000, s96
buffer_load_dwordx2 v[52:53], v12, s[36:39], s78 offen sc1
s_waitcnt vmcnt(0)
v_cmp_ne_u64 vcc, s[60:61], v[52:53]
s_cmp_lg_u64 vcc, 0
s_cbranch_scc0 .Lfk_done64
s_sub_u32 s84, s84, 1
s_cbranch_scc1 .Lfk_done64
s_sleep 4
s_branch .Lfk_poll63
.Lfk_done64:
s_branch .Lfk_join61
.Lfk_rare60:
s_waitcnt vmcnt(5)
v_cmp_ne_u64 vcc, s[60:61], v[52:53]
s_cmp_lg_u64 vcc, 0
s_cbranch_scc1 .Lfk_rare62
.Lfk_join61:
s_branch .Lfk_join59
.Lfk_rare67:
s_mov_b32 s84, 0x10000
.Lfk_poll68:
s_add_u32 s78, s76, 0x0
s_mov_b64 exec, s[86:87]
buffer_load_dwordx4 v[32:35], v27, s[36:39], s78 offen sc1
s_mov_b64 exec, -1
s_waitcnt vmcnt(0)
v_cmp_eq_u32 vcc, v32, v40
v_cmp_eq_u32 s[100:101], v33, v41
s_or_b64 vcc, vcc, s[100:101]
v_cmp_eq_u32 s[100:101], v34, v42
s_or_b64 vcc, vcc, s[100:101]
v_cmp_eq_u32 s[100:101], v35, v43
s_or_b64 vcc, vcc, s[100:101]
s_cbranch_scc0 .Lfk_done69
s_sub_u32 s84, s84, 1
s_cbranch_scc1 .Lfk_done69
s_sleep 1
s_branch .Lfk_poll68
.Lfk_done69:
s_branch .Lfk_join66
.Lfk_rare71:
s_mov_b32 s84, 0x10000
.Lfk_poll72:
s_add_u32 m0, s75, 0xc000
s_add_u32 s78, s29, 0x800
buffer_load_dwordx4 v1, s[36:39], s78 offen sc1 lds
buffer_load_dwordx4 v1, s[36:39], s78 offen offset:1024 sc1 lds
s_add_u32 m0, s75, 0xc800
s_add_u32 s78, s29, 0x1800
buffer_load_dwordx4 v1, s[36:39], s78 offen sc1 lds
buffer_load_dwordx4 v1, s[36:39], s78 offen offset:1024 sc1 lds
s_waitcnt vmcnt(0)
ds_read_b128 v[128:131], v5 offset:49152
ds_read_b128 v[132:135], v5 offset:50176
ds_read_b128 v[136:139], v5 offset:51200
ds_read_b128 v[140:143], v5 offset:52224
s_waitcnt lgkmcnt(3)
v_max3_u32 v8, v128, v129, v130
s_waitcnt lgkmcnt(2)
v_max3_u32 v8, v8, v131, v132
v_max3_u32 v8, v8, v133, v134
s_waitcnt lgkmcnt(1)
v_max3_u32 v8, v8, v135, v136
v_max3_u32 v8, v8, v137, v138
s_waitcnt lgkmcnt(0)
v_max3_u32 v8, v8, v139, v140
v_max3_u32 v8, v8, v141, v142
v_max_u32 v8, v8, v143
v_cmp_eq_u32 vcc, -1, v8
s_cmp_lg_u64 vcc, 0
s_cbranch_scc0 .Lfk_done73
s_sub_u32 s84, s84, 1
s_cbranch_scc1 .Lfk_done73
s_sleep 1
s_branch .Lfk_poll72
.Lfk_done73:
s_branch .Lfk_join70
.Lfk_rare75:
s_mov_b64 exec, 1
buffer_store_dwordx2 v[10:11], off, s[36:39], 0 sc1
s_mov_b64 exec, -1
s_branch .Lfk_join74
.Lfk_rare77:
s_mov_b32 s84, 0x10000
.Lfk_poll78:
s_add_u32 m0, s75, 0x20000
s_add_u32 s78, s29, 0x8000
buffer_load_dwordx4 v1, s[36:39], s78 offen sc1 lds
buffer_load_dwordx4 v1, s[36:39], s78 offen offset:1024 sc1 lds
s_add_u32 m0, s75, 0x20800
s_add_u32 s78, s29, 0x9000
buffer_load_dwordx4 v1, s[36:39], s78 offen sc1 lds
buffer_load_dwordx4 v1, s[36:39], s78 offen offset:1024 sc1 lds
s_waitcnt vmcnt(0)
ds_read_b128 v[128:131], v6
ds_read_b128 v[132:135], v6 offset:1024
ds_read_b128 v[136:139], v6 offset:2048
ds_read_b128 v[140:143], v6 offset:3072
s_waitcnt lgkmcnt(3)
v_max3_u32 v8, v128, v129, v130
s_waitcnt lgkmcnt(2)
v_max3_u32 v8, v8, v131, v132
v_max3_u32 v8, v8, v133, v134
s_waitcnt lgkmcnt(1)
v_max3_u32 v8, v8, v135, v136
v_max3_u32 v8, v8, v137, v138
s_waitcnt lgkmcnt(0)
v_max3_u32 v8, v8, v139, v140
v_max3_u32 v8, v8, v141, v142
v_max_u32 v8, v8, v143
v_cmp_eq_u32 vcc, -1, v8
s_cmp_lg_u64 vcc, 0
s_cbranch_scc0 .Lfk_done79
s_sub_u32 s84, s84, 1
s_cbranch_scc1 .Lfk_done79
s_sleep 1
s_branch .Lfk_poll78
.Lfk_done79:
s_branch .Lfk_join76
.Lfk_rare81:
s_mov_b32 s84, 0x10000
.Lfk_poll82:
s_add_u32 s78, s76, 0x8000
s_mov_b64 exec, s[86:87]
buffer_load_dwordx4 v[36:39], v27, s[36:39], s78 offen sc1
s_mov_b64 exec, -1
s_waitcnt vmcnt(0)
v_cmp_eq_u32 vcc, v36, v44
v_cmp_eq_u32 s[100:101], v37, v45
s_or_b64 vcc, vcc, s[100:101]
v_cmp_eq_u32 s[100:101], v38, v46
s_or_b64 vcc, vcc, s[100:101]
v_cmp_eq_u32 s[100:101], v39, v47
s_or_b64 vcc, vcc, s[100:101]
s_cbranch_scc0 .Lfk_done83
s_sub_u32 s84, s84, 1
s_cbranch_scc1 .Lfk_done83
s_sleep 1
s_branch .Lfk_poll82
.Lfk_done83:
s_branch .Lfk_join80
.Lfk_rare85:
s_mov_b32 s84, 0x10000
.Lfk_poll86:
s_add_u32 m0, s75, 0x8000
s_add_u32 s78, s29, 0x8800
buffer_load_dwordx4 v1, s[36:39], s78 offen sc1 lds
buffer_load_dwordx4 v1, s[36:39], s78 offen offset:1024 sc1 lds
s_add_u32 m0, s75, 0x8800
s_add_u32 s78, s29, 0x9800
buffer_load_dwordx4 v1, s[36:39], s78 offen sc1 lds
buffer_load_dwordx4 v1, s[36:39], s78 offen offset:1024 sc1 lds
s_waitcnt vmcnt(0)
ds_read_b128 v[128:131], v5 offset:32768
ds_read_b128 v[132:135], v5 offset:33792
ds_read_b128 v[136:139], v5 offset:34816
ds_read_b128 v[140:143], v5 offset:35840
s_waitcnt lgkmcnt(3)
v_max3_u32 v8, v128, v129, v130
s_waitcnt lgkmcnt(2)
v_max3_u32 v8, v8, v131, v132
v_max3_u32 v8, v8, v133, v134
s_waitcnt lgkmcnt(1)
v_max3_u32 v8, v8, v135, v136
v_max3_u32 v8, v8, v137, v138
s_waitcnt lgkmcnt(0)
v_max3_u32 v8, v8, v139, v140
v_max3_u32 v8, v8, v141, v142
v_max_u32 v8, v8, v143
v_cmp_eq_u32 vcc, -1, v8
s_cmp_lg_u64 vcc, 0
s_cbranch_scc0 .Lfk_done87
s_sub_u32 s84, s84, 1
s_cbranch_scc1 .Lfk_done87
s_sleep 1
s_branch .Lfk_poll86
.Lfk_done87:
s_branch .Lfk_join84
.Lfk_rare89:
s_mov_b32 s84, 0x10000
.Lfk_poll90:
s_add_u32 m0, s75, 0xc000
s_add_u32 s78, s29, 0x10000
buffer_load_dwordx4 v1, s[36:39], s78 offen sc1 lds
buffer_load_dwordx4 v1, s[36:39], s78 offen offset:1024 sc1 lds
s_add_u32 m0, s75, 0xc800
s_add_u32 s78, s29, 0x11000
buffer_load_dwordx4 v1, s[36:39], s78 offen sc1 lds
buffer_load_dwordx4 v1, s[36:39], s78 offen offset:1024 sc1 lds
s_waitcnt vmcnt(0)
ds_read_b128 v[128:131], v5 offset:49152
ds_read_b128 v[132:135], v5 offset:50176
ds_read_b128 v[136:139], v5 offset:51200
ds_read_b128 v[140:143], v5 offset:52224
s_waitcnt lgkmcnt(3)
v_max3_u32 v8, v128, v129, v130
s_waitcnt lgkmcnt(2)
v_max3_u32 v8, v8, v131, v132
v_max3_u32 v8, v8, v133, v134
s_waitcnt lgkmcnt(1)
v_max3_u32 v8, v8, v135, v136
v_max3_u32 v8, v8, v137, v138
s_waitcnt lgkmcnt(0)
v_max3_u32 v8, v8, v139, v140
v_max3_u32 v8, v8, v141, v142
v_max_u32 v8, v8, v143
v_cmp_eq_u32 vcc, -1, v8
s_cmp_lg_u64 vcc, 0
s_cbranch_scc0 .Lfk_done91
s_sub_u32 s84, s84, 1
s_cbranch_scc1 .Lfk_done91
s_sleep 1
s_branch .Lfk_poll90
.Lfk_done91:
s_branch .Lfk_join88
.Lfk_rare93:
s_mov_b32 s84, 0x10000
.Lfk_poll94:
s_add_u32 m0, s75, 0x20000
s_add_u32 s78, s29, 0x10800
buffer_load_dwordx4 v1, s[36:39], s78 offen sc1 lds
buffer_load_dwordx4 v1, s[36:39], s78 offen offset:1024 sc1 lds
s_add_u32 m0, s75, 0x20800
s_add_u32 s78, s29, 0x11800
buffer_load_dwordx4 v1, s[36:39], s78 offen sc1 lds
buffer_load_dwordx4 v1, s[36:39], s78 offen offset:1024 sc1 lds
s_waitcnt vmcnt(0)
ds_read_b128 v[128:131], v6
ds_read_b128 v[132:135], v6 offset:1024
ds_read_b128 v[136:139], v6 offset:2048
ds_read_b128 v[140:143], v6 offset:3072
s_waitcnt lgkmcnt(3)
v_max3_u32 v8, v128, v129, v130
s_waitcnt lgkmcnt(2)
v_max3_u32 v8, v8, v131, v132
v_max3_u32 v8, v8, v133, v134
s_waitcnt lgkmcnt(1)
v_max3_u32 v8, v8, v135, v136
v_max3_u32 v8, v8, v137, v138
s_waitcnt lgkmcnt(0)
v_max3_u32 v8, v8, v139, v140
v_max3_u32 v8, v8, v141, v142
v_max_u32 v8, v8, v143
v_cmp_eq_u32 vcc, -1, v8
s_cmp_lg_u64 vcc, 0
s_cbranch_scc0 .Lfk_done95
s_sub_u32 s84, s84, 1
s_cbranch_scc1 .Lfk_done95
s_sleep 1
s_branch .Lfk_poll94
.Lfk_done95:
s_branch .Lfk_join92
.Lfk_rare97:
s_mov_b32 s84, 0x10000
.Lfk_poll98:
s_add_u32 m0, s75, 0x8000
s_add_u32 s78, s29, 0x18000
buffer_load_dwordx4 v1, s[36:39], s78 offen sc1 lds
buffer_load_dwordx4 v1, s[36:39], s78 offen offset:1024 sc1 lds
s_add_u32 m0, s75, 0x8800
s_add_u32 s78, s29, 0x19000
buffer_load_dwordx4 v1, s[36:39], s78 offen sc1 lds
buffer_load_dwordx4 v1, s[36:39], s78 offen offset:1024 sc1 lds
s_waitcnt vmcnt(0)
ds_read_b128 v[128:131], v5 offset:32768
ds_read_b128 v[132:135], v5 offset:33792
ds_read_b128 v[136:139], v5 offset:34816
ds_read_b128 v[140:143], v5 offset:35840
s_waitcnt lgkmcnt(3)
v_max3_u32 v8, v128, v129, v130
s_waitcnt lgkmcnt(2)
v_max3_u32 v8, v8, v131, v132
v_max3_u32 v8, v8, v133, v134
s_waitcnt lgkmcnt(1)
v_max3_u32 v8, v8, v135, v136
v_max3_u32 v8, v8, v137, v138
s_waitcnt lgkmcnt(0)
v_max3_u32 v8, v8, v139, v140
v_max3_u32 v8, v8, v141, v142
v_max_u32 v8, v8, v143
v_cmp_eq_u32 vcc, -1, v8
s_cmp_lg_u64 vcc, 0
s_cbranch_scc0 .Lfk_done99
s_sub_u32 s84, s84, 1
s_cbranch_scc1 .Lfk_done99
s_sleep 1
s_branch .Lfk_poll98
.Lfk_done99:
s_branch .Lfk_join96
.Lfk_rare101:
s_mov_b32 s84, 0x10000
.Lfk_poll102:
s_lshl_b32 s96, s83, 3
s_add_u32 s96, s96, s62
s_lshl_b32 s96, s96, 12
s_add_u32 s78, 0xe00000, s96
buffer_load_dwordx4 v[96:99], v1, s[36:39], s78 offen sc1
buffer_load_dwordx4 v[100:103], v1, s[36:39], s78 offen offset:1024 sc1
buffer_load_dwordx4 v[104:107], v1, s[36:39], s78 offen offset:2048 sc1
buffer_load_dwordx4 v[108:111], v1, s[36:39], s78 offen offset:3072 sc1
s_waitcnt vmcnt(0)
v_cmp_ne_u32 vcc, s60, v98
v_cmp_ne_u32 s[100:101], s60, v102
s_or_b64 vcc, vcc, s[100:101]
v_cmp_ne_u32 s[100:101], s60, v106
s_or_b64 vcc, vcc, s[100:101]
v_cmp_ne_u32 s[100:101], s60, v110
s_or_b64 vcc, vcc, s[100:101]
s_cbranch_scc0 .Lfk_done103
s_sub_u32 s84, s84, 1
s_cbranch_scc1 .Lfk_done103
s_sleep 1
s_branch .Lfk_poll102
.Lfk_done103:
s_branch .Lfk_join100
.Lfk_rare105:
s_mov_b32 s84, 0x10000
.Lfk_poll106:
s_add_u32 m0, s75, 0xc000
s_add_u32 s78, s29, 0x18800
buffer_load_dwordx4 v1, s[36:39], s78 offen sc1 lds
buffer_load_dwordx4 v1, s[36:39], s78 offen offset:1024 sc1 lds
s_add_u32 m0, s75, 0xc800
s_add_u32 s78, s29, 0x19800
buffer_load_dwordx4 v1, s[36:39], s78 offen sc1 lds
buffer_load_dwordx4 v1, s[36:39], s78 offen offset:1024 sc1 lds
s_waitcnt vmcnt(0)
ds_read_b128 v[128:131], v5 offset:49152
ds_read_b128 v[132:135], v5 offset:50176
ds_read_b128 v[136:139], v5 offset:51200
ds_read_b128 v[140:143], v5 offset:52224
s_waitcnt lgkmcnt(3)
v_max3_u32 v8, v128, v129, v130
s_waitcnt lgkmcnt(2)
v_max3_u32 v8, v8, v131, v132
v_max3_u32 v8, v8, v133, v134
s_waitcnt lgkmcnt(1)
v_max3_u32 v8, v8, v135, v136
v_max3_u32 v8, v8, v137, v138
s_waitcnt lgkmcnt(0)
v_max3_u32 v8, v8, v139, v140
v_max3_u32 v8, v8, v141, v142
v_max_u32 v8, v8, v143
v_cmp_eq_u32 vcc, -1, v8
s_cmp_lg_u64 vcc, 0
s_cbranch_scc0 .Lfk_done107
s_sub_u32 s84, s84, 1
s_cbranch_scc1 .Lfk_done107
s_sleep 1
s_branch .Lfk_poll106
.Lfk_done107:
s_branch .Lfk_join104
.Lfk_rare116:
s_mov_b32 s84, 0x10000
.Lfk_poll117:
s_add_u32 s78, s76, 0x0
s_mov_b64 exec, s[86:87]
buffer_load_dwordx4 v[40:43], v27, s[36:39], s78 offen sc1 nt
s_mov_b64 exec, -1
s_waitcnt vmcnt(0)
v_max3_u32 v104, v40, v41, v42
v_max_u32 v104, v104, v43
v_cmp_eq_u32 vcc, -1, v104
s_cmp_lg_u64 vcc, 0
s_cbranch_scc0 .Lfk_done118
s_sub_u32 s84, s84, 1
s_cbranch_scc1 .Lfk_done118
s_sleep 1
s_branch .Lfk_poll117
.Lfk_done118:
s_branch .Lfk_join115
.Lfk_rare122:
s_mov_b32 s84, 0x10000
.Lfk_poll123:
s_add_u32 s78, s76, 0x8000
s_mov_b64 exec, s[86:87]
buffer_load_dwordx4 v[44:47], v27, s[36:39], s78 offen sc1 nt
s_mov_b64 exec, -1
s_waitcnt vmcnt(0)
v_max3_u32 v104, v44, v45, v46
v_max_u32 v104, v104, v47
v_cmp_eq_u32 vcc, -1, v104
s_cmp_lg_u64 vcc, 0
s_cbranch_scc0 .Lfk_done124
s_sub_u32 s84, s84, 1
s_cbranch_scc1 .Lfk_done124
s_sleep 1
s_branch .Lfk_poll123
.Lfk_done124:
s_branch .Lfk_join121
.Lfk_rare128:
s_mov_b32 s84, 0x10000
.Lfk_poll129:
s_add_u32 s78, s76, 0x0
s_mov_b64 exec, s[86:87]
buffer_load_dwordx4 v[32:35], v27, s[36:39], s78 offen sc1
s_mov_b64 exec, -1
s_waitcnt vmcnt(0)
v_cmp_eq_u32 vcc, v32, v40
v_cmp_eq_u32 s[100:101], v33, v41
s_or_b64 vcc, vcc, s[100:101]
v_cmp_eq_u32 s[100:101], v34, v42
s_or_b64 vcc, vcc, s[100:101]
v_cmp_eq_u32 s[100:101], v35, v43
s_or_b64 vcc, vcc, s[100:101]
s_cbranch_scc0 .Lfk_done130
s_sub_u32 s84, s84, 1
s_cbranch_scc1 .Lfk_done130
s_sleep 1
s_branch .Lfk_poll129
.Lfk_done130:
s_branch .Lfk_join127
.Lfk_rare132:
s_mov_b32 s84, 0x10000
.Lfk_poll133:
s_add_u32 s78, s76, 0x8000
s_mov_b64 exec, s[86:87]
buffer_load_dwordx4 v[36:39], v27, s[36:39], s78 offen sc1
s_mov_b64 exec, -1
s_waitcnt vmcnt(0)
v_cmp_eq_u32 vcc, v36, v44
v_cmp_eq_u32 s[100:101], v37, v45
s_or_b64 vcc, vcc, s[100:101]
v_cmp_eq_u32 s[100:101], v38, v46
s_or_b64 vcc, vcc, s[100:101]
v_cmp_eq_u32 s[100:101], v39, v47
s_or_b64 vcc, vcc, s[100:101]
s_cbranch_scc0 .Lfk_done134
s_sub_u32 s84, s84, 1
s_cbranch_scc1 .Lfk_done134
s_sleep 1
s_branch .Lfk_poll133
.Lfk_done134:
s_branch .Lfk_join131
.Lfk_rare136:
s_mov_b32 s84, 0x10000
.Lfk_poll137:
s_lshl_b32 s96, s83, 3
s_add_u32 s96, s96, s62
s_lshl_b32 s96, s96, 12
s_add_u32 s78, 0xe00000, s96
buffer_load_dwordx4 v[96:99], v1, s[36:39], s78 offen sc1
buffer_load_dwordx4 v[100:103], v1, s[36:39], s78 offen offset:1024 sc1
buffer_load_dwordx4 v[104:107], v1, s[36:39], s78 offen offset:2048 sc1
buffer_load_dwordx4 v[108:111], v1, s[36:39], s78 offen offset:3072 sc1
s_waitcnt vmcnt(0)
v_cmp_ne_u32 vcc, s60, v98
v_cmp_ne_u32 s[100:101], s60, v102
s_or_b64 vcc, vcc, s[100:101]
v_cmp_ne_u32 s[100:101], s60, v106
s_or_b64 vcc, vcc, s[100:101]
v_cmp_ne_u32 s[100:101], s60, v110
s_or_b64 vcc, vcc, s[100:101]
s_cbranch_scc0 .Lfk_done138
s_sub_u32 s84, s84, 1
s_cbranch_scc1 .Lfk_done138
s_sleep 1
s_branch .Lfk_poll137
.Lfk_done138:
s_branch .Lfk_join135
.Lfk_end:
)ASM"
               : "+{v0}"(tid)
               : "{s[0:1]}"(__builtin_amdgcn_kernarg_segment_ptr()), "{s2}"(blockIdx.x)
               : "v1", "v2", "v3", "v4", "v5", "v6", "v7", "v8", "v9", "v10", "v11", "v12", "v13", "v14", "v15", "v16", "v17", "v18", "v19", "v20", "v21", "v22", "v23", "v24", "v25", "v26", "v27", "v28", "v29", "v30", "v31", "v32", "v33", "v34", "v35", "v36", "v37", "v38", "v39", "v40", "v41", "v42", "v43", "v44", "v45", "v46", "v47", "v48", "v49", "v50", "v51", "v52", "v53", "v54", "v55", "v56", "v57", "v58", "v59", "v60", "v61", "v62", "v63", "v64", "v65", "v66", "v67", "v68", "v69", "v70", "v71", "v72", "v73", "v74", "v75", "v76", "v77", "v78", "v79", "v80", "v81", "v82", "v83", "v84", "v85", "v86", "v87", "v88", "v89", "v90", "v91", "v92", "v93", "v94", "v95", "v96", "v97", "v98", "v99", "v100", "v101", "v102", "v103", "v104", "v105", "v106", "v107", "v108", "v109", "v110", "v111", "v112", "v113", "v114", "v115", "v116", "v117", "v118", "v119", "v120", "v121", "v122", "v123", "v124", "v125", "v126", "v127", "v128", "v129", "v130", "v131", "v132", "v133", "v134", "v135", "v136", "v137", "v138", "v139", "v140", "v141", "v142", "v143", "v144", "v145", "v146", "v147", "v148", "v149", "v150", "v151", "v152", "v153", "v154", "v155", "v156", "v157", "v158", "v159", "v160", "v161", "v162", "v163", "v164", "v165", "v166", "v167", "v168", "v169", "v170", "v171", "v172", "v173", "v174", "v175", "v176", "v177", "v178", "v179", "v180", "v181", "v182", "v183", "v184", "v185", "v186", "v187", "v188", "v189", "v190", "v191", "v192", "v193", "v194", "v195", "v196", "v197", "v198", "v199", "v200", "v201", "v202", "v203", "v204", "v205", "v206", "v207", "v208", "v209", "v210", "v211", "v212", "v213", "v214", "v215", "v216", "v217", "v218", "v219", "v220", "v221", "v222", "v223", "v224", "v225", "v226", "v227", "v228", "v229", "v230", "v231", "v232", "v233", "v234", "v235", "v236", "v237", "v238", "v239", "v240", "v241", "v242", "v243", "v244", "v245", "v246", "v247", "v248", "v249", "v250", "v251", "v252", "v253", "v254", "v255", "s3", "s4", "s5", "s6", "s7", "s8", "s9", "s10", "s11", "s12", "s13", "s14", "s15", "s16", "s17", "s18", "s19", "s20", "s21", "s22", "s23", "s24", "s25", "s26", "s27", "s28", "s29", "s30", "s31", "s32", "s33", "s34", "s35", "s36", "s37", "s38", "s39", "s40", "s41", "s42", "s43", "s44", "s45", "s46", "s47", "s48", "s49", "s50", "s51", "s52", "s53", "s54", "s55", "s56", "s57", "s58", "s59", "s60", "s61", "s62", "s63", "s64", "s65", "s66", "s67", "s68", "s69", "s70", "s71", "s72", "s73", "s74", "s75", "s76", "s77", "s78", "s79", "s80", "s81", "s82", "s83", "s84", "s85", "s86", "s87", "s88", "s89", "s90", "s91", "s92", "s93", "s94", "s95", "s96", "s97", "s98", "s99", "s100", "s101", "vcc", "scc", "memory");
}


extern "C" void kernel_launch(void* const* d_in, const int* in_sizes, int n_in, void* d_out, int out_size,
                              void* d_ws, size_t ws_size, hipStream_t stream) {
  (void)in_sizes; (void)n_in; (void)out_size; (void)ws_size;
  (void)hipFuncSetAttribute((const void*)fused_kernel, hipFuncAttributeMaxDynamicSharedMemorySize, LDS_BYTES);
  fused_kernel<<<256, 512, LDS_BYTES, stream>>>(d_in[0], d_in[1], d_in[2], d_in[3], d_in[4], d_in[6], d_in[7], d_in[8], d_out, d_ws);
}
```
